```python
import torch

def _softmax(x, axis=-1):
    return torch.softmax(x, dim=axis)
def _gelu(x):
    return torch.nn.functional.gelu(x, approximate="tanh")
def _silu(x):
    return torch.nn.functional.silu(x)
def _sigmoid(x):
    return torch.sigmoid(x)
def _layernorm(x, axis=-1, eps=1e-5):
    m = x.mean(dim=axis, keepdim=True)
    v = x.var(dim=axis, keepdim=True, unbiased=False)
    return (x - m) / torch.sqrt(v + eps)
def _scatter_add(tgt, idx, src, axis=0):
    return tgt.clone().index_add_(axis, idx, src)
def _segment_sum(src, seg, n):
    out = torch.zeros((n,) + src.shape[1:], dtype=src.dtype)
    return out.index_add_(0, seg, src)
def _pad(x, pw):
    flat = tuple(v for pair in reversed(pw) for v in pair)
    return torch.nn.functional.pad(x, flat)

kernel_name = 'concat_heads__gqa_block_cached__compressed_kv_cla__decode'

def setup_inputs():

    _w = (lambda a: (lambda t: t.to(torch.bfloat16) if t.is_floating_point() else t)(torch.as_tensor(a)))
    def _A_setup():
        B, Sq, D = 2, 1, 512
        Hq, Hkv, Sk, Dh = 32, 8, 8192, 128
        assert Hq % Hkv == 0, (Hq, Hkv)
        return {
            "x": _w((lambda *s: torch.randn(*s, dtype=torch.bfloat16) * 1.6)(B, Sq, D)),
            "k_cache": _w((lambda *s: torch.randn(*s, dtype=torch.bfloat16) * 1.6)(B, Hkv, Sk, Dh)),
            "v_cache": _w((lambda *s: torch.randn(*s, dtype=torch.bfloat16) * 0.05)(B, Hkv, Sk, Dh)),
            "wq": _w((lambda *s: torch.randn(*s, dtype=torch.bfloat16) * 1.6)(D, Hq * Dh) * (D ** -0.5)),
            "wo": _w((lambda *s: torch.randn(*s, dtype=torch.bfloat16) * 0.05)(Hq * Dh, D) * ((Hq * Dh) ** -0.5)),
        }
    _A_in = _A_setup()

    def _B_setup():
        B, H, Sq, Sk, D, M = 2, 32, 1, 8192, 128, 512
        Dc = 4 * D
        return {
            "x": _w((lambda *s: torch.randn(*s, dtype=torch.bfloat16) * 3.2)(B, Sq, M)),
            "c_kv": _w((lambda *s: torch.randn(*s, dtype=torch.bfloat16) * 0.1)(B, Sk, Dc)),
            "w_q": _w((lambda *s: torch.randn(*s, dtype=torch.bfloat16) * 6.4)(M, H, D) * (M ** -0.5)),
            "w_uk": _w((lambda *s: torch.randn(*s, dtype=torch.bfloat16) * 6.4)(Dc, H, D) * (Dc ** -0.5)),
            "w_uv": _w((lambda *s: torch.randn(*s, dtype=torch.bfloat16) * 0.1)(Dc, H, D) * (Dc ** -0.5)),
            "w_o": _w((lambda *s: torch.randn(*s, dtype=torch.bfloat16) * 0.1)(H, D, M) * ((H * D) ** -0.5)),
        }
    _B_in = _B_setup()

    return {
        "A_x": _A_in["x"],
        "A_k_cache": _A_in["k_cache"],
        "A_v_cache": _A_in["v_cache"],
        "A_wq": _A_in["wq"],
        "A_wo": _A_in["wo"],
        "B_x": _B_in["x"],
        "B_c_kv": _B_in["c_kv"],
        "B_w_q": _B_in["w_q"],
        "B_w_uk": _B_in["w_uk"],
        "B_w_uv": _B_in["w_uv"],
        "B_w_o": _B_in["w_o"],
    }

def reference(A_x, A_k_cache, A_v_cache, A_wq, A_wo, B_x, B_c_kv, B_w_q, B_w_uk, B_w_uv, B_w_o):
    def _A_ref(x, k_cache, v_cache, wq, wo):
        B, Sq, D = x.shape
        Hq, Dh = 32, 128
        Hkv = k_cache.shape[1]
        rep = Hq // Hkv
        q = (lambda x, *d: x.permute(*d))((x @ wq).reshape(B, Sq, Hq, Dh), 0, 2, 1, 3)
        k = (lambda x, n, axis: torch.repeat_interleave(x, n, dim=axis))(k_cache, rep, axis=1)
        v = (lambda x, n, axis: torch.repeat_interleave(x, n, dim=axis))(v_cache, rep, axis=1)
        scores = torch.einsum("bhqd,bhkd->bhqk", q, k) * (Dh ** -0.5)
        p = _softmax(scores, axis=-1)
        o = torch.einsum("bhqk,bhkd->bhqd", p, v)
        return (lambda x, *d: x.permute(*d))(o, 0, 2, 1, 3).reshape(B, Sq, Hq * Dh) @ wo

    def _B_ref(x, c_kv, w_q, w_uk, w_uv, w_o):
        B, Sq, M = x.shape[0], x.shape[1], x.shape[2]
        Sk = c_kv.shape[1]
        H, D = w_q.shape[1], w_q.shape[2]
        q = torch.einsum("bqm,mhd->bhqd", x, w_q)
        k = torch.einsum("bkc,chd->bhkd", c_kv, w_uk)
        v = torch.einsum("bkc,chd->bhkd", c_kv, w_uv)
        scores = torch.einsum("bhqd,bhkd->bhqk", q, k) * (D ** -0.5)
        off = Sk - Sq
        qi = torch.arange(Sq)[:, None]
        kj = torch.arange(Sk)[None, :]
        cmask = kj <= (qi + off)
        scores = torch.where(cmask[None, None], scores, -1e9)
        p = _softmax(scores, axis=-1)
        ctx = torch.einsum("bhqk,bhkd->bhqd", p, v)
        return torch.einsum("bhqd,hdm->bqm", ctx, w_o)

    a = _A_ref(A_x, A_k_cache, A_v_cache, A_wq, A_wo)
    b = _B_ref(B_x, B_c_kv, B_w_q, B_w_uk, B_w_uv, B_w_o)
    return torch.concatenate([a, b], axis=-1)
```

```cpp
#include <hip/hip_runtime.h>
#include <hip/hip_bf16.h>
#include <stdint.h>

typedef unsigned short u16;
__device__ __forceinline__ float bf2f(u16 v) { return __uint_as_float(((unsigned)v) << 16); }
__device__ __forceinline__ u16 f2bf(float f) { unsigned u = __float_as_uint(f); u += 0x7FFFu + ((u >> 16) & 1u); return (u16)(u >> 16); }

constexpr int SK = 8192;
constexpr int NSA = 32;
constexpr int NSB = 64;
constexpr float SCALE = 0.08838834764831845f;

__global__ void k_q(const u16* xa, const u16* wqa, const u16* xb, const u16* wqb, float* qa, float* qb) {
  const u16* x = blockIdx.y ? xb : xa; const u16* w = blockIdx.y ? wqb : wqa; float* q = blockIdx.y ? qb : qa;
  int n = blockIdx.x * 64 + (threadIdx.x & 63); int part = threadIdx.x >> 6;
  __shared__ float red[4][2][64];
  float a0 = 0, a1 = 0;
  for (int m = part * 128; m < part * 128 + 128; ++m) { float wv = bf2f(w[(size_t)m * 4096 + n]); a0 += bf2f(x[m]) * wv; a1 += bf2f(x[512 + m]) * wv; }
  red[part][0][threadIdx.x & 63] = a0; red[part][1][threadIdx.x & 63] = a1;
  __syncthreads();
  if (part < 2) { float s = 0; for (int p = 0; p < 4; ++p) s += red[p][part][threadIdx.x & 63]; q[part * 4096 + n] = bf2f(f2bf(s)); }
}
__global__ void k_qabs(const float* qb, const u16* wuk, float* qabs) {
  int h = blockIdx.x, c = blockIdx.y * 64 + threadIdx.x;
  float a0 = 0, a1 = 0;
  for (int d = 0; d < 128; ++d) { float wv = bf2f(wuk[((size_t)c * 32 + h) * 128 + d]); a0 += qb[h * 128 + d] * wv; a1 += qb[4096 + h * 128 + d] * wv; }
  qabs[(0 * 32 + h) * 512 + c] = a0; qabs[(1 * 32 + h) * 512 + c] = a1;
}
__global__ void k_attn_a(const float* qa, const u16* K, const u16* V, float* po, float* pm, float* pl) {
  int s = blockIdx.x, h = blockIdx.y, b = blockIdx.z, kvh = h >> 2, t = threadIdx.x;
  __shared__ float q[128], p[256], red[256];
  if (t < 128) q[t] = qa[b * 4096 + h * 128 + t];
  __syncthreads();
  const u16* kr = K + ((size_t)(b * 8 + kvh) * SK + s * 256 + t) * 128;
  float a = 0; for (int d = 0; d < 128; ++d) a += q[d] * bf2f(kr[d]);
  a *= SCALE;
  red[t] = a; __syncthreads();
  for (int o = 128; o > 0; o >>= 1) { if (t < o) red[t] = fmaxf(red[t], red[t + o]); __syncthreads(); }
  float m = red[0]; __syncthreads();
  float e = __expf(a - m); p[t] = e; red[t] = e; __syncthreads();
  for (int o = 128; o > 0; o >>= 1) { if (t < o) red[t] += red[t + o]; __syncthreads(); }
  float l = red[0];
  if (t < 128) { const u16* vr = V + ((size_t)(b * 8 + kvh) * SK + s * 256) * 128 + t; float o = 0; for (int k = 0; k < 256; ++k) o += p[k] * bf2f(vr[(size_t)k * 128]);
    po[((size_t)(b * 32 + h) * NSA + s) * 128 + t] = o; }
  if (t == 0) { pm[(b * 32 + h) * NSA + s] = m; pl[(b * 32 + h) * NSA + s] = l; }
}
__global__ void k_attn_b(const float* qabs, const u16* C, float* po, float* pm, float* pl) {
  int s = blockIdx.x, h = blockIdx.y, b = blockIdx.z, t = threadIdx.x;
  __shared__ float q[512], p[128], red[128];
  for (int i = t; i < 512; i += 128) q[i] = qabs[(b * 32 + h) * 512 + i];
  __syncthreads();
  const u16* kr = C + ((size_t)b * SK + s * 128 + t) * 512;
  float a = 0; for (int d = 0; d < 512; ++d) a += q[d] * bf2f(kr[d]);
  a *= SCALE;
  red[t] = a; __syncthreads();
  for (int o = 64; o > 0; o >>= 1) { if (t < o) red[t] = fmaxf(red[t], red[t + o]); __syncthreads(); }
  float m = red[0]; __syncthreads();
  float e = __expf(a - m); p[t] = e; red[t] = e; __syncthreads();
  for (int o = 64; o > 0; o >>= 1) { if (t < o) red[t] += red[t + o]; __syncthreads(); }
  float l = red[0];
  for (int c = t; c < 512; c += 128) { const u16* vr = C + ((size_t)b * SK + s * 128) * 512 + c; float o = 0; for (int k = 0; k < 128; ++k) o += p[k] * bf2f(vr[(size_t)k * 512]);
    po[((size_t)(b * 32 + h) * NSB + s) * 512 + c] = o; }
  if (t == 0) { pm[(b * 32 + h) * NSB + s] = m; pl[(b * 32 + h) * NSB + s] = l; }
}
__global__ void k_comb(const float* poa, const float* pma, const float* pla, const float* pob, const float* pmb, const float* plb, const u16* wuv, float* o) {
  int h = blockIdx.x, b = blockIdx.y, t = threadIdx.x;
  __shared__ float lat[512];
  if (blockIdx.z == 0) {
    float M = -1e30f; for (int s = 0; s < NSA; ++s) M = fmaxf(M, pma[(b * 32 + h) * NSA + s]);
    float L = 0, a = 0; for (int s = 0; s < NSA; ++s) { float w = __expf(pma[(b * 32 + h) * NSA + s] - M); L += w * pla[(b * 32 + h) * NSA + s]; a += w * poa[((size_t)(b * 32 + h) * NSA + s) * 128 + t]; }
    o[(0 * 2 + b) * 4096 + h * 128 + t] = a / L;
  } else {
    float M = -1e30f; for (int s = 0; s < NSB; ++s) M = fmaxf(M, pmb[(b * 32 + h) * NSB + s]);
    for (int c = t; c < 512; c += 128) {
      float L = 0, a = 0; for (int s = 0; s < NSB; ++s) { float w = __expf(pmb[(b * 32 + h) * NSB + s] - M); L += w * plb[(b * 32 + h) * NSB + s]; a += w * pob[((size_t)(b * 32 + h) * NSB + s) * 512 + c]; }
      lat[c] = a / L; }
    __syncthreads();
    float a = 0; for (int c = 0; c < 512; ++c) a += lat[c] * bf2f(wuv[((size_t)c * 32 + h) * 128 + t]);
    o[(1 * 2 + b) * 4096 + h * 128 + t] = a;
  }
}
__global__ void k_out(const float* o, const u16* woa, const u16* wob, u16* out) {
  int which = blockIdx.y, n = blockIdx.x * 64 + (threadIdx.x & 63), part = threadIdx.x >> 6;
  const u16* w = which ? wob : woa;
  __shared__ float red[16][2][64];
  float a0 = 0, a1 = 0;
  for (int r = part * 256; r < part * 256 + 256; ++r) { float wv = bf2f(w[(size_t)r * 512 + n]); a0 += o[(which * 2 + 0) * 4096 + r] * wv; a1 += o[(which * 2 + 1) * 4096 + r] * wv; }
  red[part][0][threadIdx.x & 63] = a0; red[part][1][threadIdx.x & 63] = a1;
  __syncthreads();
  if (part < 2) { float s = 0; for (int p = 0; p < 16; ++p) s += red[p][part][threadIdx.x & 63]; out[part * 1024 + which * 512 + n] = f2bf(s); }
}

extern "C" void kernel_launch(void* const* d_in, const int* in_sizes, int n_in, void* d_out, int out_size, void* d_ws, size_t ws_size, hipStream_t stream) {
  const u16 *Ax = (const u16*)d_in[0], *Ak = (const u16*)d_in[1], *Av = (const u16*)d_in[2], *Awq = (const u16*)d_in[3], *Awo = (const u16*)d_in[4];
  const u16 *Bx = (const u16*)d_in[5], *Bc = (const u16*)d_in[6], *Bwq = (const u16*)d_in[7], *Bwuk = (const u16*)d_in[8], *Bwuv = (const u16*)d_in[9], *Bwo = (const u16*)d_in[10];
  float* ws = (float*)d_ws;
  float* qa = ws; float* qb = qa + 8192; float* qabs = qb + 8192; float* poa = qabs + 2 * 32 * 512; float* pma = poa + 64 * NSA * 128; float* pla = pma + 64 * NSA;
  float* pob = pla + 64 * NSA; float* pmb = pob + 64 * NSB * 512; float* plb = pmb + 64 * NSB; float* o = plb + 64 * NSB;
  k_q<<<dim3(64, 2), 256, 0, stream>>>(Ax, Awq, Bx, Bwq, qa, qb);
  k_qabs<<<dim3(32, 8), 64, 0, stream>>>(qb, Bwuk, qabs);
  k_attn_a<<<dim3(NSA, 32, 2), 256, 0, stream>>>(qa, Ak, Av, poa, pma, pla);
  k_attn_b<<<dim3(NSB, 32, 2), 128, 0, stream>>>(qabs, Bc, pob, pmb, plb);
  k_comb<<<dim3(32, 2, 2), 128, 0, stream>>>(poa, pma, pla, pob, pmb, plb, Bwuv, o);
  k_out<<<dim3(8, 2), 1024, 0, stream>>>(o, Awo, Bwo, (u16*)d_out);
}
```

```cpp
#include <hip/hip_runtime.h>
#include <hip/hip_bf16.h>
#include <stdint.h>

typedef unsigned short u16;
__device__ __forceinline__ float bf2f(u16 v) { return __uint_as_float(((unsigned)v) << 16); }
__device__ __forceinline__ u16 f2bf(float f) { unsigned u = __float_as_uint(f); u += 0x7FFFu + ((u >> 16) & 1u); return (u16)(u >> 16); }

constexpr int SK = 8192;
constexpr int NSA = 32;
constexpr int NSB = 64;
constexpr float SCALE = 0.08838834764831845f;

__global__ void k_q(const u16* xa, const u16* wqa, const u16* xb, const u16* wqb, float* qa, float* qb) {
  const u16* x = blockIdx.y ? xb : xa; const u16* w = blockIdx.y ? wqb : wqa; float* q = blockIdx.y ? qb : qa;
  int n = blockIdx.x * 64 + (threadIdx.x & 63); int part = threadIdx.x >> 6;
  __shared__ float red[4][2][64];
  float a0 = 0, a1 = 0;
  for (int m = part * 128; m < part * 128 + 128; ++m) { float wv = bf2f(w[(size_t)m * 4096 + n]); a0 += bf2f(x[m]) * wv; a1 += bf2f(x[512 + m]) * wv; }
  red[part][0][threadIdx.x & 63] = a0; red[part][1][threadIdx.x & 63] = a1;
  __syncthreads();
  if (part < 2) { float s = 0; for (int p = 0; p < 4; ++p) s += red[p][part][threadIdx.x & 63]; q[part * 4096 + n] = bf2f(f2bf(s)); }
}
__global__ void k_qabs(const float* qb, const u16* wuk, float* qabs) {
  int h = blockIdx.x, c = blockIdx.y * 64 + threadIdx.x;
  float a0 = 0, a1 = 0;
  for (int d = 0; d < 128; ++d) { float wv = bf2f(wuk[((size_t)c * 32 + h) * 128 + d]); a0 += qb[h * 128 + d] * wv; a1 += qb[4096 + h * 128 + d] * wv; }
  qabs[(0 * 32 + h) * 512 + c] = a0; qabs[(1 * 32 + h) * 512 + c] = a1;
}
__global__ void k_attn_a(const float* qa, const u16* K, const u16* V, float* po, float* pm, float* pl) {
  int s = blockIdx.x, h = blockIdx.y, b = blockIdx.z, kvh = h >> 2, t = threadIdx.x;
  __shared__ float q[128], p[256], red[256];
  if (t < 128) q[t] = qa[b * 4096 + h * 128 + t];
  __syncthreads();
  const u16* kr = K + ((size_t)(b * 8 + kvh) * SK + s * 256 + t) * 128;
  float a = 0; for (int d = 0; d < 128; ++d) a += q[d] * bf2f(kr[d]);
  a *= SCALE;
  red[t] = a; __syncthreads();
  for (int o = 128; o > 0; o >>= 1) { if (t < o) red[t] = fmaxf(red[t], red[t + o]); __syncthreads(); }
  float m = red[0]; __syncthreads();
  float e = __expf(a - m); p[t] = e; red[t] = e; __syncthreads();
  for (int o = 128; o > 0; o >>= 1) { if (t < o) red[t] += red[t + o]; __syncthreads(); }
  float l = red[0];
  if (t < 128) { const u16* vr = V + ((size_t)(b * 8 + kvh) * SK + s * 256) * 128 + t; float o = 0; for (int k = 0; k < 256; ++k) o += p[k] * bf2f(vr[(size_t)k * 128]);
    po[((size_t)(b * 32 + h) * NSA + s) * 128 + t] = o; }
  if (t == 0) { pm[(b * 32 + h) * NSA + s] = m; pl[(b * 32 + h) * NSA + s] = l; }
}
__global__ void k_attn_b(const float* qabs, const u16* C, float* po, float* pm, float* pl) {
  int s = blockIdx.x, h = blockIdx.y, b = blockIdx.z, t = threadIdx.x;
  __shared__ float q[512], p[128], red[128];
  for (int i = t; i < 512; i += 128) q[i] = qabs[(b * 32 + h) * 512 + i];
  __syncthreads();
  const u16* kr = C + ((size_t)b * SK + s * 128 + t) * 512;
  float a = 0; for (int d = 0; d < 512; ++d) a += q[d] * bf2f(kr[d]);
  a *= SCALE;
  red[t] = a; __syncthreads();
  for (int o = 64; o > 0; o >>= 1) { if (t < o) red[t] = fmaxf(red[t], red[t + o]); __syncthreads(); }
  float m = red[0]; __syncthreads();
  float e = __expf(a - m); p[t] = e; red[t] = e; __syncthreads();
  for (int o = 64; o > 0; o >>= 1) { if (t < o) red[t] += red[t + o]; __syncthreads(); }
  float l = red[0];
  for (int c = t; c < 512; c += 128) { const u16* vr = C + ((size_t)b * SK + s * 128) * 512 + c; float o = 0; for (int k = 0; k < 128; ++k) o += p[k] * bf2f(vr[(size_t)k * 512]);
    po[((size_t)(b * 32 + h) * NSB + s) * 512 + c] = o; }
  if (t == 0) { pm[(b * 32 + h) * NSB + s] = m; pl[(b * 32 + h) * NSB + s] = l; }
}
__global__ void k_comb(const float* poa, const float* pma, const float* pla, const float* pob, const float* pmb, const float* plb, const u16* wuv, float* o) {
  int h = blockIdx.x, b = blockIdx.y, t = threadIdx.x;
  __shared__ float lat[512];
  if (blockIdx.z == 0) {
    float M = -1e30f; for (int s = 0; s < NSA; ++s) M = fmaxf(M, pma[(b * 32 + h) * NSA + s]);
    float L = 0, a = 0; for (int s = 0; s < NSA; ++s) { float w = __expf(pma[(b * 32 + h) * NSA + s] - M); L += w * pla[(b * 32 + h) * NSA + s]; a += w * poa[((size_t)(b * 32 + h) * NSA + s) * 128 + t]; }
    o[(0 * 2 + b) * 4096 + h * 128 + t] = a / L;
  } else {
    float M = -1e30f; for (int s = 0; s < NSB; ++s) M = fmaxf(M, pmb[(b * 32 + h) * NSB + s]);
    for (int c = t; c < 512; c += 128) {
      float L = 0, a = 0; for (int s = 0; s < NSB; ++s) { float w = __expf(pmb[(b * 32 + h) * NSB + s] - M); L += w * plb[(b * 32 + h) * NSB + s]; a += w * pob[((size_t)(b * 32 + h) * NSB + s) * 512 + c]; }
      lat[c] = a / L; }
    __syncthreads();
    float a = 0; for (int c = 0; c < 512; ++c) a += lat[c] * bf2f(wuv[((size_t)c * 32 + h) * 128 + t]);
    o[(1 * 2 + b) * 4096 + h * 128 + t] = a;
  }
}
__global__ void k_out(const float* o, const u16* woa, const u16* wob, u16* out) {
  int which = blockIdx.y, n = blockIdx.x * 64 + (threadIdx.x & 63), part = threadIdx.x >> 6;
  const u16* w = which ? wob : woa;
  __shared__ float red[16][2][64];
  float a0 = 0, a1 = 0;
  for (int r = part * 256; r < part * 256 + 256; ++r) { float wv = bf2f(w[(size_t)r * 512 + n]); a0 += o[(which * 2 + 0) * 4096 + r] * wv; a1 += o[(which * 2 + 1) * 4096 + r] * wv; }
  red[part][0][threadIdx.x & 63] = a0; red[part][1][threadIdx.x & 63] = a1;
  __syncthreads();
  if (part < 2) { float s = 0; for (int p = 0; p < 16; ++p) s += red[p][part][threadIdx.x & 63]; out[part * 1024 + which * 512 + n] = f2bf(s); }
}


#define GAS __attribute__((address_space(1)))
typedef unsigned v4u __attribute__((ext_vector_type(4)));
typedef GAS const v4u gv4u;
template <bool NT> __device__ __forceinline__ v4u ld(gv4u* p) { if (NT) return __builtin_nontemporal_load(p); else return *p; }
struct PArgs { const v4u* p[9]; };
template <bool NT>
__global__ void __launch_bounds__(512) k_probe(const v4u* k, const v4u* v, const v4u* c, const v4u* w0, const v4u* w1, const v4u* w2, const v4u* w3, const v4u* w4, const v4u* w5, unsigned* sink) {
  extern __shared__ char smem[];
  const unsigned long long t0 = __builtin_amdgcn_s_memrealtime();
  const int lane = threadIdx.x & 63, wave = __builtin_amdgcn_readfirstlane(threadIdx.x >> 6);
  const int gw = blockIdx.x * 8 + wave;
  v4u acc = {0,0,0,0};
  auto run = [&](const v4u* b_, int n) {
    gv4u* p = (gv4u*)b_ + (size_t)gw * n * 64 + lane;
    for (int t = 0; t < n; t += 2) { v4u a0 = ld<NT>(p + t * 64), a1 = ld<NT>(p + t * 64 + 64); acc ^= a0; acc ^= a1; }
  };
  {
    gv4u* pk = (gv4u*)k + (size_t)gw * 16 * 64 + lane; gv4u* pv = (gv4u*)v + (size_t)gw * 16 * 64 + lane; gv4u* pc = (gv4u*)c + (size_t)gw * 8 * 64 + lane;
    v4u a[12];
    a[0] = ld<NT>((gv4u*)w0 + (size_t)gw * 128 + lane); a[1] = ld<NT>((gv4u*)w0 + (size_t)gw * 128 + 64 + lane);
    a[2] = ld<NT>((gv4u*)w1 + (size_t)gw * 128 + lane); a[3] = ld<NT>((gv4u*)w1 + (size_t)gw * 128 + 64 + lane);
    a[4] = ld<NT>((gv4u*)w2 + (size_t)gw * 128 + lane); a[5] = ld<NT>((gv4u*)w2 + (size_t)gw * 128 + 64 + lane);
    a[6] = ld<NT>((gv4u*)w3 + (size_t)gw * 128 + lane); a[7] = ld<NT>((gv4u*)w3 + (size_t)gw * 128 + 64 + lane);
    a[8] = ld<NT>((gv4u*)w4 + (size_t)gw * 128 + lane); a[9] = ld<NT>((gv4u*)w4 + (size_t)gw * 128 + 64 + lane);
    a[10] = ld<NT>((gv4u*)w5 + (size_t)gw * 128 + lane); a[11] = ld<NT>((gv4u*)w5 + (size_t)gw * 128 + 64 + lane);
    __builtin_amdgcn_sched_barrier(0);
#pragma unroll
    for (int i = 0; i < 40; ++i) { acc ^= a[i % 12]; a[i % 12] = i < 16 ? ld<NT>(pk + i * 64) : i < 32 ? ld<NT>(pv + (i - 16) * 64) : ld<NT>(pc + (i - 32) * 64); __builtin_amdgcn_sched_barrier(0); }
#pragma unroll
    for (int i = 0; i < 12; ++i) acc ^= a[i];
  }
  unsigned r = acc.x ^ acc.y ^ acc.z ^ acc.w;
  if (r == 0x12345678u) sink[threadIdx.x] = r;
  __syncthreads();
  const unsigned long long t1 = __builtin_amdgcn_s_memrealtime();
  const unsigned long long until = t0 + 21 * (t1 - t0);
  while (__builtin_amdgcn_s_memrealtime() < until) __builtin_amdgcn_s_sleep(4);
}

extern "C" void kernel_launch(void* const* d_in, const int* in_sizes, int n_in, void* d_out, int out_size, void* d_ws, size_t ws_size, hipStream_t stream) {
  const u16 *Ax = (const u16*)d_in[0], *Ak = (const u16*)d_in[1], *Av = (const u16*)d_in[2], *Awq = (const u16*)d_in[3], *Awo = (const u16*)d_in[4];
  const u16 *Bx = (const u16*)d_in[5], *Bc = (const u16*)d_in[6], *Bwq = (const u16*)d_in[7], *Bwuk = (const u16*)d_in[8], *Bwuv = (const u16*)d_in[9], *Bwo = (const u16*)d_in[10];
  float* ws = (float*)d_ws;
  float* qa = ws; float* qb = qa + 8192; float* qabs = qb + 8192; float* poa = qabs + 2 * 32 * 512; float* pma = poa + 64 * NSA * 128; float* pla = pma + 64 * NSA;
  float* pob = pla + 64 * NSA; float* pmb = pob + 64 * NSB * 512; float* plb = pmb + 64 * NSB; float* o = plb + 64 * NSB;
  hipFuncSetAttribute((const void*)k_probe<false>, hipFuncAttributeMaxDynamicSharedMemorySize, 96 * 1024);
  k_probe<false><<<256, 512, 96 * 1024, stream>>>((const v4u*)Ak, (const v4u*)Av, (const v4u*)Bc, (const v4u*)Awq, (const v4u*)Awo, (const v4u*)Bwq, (const v4u*)Bwuk, (const v4u*)Bwuv, (const v4u*)Bwo, (unsigned*)d_ws + (32 << 20));
  k_q<<<dim3(64, 2), 256, 0, stream>>>(Ax, Awq, Bx, Bwq, qa, qb);
  k_qabs<<<dim3(32, 8), 64, 0, stream>>>(qb, Bwuk, qabs);
  k_attn_a<<<dim3(NSA, 32, 2), 256, 0, stream>>>(qa, Ak, Av, poa, pma, pla);
  k_attn_b<<<dim3(NSB, 32, 2), 128, 0, stream>>>(qabs, Bc, pob, pmb, plb);
  k_comb<<<dim3(32, 2, 2), 128, 0, stream>>>(poa, pma, pla, pob, pmb, plb, Bwuv, o);
  k_out<<<dim3(8, 2), 1024, 0, stream>>>(o, Awo, Bwo, (u16*)d_out);
}
```

```cpp
#include <hip/hip_runtime.h>
#include <hip/hip_bf16.h>
#include <stdint.h>

typedef unsigned short u16;
__device__ __forceinline__ float bf2f(u16 v) { return __uint_as_float(((unsigned)v) << 16); }
__device__ __forceinline__ u16 f2bf(float f) { unsigned u = __float_as_uint(f); u += 0x7FFFu + ((u >> 16) & 1u); return (u16)(u >> 16); }

constexpr int SK = 8192;
constexpr int NSA = 32;
constexpr int NSB = 64;
constexpr float SCALE = 0.08838834764831845f;

__global__ void k_q(const u16* xa, const u16* wqa, const u16* xb, const u16* wqb, float* qa, float* qb) {
  const u16* x = blockIdx.y ? xb : xa; const u16* w = blockIdx.y ? wqb : wqa; float* q = blockIdx.y ? qb : qa;
  int n = blockIdx.x * 64 + (threadIdx.x & 63); int part = threadIdx.x >> 6;
  __shared__ float red[4][2][64];
  float a0 = 0, a1 = 0;
  for (int m = part * 128; m < part * 128 + 128; ++m) { float wv = bf2f(w[(size_t)m * 4096 + n]); a0 += bf2f(x[m]) * wv; a1 += bf2f(x[512 + m]) * wv; }
  red[part][0][threadIdx.x & 63] = a0; red[part][1][threadIdx.x & 63] = a1;
  __syncthreads();
  if (part < 2) { float s = 0; for (int p = 0; p < 4; ++p) s += red[p][part][threadIdx.x & 63]; q[part * 4096 + n] = bf2f(f2bf(s)); }
}
__global__ void k_qabs(const float* qb, const u16* wuk, float* qabs) {
  int h = blockIdx.x, c = blockIdx.y * 64 + threadIdx.x;
  float a0 = 0, a1 = 0;
  for (int d = 0; d < 128; ++d) { float wv = bf2f(wuk[((size_t)c * 32 + h) * 128 + d]); a0 += qb[h * 128 + d] * wv; a1 += qb[4096 + h * 128 + d] * wv; }
  qabs[(0 * 32 + h) * 512 + c] = a0; qabs[(1 * 32 + h) * 512 + c] = a1;
}
__global__ void k_attn_a(const float* qa, const u16* K, const u16* V, float* po, float* pm, float* pl) {
  int s = blockIdx.x, h = blockIdx.y, b = blockIdx.z, kvh = h >> 2, t = threadIdx.x;
  __shared__ float q[128], p[256], red[256];
  if (t < 128) q[t] = qa[b * 4096 + h * 128 + t];
  __syncthreads();
  const u16* kr = K + ((size_t)(b * 8 + kvh) * SK + s * 256 + t) * 128;
  float a = 0; for (int d = 0; d < 128; ++d) a += q[d] * bf2f(kr[d]);
  a *= SCALE;
  red[t] = a; __syncthreads();
  for (int o = 128; o > 0; o >>= 1) { if (t < o) red[t] = fmaxf(red[t], red[t + o]); __syncthreads(); }
  float m = red[0]; __syncthreads();
  float e = __expf(a - m); p[t] = e; red[t] = e; __syncthreads();
  for (int o = 128; o > 0; o >>= 1) { if (t < o) red[t] += red[t + o]; __syncthreads(); }
  float l = red[0];
  if (t < 128) { const u16* vr = V + ((size_t)(b * 8 + kvh) * SK + s * 256) * 128 + t; float o = 0; for (int k = 0; k < 256; ++k) o += p[k] * bf2f(vr[(size_t)k * 128]);
    po[((size_t)(b * 32 + h) * NSA + s) * 128 + t] = o; }
  if (t == 0) { pm[(b * 32 + h) * NSA + s] = m; pl[(b * 32 + h) * NSA + s] = l; }
}
__global__ void k_attn_b(const float* qabs, const u16* C, float* po, float* pm, float* pl) {
  int s = blockIdx.x, h = blockIdx.y, b = blockIdx.z, t = threadIdx.x;
  __shared__ float q[512], p[128], red[128];
  for (int i = t; i < 512; i += 128) q[i] = qabs[(b * 32 + h) * 512 + i];
  __syncthreads();
  const u16* kr = C + ((size_t)b * SK + s * 128 + t) * 512;
  float a = 0; for (int d = 0; d < 512; ++d) a += q[d] * bf2f(kr[d]);
  a *= SCALE;
  red[t] = a; __syncthreads();
  for (int o = 64; o > 0; o >>= 1) { if (t < o) red[t] = fmaxf(red[t], red[t + o]); __syncthreads(); }
  float m = red[0]; __syncthreads();
  float e = __expf(a - m); p[t] = e; red[t] = e; __syncthreads();
  for (int o = 64; o > 0; o >>= 1) { if (t < o) red[t] += red[t + o]; __syncthreads(); }
  float l = red[0];
  for (int c = t; c < 512; c += 128) { const u16* vr = C + ((size_t)b * SK + s * 128) * 512 + c; float o = 0; for (int k = 0; k < 128; ++k) o += p[k] * bf2f(vr[(size_t)k * 512]);
    po[((size_t)(b * 32 + h) * NSB + s) * 512 + c] = o; }
  if (t == 0) { pm[(b * 32 + h) * NSB + s] = m; pl[(b * 32 + h) * NSB + s] = l; }
}
__global__ void k_comb(const float* poa, const float* pma, const float* pla, const float* pob, const float* pmb, const float* plb, const u16* wuv, float* o) {
  int h = blockIdx.x, b = blockIdx.y, t = threadIdx.x;
  __shared__ float lat[512];
  if (blockIdx.z == 0) {
    float M = -1e30f; for (int s = 0; s < NSA; ++s) M = fmaxf(M, pma[(b * 32 + h) * NSA + s]);
    float L = 0, a = 0; for (int s = 0; s < NSA; ++s) { float w = __expf(pma[(b * 32 + h) * NSA + s] - M); L += w * pla[(b * 32 + h) * NSA + s]; a += w * poa[((size_t)(b * 32 + h) * NSA + s) * 128 + t]; }
    o[(0 * 2 + b) * 4096 + h * 128 + t] = a / L;
  } else {
    float M = -1e30f; for (int s = 0; s < NSB; ++s) M = fmaxf(M, pmb[(b * 32 + h) * NSB + s]);
    for (int c = t; c < 512; c += 128) {
      float L = 0, a = 0; for (int s = 0; s < NSB; ++s) { float w = __expf(pmb[(b * 32 + h) * NSB + s] - M); L += w * plb[(b * 32 + h) * NSB + s]; a += w * pob[((size_t)(b * 32 + h) * NSB + s) * 512 + c]; }
      lat[c] = a / L; }
    __syncthreads();
    float a = 0; for (int c = 0; c < 512; ++c) a += lat[c] * bf2f(wuv[((size_t)c * 32 + h) * 128 + t]);
    o[(1 * 2 + b) * 4096 + h * 128 + t] = a;
  }
}
__global__ void k_out(const float* o, const u16* woa, const u16* wob, u16* out) {
  int which = blockIdx.y, n = blockIdx.x * 64 + (threadIdx.x & 63), part = threadIdx.x >> 6;
  const u16* w = which ? wob : woa;
  __shared__ float red[16][2][64];
  float a0 = 0, a1 = 0;
  for (int r = part * 256; r < part * 256 + 256; ++r) { float wv = bf2f(w[(size_t)r * 512 + n]); a0 += o[(which * 2 + 0) * 4096 + r] * wv; a1 += o[(which * 2 + 1) * 4096 + r] * wv; }
  red[part][0][threadIdx.x & 63] = a0; red[part][1][threadIdx.x & 63] = a1;
  __syncthreads();
  if (part < 2) { float s = 0; for (int p = 0; p < 16; ++p) s += red[p][part][threadIdx.x & 63]; out[part * 1024 + which * 512 + n] = f2bf(s); }
}


#define R4(x) x x x x
#define R16(x) R4(R4(x))
#define R256(x) R16(R16(x))
#define BLK8K R4(R256("v_fma_f32 %0, %0, %1, %1\n"))
__global__ void __launch_bounds__(64) k_probe(float* sink, int iters) {
  extern __shared__ char smem[];
  float a = threadIdx.x, b = 0.5f; unsigned long long c0 = 0, c1 = 0;
#pragma unroll 1
  for (int it = 0; it < iters; ++it) {
    unsigned long long s = __builtin_amdgcn_s_memrealtime();
    asm volatile(BLK8K : "+v"(a) : "v"(b));
    unsigned long long e = __builtin_amdgcn_s_memrealtime();
    if (it == 0) c0 = e - s; else c1 = e - s;
  }
  if (a == 12345.f) sink[0] = a;
  const unsigned long long t1 = __builtin_amdgcn_s_memrealtime();
  const unsigned long long until = t1 + 1000 * (c1);
  while (__builtin_amdgcn_s_memrealtime() < until) __builtin_amdgcn_s_sleep(4);
}

extern "C" void kernel_launch(void* const* d_in, const int* in_sizes, int n_in, void* d_out, int out_size, void* d_ws, size_t ws_size, hipStream_t stream) {
  const u16 *Ax = (const u16*)d_in[0], *Ak = (const u16*)d_in[1], *Av = (const u16*)d_in[2], *Awq = (const u16*)d_in[3], *Awo = (const u16*)d_in[4];
  const u16 *Bx = (const u16*)d_in[5], *Bc = (const u16*)d_in[6], *Bwq = (const u16*)d_in[7], *Bwuk = (const u16*)d_in[8], *Bwuv = (const u16*)d_in[9], *Bwo = (const u16*)d_in[10];
  float* ws = (float*)d_ws;
  float* qa = ws; float* qb = qa + 8192; float* qabs = qb + 8192; float* poa = qabs + 2 * 32 * 512; float* pma = poa + 64 * NSA * 128; float* pla = pma + 64 * NSA;
  float* pob = pla + 64 * NSA; float* pmb = pob + 64 * NSB * 512; float* plb = pmb + 64 * NSB; float* o = plb + 64 * NSB;
  (void)hipFuncSetAttribute((const void*)k_probe, hipFuncAttributeMaxDynamicSharedMemorySize, 96 * 1024);
  k_probe<<<256, 64, 96 * 1024, stream>>>((float*)d_ws + (32 << 20), 2);
  k_q<<<dim3(64, 2), 256, 0, stream>>>(Ax, Awq, Bx, Bwq, qa, qb);
  k_qabs<<<dim3(32, 8), 64, 0, stream>>>(qb, Bwuk, qabs);
  k_attn_a<<<dim3(NSA, 32, 2), 256, 0, stream>>>(qa, Ak, Av, poa, pma, pla);
  k_attn_b<<<dim3(NSB, 32, 2), 128, 0, stream>>>(qabs, Bc, pob, pmb, plb);
  k_comb<<<dim3(32, 2, 2), 128, 0, stream>>>(poa, pma, pla, pob, pmb, plb, Bwuv, o);
  k_out<<<dim3(8, 2), 1024, 0, stream>>>(o, Awo, Bwo, (u16*)d_out);
}
```

```cpp
#include <hip/hip_runtime.h>
#include <stdint.h>

typedef unsigned short u16;
typedef unsigned u32;
typedef unsigned long long u64;
#define GAS __attribute__((address_space(1)))
#define LAS __attribute__((address_space(3)))
typedef u32 v4u __attribute__((ext_vector_type(4)));
typedef u32 v2u __attribute__((ext_vector_type(2)));
typedef float f32x2 __attribute__((ext_vector_type(2)));
typedef float f32x4 __attribute__((ext_vector_type(4)));
typedef float f32x16 __attribute__((ext_vector_type(16)));
typedef __bf16 bf16x8 __attribute__((ext_vector_type(8)));
typedef __bf16 bf16x2 __attribute__((ext_vector_type(2)));
typedef short s16x4 __attribute__((ext_vector_type(4)));
#define RLX_AGENT __ATOMIC_RELAXED, __HIP_MEMORY_SCOPE_AGENT

constexpr int SK = 8192;
constexpr int NS = 8;
constexpr int TA = 8;
constexpr float C2 = 0.08838834764831845f * 1.4426950408889634f;
constexpr u32 SPIN_CAP = 1u << 16;

constexpr u32 QG_OFF = 4096;
constexpr u32 QA_OFF = QG_OFF + 131072;
constexpr u32 OA_OFF = QA_OFF + 262144;
constexpr u32 LA_OFF = OA_OFF + 524288;
constexpr u32 LB_OFF = LA_OFF + 8192;
constexpr u32 CX_OFF = LB_OFF + 32768;
constexpr u32 YG_OFF = CX_OFF + 262144;
constexpr u32 PB_OFF = YG_OFF + 524288;
constexpr u32 WS_USED = PB_OFF + 4194304;
#define STAMP(k)

constexpr u32 EXTRA = 131072;
constexpr u32 L_XS = EXTRA, L_MRG = EXTRA, L_PART = EXTRA + 16384, L_XBUF = EXTRA + 24576, L_QIMG = EXTRA + 25600, L_MST = EXTRA + 26624;
constexpr u32 L_QB = EXTRA, L_SX = EXTRA, L_PF = EXTRA + 16384, L_BM = EXTRA + 24576, L_BL = EXTRA + 25600, L_OST = EXTRA;

__device__ __forceinline__ u32 cvtpk(float lo, float hi) { bf16x2 p = {(__bf16)lo, (__bf16)hi}; return __builtin_bit_cast(u32, p); }
__device__ __forceinline__ f32x4 mfma16(v4u a, v4u b, f32x4 c) { return __builtin_amdgcn_mfma_f32_16x16x32_bf16(__builtin_bit_cast(bf16x8, a), __builtin_bit_cast(bf16x8, b), c, 0, 0, 0); }
__device__ __forceinline__ f32x4 mfma16h(v2u a, v2u b, f32x4 c) { return __builtin_amdgcn_mfma_f32_16x16x16bf16_1k(__builtin_bit_cast(s16x4, a), __builtin_bit_cast(s16x4, b), c, 0, 0, 0); }
__device__ __forceinline__ f32x16 mfma32(v4u a, v4u b, f32x16 c) { return __builtin_amdgcn_mfma_f32_32x32x16_bf16(__builtin_bit_cast(bf16x8, a), __builtin_bit_cast(bf16x8, b), c, 0, 0, 0); }
__device__ __forceinline__ float ex2(float x) { return __builtin_amdgcn_exp2f(x); }
__device__ __forceinline__ float asf(u32 x) { return __builtin_bit_cast(float, x); }
__device__ __forceinline__ u32 asu(float x) { return __builtin_bit_cast(u32, x); }
template <int CTRL> __device__ __forceinline__ float dpp(float x) { return asf((u32)__builtin_amdgcn_mov_dpp((int)asu(x), CTRL, 0xf, 0xf, true)); }
__device__ __forceinline__ float x16max(float x) { auto s = __builtin_amdgcn_permlane16_swap(asu(x), asu(x), false, false); return fmaxf(asf(s[0]), asf(s[1])); }
__device__ __forceinline__ float x32max(float x) { auto s = __builtin_amdgcn_permlane32_swap(asu(x), asu(x), false, false); return fmaxf(asf(s[0]), asf(s[1])); }
__device__ __forceinline__ float x16sum(float x) { auto s = __builtin_amdgcn_permlane16_swap(asu(x), asu(x), false, false); return asf(s[0]) + asf(s[1]); }
__device__ __forceinline__ float x32sum(float x) { auto s = __builtin_amdgcn_permlane32_swap(asu(x), asu(x), false, false); return asf(s[0]) + asf(s[1]); }
__device__ __forceinline__ float row16max(float x) { x = fmaxf(x, dpp<0x128>(x)); x = fmaxf(x, dpp<0x124>(x)); x = fmaxf(x, dpp<0x122>(x)); return fmaxf(x, dpp<0x121>(x)); }
__device__ __forceinline__ float row16sum(float x) { x += dpp<0x128>(x); x += dpp<0x124>(x); x += dpp<0x122>(x); return x + dpp<0x121>(x); }

#define BAR() asm volatile("s_waitcnt lgkmcnt(0)\n\ts_barrier" ::: "memory")
#define VMWAIT(n) asm volatile("s_waitcnt vmcnt(" #n ")" ::: "memory")

template <bool PACED, bool NT, bool WAR = true>
__device__ __forceinline__ void dma4(u64 base, u32 o0, u32 o1, u32 o2, u32 o3, u32 lds) {
  if (WAR) asm volatile("s_waitcnt lgkmcnt(0)" ::: "memory");
#define DMA_BODY(W, P) \
  asm volatile("s_mov_b32 m0, %5\n\ts_nop 0\n\t" W "global_load_lds_dwordx4 %0, %4" P "\n\t" W "global_load_lds_dwordx4 %1, %4 offset:1024" P "\n\t" \
               W "global_load_lds_dwordx4 %2, %4 offset:2048" P "\n\t" W "global_load_lds_dwordx4 %3, %4 offset:3072" P \
               :: "v"(o0), "v"(o1), "v"(o2), "v"(o3), "s"(base), "s"(lds) : "memory")
  if (PACED && NT) DMA_BODY("s_waitcnt vmcnt(3)\n\t", " nt");
  else if (PACED) DMA_BODY("s_waitcnt vmcnt(3)\n\t", "");
  else if (NT) DMA_BODY("", " nt");
  else DMA_BODY("", "");
#undef DMA_BODY
}

struct Lane { u32 m0, m1, m2, m3; };
__device__ __forceinline__ f32x4 gemv4(const v4u (&W)[4], v2u xs, const Lane& L, f32x4 acc) {
#pragma unroll
  for (int i = 0; i < 4; ++i) {
    const u32 src = i < 2 ? xs.x : xs.y;
    const u32 pp = __builtin_amdgcn_perm(src, src, (i & 1) ? 0x03020302u : 0x01000100u);
    acc = mfma16(W[i], v4u{pp & L.m0, pp & L.m1, pp & L.m2, pp & L.m3}, acc);
  }
  return acc;
}
__device__ __forceinline__ void ldsW(v4u (&W)[4], LAS char* sm, u32 unit, int g, int c) {
#pragma unroll
  for (int i = 0; i < 4; ++i) W[i] = *(LAS v4u*)(sm + unit + (4 * g + i) * 256 + c * 16);
}
__device__ __forceinline__ float reduce8(LAS char* sm, int t) {
  float s = 0.f;
#pragma unroll
  for (int w = 0; w < 8; ++w) s += *(LAS float*)(sm + L_PART + w * 1024 + t * 4);
  return s;
}
__device__ __forceinline__ void st_gran(char* ws, u32 off, u32 val, u32 tag) { __hip_atomic_store((GAS u64*)(ws + off), ((u64)tag << 32) | val, RLX_AGENT); }
__device__ __forceinline__ u64 ld_gran(char* ws, u32 off) { return __hip_atomic_load((GAS u64*)(ws + off), RLX_AGENT); }
__device__ __forceinline__ v4u ld2(__amdgpu_buffer_rsrc_t rs, u32 off) { return __builtin_amdgcn_raw_buffer_load_b128(rs, off, 0, 16); }
__device__ __forceinline__ bool spin(u32& n) { __builtin_amdgcn_s_sleep(2); asm volatile("" ::: "memory"); return ++n > SPIN_CAP; }

__global__ void __launch_bounds__(576, 1)
fused_decode(const u16* __restrict__ Ax, const u16* __restrict__ Ak, const u16* __restrict__ Av, const u16* __restrict__ Awq, const u16* __restrict__ Awo,
             const u16* __restrict__ Bx, const u16* __restrict__ Bc, const u16* __restrict__ Bwq, const u16* __restrict__ Bwuk, const u16* __restrict__ Bwuv,
             const u16* __restrict__ Bwo, u16* __restrict__ out, char* ws) {
  __shared__ __attribute__((aligned(16))) char smem[163840];
  LAS char* sm = (LAS char*)smem;
  const u32 sm0 = (u32)(size_t)sm;
  STAMP(0);
  const int tid = threadIdx.x, lane = tid & 63, w = __builtin_amdgcn_readfirstlane(tid >> 6), g = lane >> 4, c = lane & 15;
  const int bid = blockIdx.x, xcd = bid & 7, role = bid >> 7, jj = (bid >> 3) & 15, qt = jj & 3, h = 4 * xcd + (jj >> 2);
  u64 pAx = (u64)Ax, pAk = (u64)Ak, pAv = (u64)Av, pAwq = (u64)Awq, pAwo = (u64)Awo, pBx = (u64)Bx, pBc = (u64)Bc, pBwq = (u64)Bwq, pBwuk = (u64)Bwuk, pBwuv = (u64)Bwuv, pBwo = (u64)Bwo, pOut = (u64)out;
  asm volatile("" : "+s"(pAx), "+s"(pAk), "+s"(pAv), "+s"(pAwq), "+s"(pAwo), "+s"(pBx), "+s"(pBc), "+s"(pBwq), "+s"(pBwuk), "+s"(pBwuv), "+s"(pBwo), "+s"(pOut));
  u32 tag = *(const __attribute__((address_space(4))) u32*)(u64)ws;
  const __amdgpu_buffer_rsrc_t rs = __builtin_amdgcn_make_buffer_rsrc(ws, 0, (int)WS_USED, 0x00020000);
  Lane L;
  { const int j0 = c & 7; const u32 hm = (j0 & 1) ? 0xFFFF0000u : 0x0000FFFFu;
    L.m0 = (j0 >> 1) == 0 ? hm : 0u; L.m1 = (j0 >> 1) == 1 ? hm : 0u; L.m2 = (j0 >> 1) == 2 ? hm : 0u; L.m3 = (j0 >> 1) == 3 ? hm : 0u; }
  const int tb = (tid >> 5) & 1, tcol = 32 * ((tid >> 6) & 3) + 8 * (tid & 3) + ((tid >> 2) & 7);
  v4u FW[4];

  if (role == 0) {
    const int b = jj >> 3, s = jj & 7;
    if (w == 8) {
      asm volatile("" : "+s"(tag)); tag += 1u;
      BAR(); BAR(); BAR();
      u32 n = 0;
      const u32 so = QA_OFF + (u32)(((4 * xcd + (c >> 2)) * 4 + (c & 3)) * 256 + b * 128) * 8;
      for (;;) { const u64 x = ld_gran(ws, so); if (__all((u32)(x >> 32) == tag) || spin(n)) break; }
      STAMP(11);
      for (;;) {
        v4u q[16]; bool ok = true;
#pragma unroll
        for (int k = 0; k < 16; ++k) q[k] = ld2(rs, QA_OFF + (u32)(((4 * xcd * 4 + k) * 256 + b * 128 + 2 * lane) * 8));
#pragma unroll
        for (int k = 0; k < 16; ++k) ok &= (q[k].y == tag) & (q[k].w == tag);
#pragma unroll
        for (int hh = 0; hh < 4; ++hh) {
          const float a0 = (asf(q[4 * hh].x) + asf(q[4 * hh + 1].x)) + (asf(q[4 * hh + 2].x) + asf(q[4 * hh + 3].x));
          const float a1 = (asf(q[4 * hh].z) + asf(q[4 * hh + 1].z)) + (asf(q[4 * hh + 2].z) + asf(q[4 * hh + 3].z));
          *(LAS u32*)(sm + L_QIMG + hh * 256 + lane * 4) = cvtpk(a0, a1);
        }
        if (__all(ok) || spin(n)) break;
      }
      STAMP(12);
      BAR();
      return;
    }
    const u32 ring = sm0 + w * 16384, ringo = w * 16384;
    {
      const int q4 = (lane >> 2) & 3, xb = (lane >> 1) & 1;
      const u64 src = (lane < 16 ? pBx : pAx) + (u32)(xb * 512 + 128 * (lane < 16 ? q4 : qt) + 16 * w + 8 * (lane & 1)) * 2;
      asm volatile("s_mov_b32 m0, %1\n\ts_nop 4\n\tglobal_load_lds_dwordx4 %0, off" :: "v"(src), "s"(sm0 + L_XS + w * 1024) : "memory");
    }
    const u32 wo0 = g * 8192 + c * 16, wo1 = wo0 + 31744, wo2 = wo0 + 2 * 31744, wo3 = wo0 + 3 * 31744;
    const u32 sw = (c ^ g) * 16;
    const u64 wq_b = pBwq + (u32)(16 * w * 8192 + h * 256);
    dma4<false, false, false>(wq_b + (u32)(qt * (128 * 8192)), wo0, wo1, wo2, wo3, ring + 4096);
    dma4<false, true, false>(pBwuk + (u32)((128 * qt + 16 * w) * 8192 + h * 256), g * 8192 + sw, g * 8192 + (sw ^ 64) + 31744, g * 8192 + (sw ^ 128) + 2 * 31744, g * 8192 + (sw ^ 192) + 3 * 31744, ring + 3 * 4096);
    dma4<false, false, false>(wq_b + (u32)(((qt + 1) & 3) * (128 * 8192)), wo0, wo1, wo2, wo3, ring + 2 * 4096);
    dma4<false, false, false>(wq_b + (u32)(((qt + 2) & 3) * (128 * 8192)), wo0, wo1, wo2, wo3, ring);
    const u32 xso = L_XS + w * 1024 + (c >> 3) * 32 + g * 8;
    const u32 ko0 = g * 256 + sw, ko1 = g * 256 + (sw ^ 64), ko2 = g * 256 + (sw ^ 128), ko3 = g * 256 + (sw ^ 192), vo = lane * 16;
    const u32 kv_off = (u32)((b * 8 + xcd) * SK + s * (128 * TA) + w * (16 * TA)) * 256;
    const u64 kb = pAk + kv_off, vb = pAv + kv_off;
    u64 fb = pAwo + (u32)((128 * h + 16 * w) * 1024 + qt * 256);
    asm volatile("" : "+s"(fb));
    f32x4 acc = {0.f, 0.f, 0.f, 0.f};
    v4u W[4];
    asm volatile("" : "+s"(tag)); tag += 1u;
    VMWAIT(12);
    ldsW(W, sm, ringo + 4096, g, c); acc = gemv4(W, *(LAS v2u*)(sm + xso + qt * 64), L, acc);
    dma4<false, false>(wq_b + (u32)(((qt + 3) & 3) * (128 * 8192)), wo0, wo1, wo2, wo3, ring + 4096);
    VMWAIT(8);
    ldsW(W, sm, ringo + 2 * 4096, g, c); acc = gemv4(W, *(LAS v2u*)(sm + xso + ((qt + 1) & 3) * 64), L, acc);
    dma4<false, true>(pAwq + (u32)((128 * qt + 16 * w) * 8192 + h * 256), wo0, wo1, wo2, wo3, ring + 2 * 4096);
    VMWAIT(8);
    ldsW(W, sm, ringo, g, c); acc = gemv4(W, *(LAS v2u*)(sm + xso + ((qt + 2) & 3) * 64), L, acc);
    dma4<false, true>(kb, ko0, ko1, ko2, ko3, ring);
    VMWAIT(8);
    ldsW(W, sm, ringo + 4096, g, c); acc = gemv4(W, *(LAS v2u*)(sm + xso + ((qt + 3) & 3) * 64), L, acc);
    *(LAS f32x4*)(sm + L_PART + w * 1024 + lane * 16) = acc;
    STAMP(1);
    BAR();
    if (tid < 256) *(LAS __bf16*)(sm + L_XBUF + (tb * 128 + tcol) * 2) = (__bf16)reduce8(sm, tid);
    BAR();
    {
      f32x4 qa = {0.f, 0.f, 0.f, 0.f};
#pragma unroll
      for (int k = 0; k < 4; ++k)
        qa = mfma16(*(LAS v4u*)(sm + ringo + 3 * 4096 + c * 256 + ((4 * k + g) ^ c) * 16), *(LAS v4u*)(sm + L_XBUF + (c & 1) * 256 + k * 64 + g * 16), qa);
      if (c < 2) {
        const int c0 = 128 * qt + 16 * w + 4 * g;
        const u32 G = ((c0 >> 3) * 32 + h) * 4 + ((c0 & 7) >> 1);
        __builtin_amdgcn_raw_buffer_store_b128(v4u{cvtpk(qa.x, qa.y), tag, cvtpk(qa.z, qa.w), tag}, rs, QG_OFF + (u32)c * 65536 + G * 8, 0, 16);
      }
    }
    STAMP(2);
    VMWAIT(5);
    ldsW(W, sm, ringo + 2 * 4096, g, c);
    acc = gemv4(W, *(LAS v2u*)(sm + xso + 256), L, f32x4{0.f, 0.f, 0.f, 0.f});
    *(LAS f32x4*)(sm + L_PART + w * 1024 + lane * 16) = acc;
    BAR();
    if (tid < 256) st_gran(ws, QA_OFF + (u32)((h * 4 + qt) * 256 + tb * 128 + tcol) * 8, asu(reduce8(sm, tid)), tag);
    STAMP(3);
    dma4<true, true>(vb, vo, vo, vo, vo, ring + 4096);
    dma4<true, true>(kb + 4096, ko0, ko1, ko2, ko3, ring + 2 * 4096);
    dma4<true, true>(vb + 4096, vo, vo, vo, vo, ring + 3 * 4096);
    STAMP(4);
    BAR();
    STAMP(5);
    v4u qf[4];
#pragma unroll
    for (int k = 0; k < 4; ++k) qf[k] = *(LAS v4u*)(sm + L_QIMG + (c & 3) * 256 + k * 64 + g * 16);
    const u32 pm0 = (c >> 2) == 0 ? 0x0000FFFFu : (c >> 2) == 1 ? 0xFFFF0000u : 0u, pm1 = (c >> 2) == 2 ? 0x0000FFFFu : (c >> 2) == 3 ? 0xFFFF0000u : 0u;
    float m = -INFINITY, l = 0.f;
    f32x4 o0 = {0.f, 0.f, 0.f, 0.f}, o1 = {0.f, 0.f, 0.f, 0.f};
    const u32 kr = ringo + c * 256, vr = ringo + 4096 + g * 1024 + c * 16;
#pragma unroll 1
    for (int t = 0; t < TA; ++t) {
      const u32 so = (t & 1) * 8192;
      f32x4 sa = {0.f, 0.f, 0.f, 0.f}, sb = {0.f, 0.f, 0.f, 0.f};
      sa = mfma16(*(LAS v4u*)(sm + kr + so + ((0 + g) ^ c) * 16), qf[0], sa);
      sb = mfma16(*(LAS v4u*)(sm + kr + so + ((4 + g) ^ c) * 16), qf[1], sb);
      sa = mfma16(*(LAS v4u*)(sm + kr + so + ((8 + g) ^ c) * 16), qf[2], sa);
      sb = mfma16(*(LAS v4u*)(sm + kr + so + ((12 + g) ^ c) * 16), qf[3], sb);
      v4u V[4];
#pragma unroll
      for (int i = 0; i < 4; ++i) V[i] = *(LAS v4u*)(sm + vr + so + i * 256);
      sa += sb;
      const float mn = fmaxf(m, x32max(x16max(fmaxf(fmaxf(sa.x, sa.y), fmaxf(sa.z, sa.w)))));
      const float al = ex2((m - mn) * C2), mc = mn * C2;
      m = mn;
      float p[4] = {ex2(fmaf(sa.x, C2, -mc)), ex2(fmaf(sa.y, C2, -mc)), ex2(fmaf(sa.z, C2, -mc)), ex2(fmaf(sa.w, C2, -mc))};
      l = fmaf(l, al, (p[0] + p[1]) + (p[2] + p[3]));
      o0 *= al; o1 *= al;
#pragma unroll
      for (int i = 0; i < 4; ++i) {
        const u32 pp = cvtpk(p[i], p[i]);
        const v2u y = {pp & pm0, pp & pm1};
        o0 = mfma16h(v2u{V[i].x, V[i].y}, y, o0);
        o1 = mfma16h(v2u{V[i].z, V[i].w}, y, o1);
      }
      if (t + 2 < TA) {
        dma4<true, true>(kb + (u32)((t + 2) * 4096), ko0, ko1, ko2, ko3, ring + so);
        dma4<true, true>(vb + (u32)((t + 2) * 4096), vo, vo, vo, vo, ring + so + 4096);
      } else if (t + 2 == TA) {
        const u32 f0 = g * 1024 + c * 16;
        dma4<true, true>(fb, f0, f0 + 3072, f0 + 2 * 3072, f0 + 3 * 3072, ring + so);
      }
    }
    STAMP(6);
    l = x32sum(x16sum(l));
    *(LAS f32x4*)(sm + L_MRG + w * 2048 + lane * 16) = o0;
    *(LAS f32x4*)(sm + L_MRG + w * 2048 + 1024 + lane * 16) = o1;
    if (lane < 4) { *(LAS float*)(sm + L_MST + lane * 32 + w * 4) = m; *(LAS float*)(sm + L_MST + 128 + lane * 32 + w * 4) = l; }
    BAR();
    if (tid < 256) {
      const int hh = (tid >> 1) & 3;
      const f32x4 ma = *(LAS f32x4*)(sm + L_MST + hh * 32), mb = *(LAS f32x4*)(sm + L_MST + hh * 32 + 16);
      const f32x4 la = *(LAS f32x4*)(sm + L_MST + 128 + hh * 32), lb = *(LAS f32x4*)(sm + L_MST + 128 + hh * 32 + 16);
      const float M = fmaxf(fmaxf(fmaxf(ma.x, ma.y), fmaxf(ma.z, ma.w)), fmaxf(fmaxf(mb.x, mb.y), fmaxf(mb.z, mb.w)));
      const float f[8] = {ex2((ma.x - M) * C2), ex2((ma.y - M) * C2), ex2((ma.z - M) * C2), ex2((ma.w - M) * C2),
                          ex2((mb.x - M) * C2), ex2((mb.y - M) * C2), ex2((mb.z - M) * C2), ex2((mb.w - M) * C2)};
      const float Ls = ((f[0] * la.x + f[1] * la.y) + (f[2] * la.z + f[3] * la.w)) + ((f[4] * lb.x + f[5] * lb.y) + (f[6] * lb.z + f[7] * lb.w));
      f32x2 a = {0.f, 0.f};
#pragma unroll
      for (int ww = 0; ww < 8; ++ww) a += f[ww] * *(LAS f32x2*)(sm + L_MRG + ww * 2048 + tid * 8);
      const float inv = 1.f / Ls;
      const u32 row = (u32)((4 * xcd + hh) * 2 + b) * 16 + s;
      const u32 idx = 32 * (tid >> 7) + ((tid >> 3) & 15) * 2 + (tid & 1);
      st_gran(ws, OA_OFF + (row * 64 + idx) * 8, cvtpk(a.x * inv, a.y * inv), tag);
      if ((tid & ~6) == 0) st_gran(ws, LA_OFF + row * 8, asu(fmaf(M, C2, __builtin_amdgcn_logf(Ls))), tag);
    }
    STAMP(7);
    {
      f32x4 oc = {0.f, 0.f, 0.f, 0.f};
      if (4 * w < 2 * NS) {
        const int r = 4 * w + g, rb = r / NS, rsp = r % NS;
        const u32 ro = OA_OFF + (u32)(((h * 2 + rb) * 16 + rsp) * 64 + 4 * c) * 8, lo = LA_OFF + (u32)((h * 2 + (g & 1)) * 16 + (c < NS ? c : 0)) * 8;
        v4u x0, x1; u64 ls; u32 n = 0;
        for (;;) {
          x0 = ld2(rs, ro); x1 = ld2(rs, ro + 16); ls = ld_gran(ws, lo);
          const bool ok = (x0.y == tag) & (x0.w == tag) & (x1.y == tag) & (x1.w == tag) & ((u32)(ls >> 32) == tag);
          if (__all(ok) || spin(n)) break;
        }
        const float lse = c < NS ? asf((u32)ls) : -INFINITY;
        const float e = ex2(lse - row16max(lse));
        float wt = e / row16sum(e);
        wt = __shfl(wt, rb * 16 + rsp);
        const u32 pp = (c >> 3) == rb ? cvtpk(wt, wt) : 0u;
        oc = mfma16(v4u{x0.x, x0.z, x1.x, x1.z}, v4u{pp & L.m0, pp & L.m1, pp & L.m2, pp & L.m3}, oc);
      }
      *(LAS f32x4*)(sm + L_PART + w * 1024 + lane * 16) = oc;
    }
    STAMP(8);
    VMWAIT(0);
    ldsW(FW, sm, ringo + (TA & 1) * 8192, g, c);
    BAR();
    if (tid < 256) {
      const int id = tcol >> 1, d = 8 * (4 * ((id >> 3) & 3) + 2 * (id & 1) + (tcol & 1)) + 4 * (id >> 5) + ((id >> 1) & 3);
      *(LAS __bf16*)(sm + L_XBUF + (tb * 128 + d) * 2) = (__bf16)reduce8(sm, tid);
    }
  } else {
    if (w == 8) return;
    const int b = xcd >> 2, s = (xcd & 3) * 16 + jj, h2 = lane >> 5, hd = lane & 31;
    {
      const u64 cb = pBc + (u32)((b * SK + s * 128 + 16 * w) * 1024);
      const u32 lo = lane * 16;
      asm volatile("s_nop 4" ::: "memory");
#pragma unroll
      for (int k = 0; k < 4; ++k) dma4<false, true, false>(cb + k * 4096, lo ^ (k * 16), lo ^ ((4 + k) * 16), lo ^ ((8 + k) * 16), lo ^ ((12 + k) * 16), sm0 + w * 16384 + k * 4096);
    }
    asm volatile("" : "+s"(tag)); tag += 1u;
    {
      u32 n = 0;
      for (;;) {
        v4u q[8]; bool ok = true;
#pragma unroll
        for (int k = 0; k < 8; ++k) q[k] = ld2(rs, QG_OFF + (u32)b * 65536 + (u32)(k * 512 + tid) * 16);
#pragma unroll
        for (int k = 0; k < 8; ++k) { ok &= (q[k].y == tag) & (q[k].w == tag); *(LAS v2u*)(sm + L_QB + (k * 512 + tid) * 8) = v2u{q[k].x, q[k].z}; }
        if (__all(ok) || spin(n)) break;
      }
    }
    VMWAIT(0);
    STAMP(1);
    v4u UW[4];
#pragma unroll
    for (int i = 0; i < 4; ++i) UW[i] = __builtin_nontemporal_load((const GAS v4u*)(pBwuv + (u32)((128 * qt + 16 * w + 4 * g + i) * 8192 + h * 256 + c * 16)));
#pragma unroll
    for (int i = 0; i < 4; ++i) FW[i] = __builtin_nontemporal_load((const GAS v4u*)(pBwo + (u32)((128 * h + 16 * w + 4 * g + i) * 1024 + qt * 256 + c * 16)));
    BAR();
    float sv[8];
    {
      const int kt = w & 3, ch = w >> 2;
      f32x16 sc;
#pragma unroll
      for (int k = 0; k < 16; ++k) sc[k] = 0.f;
      const u32 f = ((hd & 3) << 2) | ((hd >> 2) & 3);
      const u32 ca = (32 * kt + hd) * 1024 + ch * 512, cx = (h2 ^ f) * 16, qa = L_QB + ch * 16384 + lane * 16;
#pragma unroll
      for (int k = 0; k < 16; ++k)
        sc = mfma32(*(LAS v4u*)(sm + ca + (k >> 3) * 256 + (cx ^ ((k & 7) * 32))), *(LAS v4u*)(sm + qa + k * 1024), sc);
      STAMP(2);
      BAR();
      f32x4 s0, s1;
#pragma unroll
      for (int k = 0; k < 4; ++k) { s0[k] = ch ? sc[k] : sc[8 + k]; s1[k] = ch ? sc[4 + k] : sc[12 + k]; }
      *(LAS f32x4*)(sm + L_SX + w * 2048 + lane * 16) = s0;
      *(LAS f32x4*)(sm + L_SX + w * 2048 + 1024 + lane * 16) = s1;
      BAR();
      s0 = *(LAS f32x4*)(sm + L_SX + (w ^ 4) * 2048 + lane * 16);
      s1 = *(LAS f32x4*)(sm + L_SX + (w ^ 4) * 2048 + 1024 + lane * 16);
#pragma unroll
      for (int k = 0; k < 4; ++k) { sv[k] = s0[k] + (ch ? sc[8 + k] : sc[k]); sv[4 + k] = s1[k] + (ch ? sc[12 + k] : sc[4 + k]); }
      const float mw = x32max(fmaxf(fmaxf(fmaxf(sv[0], sv[1]), fmaxf(sv[2], sv[3])), fmaxf(fmaxf(sv[4], sv[5]), fmaxf(sv[6], sv[7]))));
      if (lane < 32) *(LAS float*)(sm + L_BM + hd * 32 + w * 4) = mw;
    }
    BAR();
    float mc;
    {
      const f32x4 ma = *(LAS f32x4*)(sm + L_BM + hd * 32), mb = *(LAS f32x4*)(sm + L_BM + hd * 32 + 16);
      mc = C2 * fmaxf(fmaxf(fmaxf(ma.x, ma.y), fmaxf(ma.z, ma.w)), fmaxf(fmaxf(mb.x, mb.y), fmaxf(mb.z, mb.w)));
#pragma unroll
      for (int k = 0; k < 8; ++k) sv[k] = ex2(fmaf(sv[k], C2, -mc));
      const float ls = x32sum(((sv[0] + sv[1]) + (sv[2] + sv[3])) + ((sv[4] + sv[5]) + (sv[6] + sv[7])));
      if (lane < 32) *(LAS float*)(sm + L_BL + hd * 32 + w * 4) = ls;
      *(LAS v4u*)(sm + L_PF + (2 * (w & 3) + (w >> 2)) * 1024 + lane * 16) = v4u{cvtpk(sv[0], sv[1]), cvtpk(sv[2], sv[3]), cvtpk(sv[4], sv[5]), cvtpk(sv[6], sv[7])};
    }
    BAR();
    f32x16 oa, ob;
#pragma unroll
    for (int k = 0; k < 16; ++k) { oa[k] = 0.f; ob[k] = 0.f; }
    {
      const int q = (lane >> 2) & 3, p = lane & 3, blk = (lane >> 4) & 1;
      const u32 base = (4 * h2 + q) * 1024 + (w >> 1) * 256 + 8 * (p & 1), lo4 = 8 * (w & 1) + 2 * blk + (p >> 1);
      const u32 a00 = base + 16 * (lo4 ^ ((q << 2) | h2)), a01 = base + 16 * ((lo4 + 4) ^ ((q << 2) | h2));
      const u32 a10 = base + 8192 + 16 * (lo4 ^ ((q << 2) | (2 + h2))), a11 = base + 8192 + 16 * ((lo4 + 4) ^ ((q << 2) | (2 + h2)));
#pragma unroll
      for (int ks = 0; ks < 8; ++ks) {
        const v4u pb = *(LAS v4u*)(sm + L_PF + ks * 1024 + lane * 16);
        const v2u t00 = __builtin_bit_cast(v2u, __builtin_amdgcn_ds_read_tr16_b64_v4i16((LAS s16x4*)(sm + a00 + ks * 16384)));
        const v2u t10 = __builtin_bit_cast(v2u, __builtin_amdgcn_ds_read_tr16_b64_v4i16((LAS s16x4*)(sm + a10 + ks * 16384)));
        const v2u t01 = __builtin_bit_cast(v2u, __builtin_amdgcn_ds_read_tr16_b64_v4i16((LAS s16x4*)(sm + a01 + ks * 16384)));
        const v2u t11 = __builtin_bit_cast(v2u, __builtin_amdgcn_ds_read_tr16_b64_v4i16((LAS s16x4*)(sm + a11 + ks * 16384)));
        oa = mfma32(v4u{t00.x, t00.y, t10.x, t10.y}, pb, oa);
        ob = mfma32(v4u{t01.x, t01.y, t11.x, t11.y}, pb, ob);
      }
    }
    float lse;
    {
      const f32x4 la = *(LAS f32x4*)(sm + L_BL + hd * 32), lb = *(LAS f32x4*)(sm + L_BL + hd * 32 + 16);
      const float Ls = ((la.x + la.y) + (la.z + la.w)) + ((lb.x + lb.y) + (lb.z + lb.w));
      lse = mc + __builtin_amdgcn_logf(Ls);
      const float inv = 1.f / Ls;
      STAMP(3);
      BAR();
#pragma unroll
      for (int k = 0; k < 4; ++k) {
        *(LAS v2u*)(sm + L_OST + hd * 1024 + ((8 * w + k) ^ (hd & 15)) * 16 + 8 * h2) = v2u{cvtpk(oa[4 * k] * inv, oa[4 * k + 1] * inv), cvtpk(oa[4 * k + 2] * inv, oa[4 * k + 3] * inv)};
        *(LAS v2u*)(sm + L_OST + hd * 1024 + ((8 * w + 4 + k) ^ (hd & 15)) * 16 + 8 * h2) = v2u{cvtpk(ob[4 * k] * inv, ob[4 * k + 1] * inv), cvtpk(ob[4 * k + 2] * inv, ob[4 * k + 3] * inv)};
      }
    }
    BAR();
#pragma unroll
    for (int i = 0; i < 4; ++i) {
      const int hh = 4 * w + i;
      const v4u v = *(LAS v4u*)(sm + L_OST + hh * 1024 + (lane ^ (hh & 15)) * 16);
      __builtin_amdgcn_raw_buffer_store_b128(v, rs, PB_OFF + (u32)(((hh * 4 + g) * 2 + b) * 64 + s) * 256 + c * 16, 0, 16);
    }
    VMWAIT(0);
    BAR();
    if (tid < 32) st_gran(ws, LB_OFF + (u32)((tid * 2 + b) * 64 + s) * 8, asu(lse), tag);
    STAMP(4);
    {
      u64 l0, l1; u32 n = 0;
      for (;;) {
        l0 = ld_gran(ws, LB_OFF + (u32)(h * 128 + lane) * 8); l1 = ld_gran(ws, LB_OFF + (u32)(h * 128 + 64 + lane) * 8);
        if (__all(((u32)(l0 >> 32) == tag) & ((u32)(l1 >> 32) == tag)) || spin(n)) break;
      }
      asm volatile("" ::: "memory");
      STAMP(5);
      v4u X[4];
#pragma unroll
      for (int i = 0; i < 4; ++i) X[i] = ld2(rs, PB_OFF + (u32)((h * 4 + qt) * 128 + 16 * w + 4 * g + i) * 256 + c * 16);
      const float ls = asf((u32)(w < 4 ? l0 : l1));
      const float e = ex2(ls - x32max(x16max(row16max(ls))));
      const float wt = e / x32sum(x16sum(row16sum(e)));
      const int sl = (16 * w + 4 * g) & 63;
      const bool mine = (c >> 3) == (w >> 2);
      const float w0 = __shfl(wt, sl), w1 = __shfl(wt, sl + 1), w2 = __shfl(wt, sl + 2), w3 = __shfl(wt, sl + 3);
      const v2u xs = mine ? v2u{cvtpk(w0, w1), cvtpk(w2, w3)} : v2u{0u, 0u};
      *(LAS f32x4*)(sm + L_PART + w * 1024 + lane * 16) = gemv4(X, xs, L, f32x4{0.f, 0.f, 0.f, 0.f});
    }
    BAR();
    if (tid < 256) *(LAS __bf16*)(sm + L_XBUF + (tb * 128 + tcol) * 2) = (__bf16)reduce8(sm, tid);
    BAR();
    *(LAS f32x4*)(sm + L_PART + w * 1024 + lane * 16) = gemv4(UW, *(LAS v2u*)(sm + L_XBUF + (c >> 3) * 256 + w * 32 + g * 8), L, f32x4{0.f, 0.f, 0.f, 0.f});
    BAR();
    if (tid < 256) {
      st_gran(ws, CX_OFF + (u32)((h * 4 + qt) * 256 + tid) * 8, asu(reduce8(sm, tid)), tag);
      STAMP(6);
      u64 x0, x1, x2, x3; u32 n = 0;
      for (;;) {
        x0 = ld_gran(ws, CX_OFF + (u32)(h * 1024 + tid) * 8); x1 = ld_gran(ws, CX_OFF + (u32)(h * 1024 + 256 + tid) * 8);
        x2 = ld_gran(ws, CX_OFF + (u32)(h * 1024 + 512 + tid) * 8); x3 = ld_gran(ws, CX_OFF + (u32)(h * 1024 + 768 + tid) * 8);
        if (__all(((u32)(x0 >> 32) == tag) & ((u32)(x1 >> 32) == tag) & ((u32)(x2 >> 32) == tag) & ((u32)(x3 >> 32) == tag)) || spin(n)) break;
      }
      *(LAS __bf16*)(sm + L_XBUF + 512 + (tb * 128 + tcol) * 2) = (__bf16)((asf((u32)x0) + asf((u32)x1)) + (asf((u32)x2) + asf((u32)x3)));
      STAMP(7);
    }
  }
  BAR();
  *(LAS f32x4*)(sm + L_PART + w * 1024 + lane * 16) = gemv4(FW, *(LAS v2u*)(sm + L_XBUF + role * 512 + (c >> 3) * 256 + w * 32 + g * 8), L, f32x4{0.f, 0.f, 0.f, 0.f});
  BAR();
  if (tid < 256) st_gran(ws, YG_OFF + (u32)((((role * 2 + tb) * 64 + 16 * qt + (tcol >> 3)) * 32 + h) * 8 + (tcol & 7)) * 8, asu(reduce8(sm, tid)), tag);
  STAMP(9);
  if (w == 7) {
    const int idx = bid & 127;
    const u32 yo = YG_OFF + (u32)((role * 128 + idx) * 256 + lane * 4) * 8;
    v4u y0, y1; u32 n = 0;
    for (;;) {
      y0 = ld2(rs, yo); y1 = ld2(rs, yo + 16);
      if (__all((y0.y == tag) & (y0.w == tag) & (y1.y == tag) & (y1.w == tag)) || spin(n)) break;
    }
    float v[4] = {asf(y0.x), asf(y0.z), asf(y1.x), asf(y1.z)};
#pragma unroll
    for (int k = 0; k < 4; ++k) { float x = v[k]; x += dpp<0x128>(x); x += dpp<0x124>(x); x += dpp<0x122>(x); v[k] = x32sum(x16sum(x)); }
    if (lane < 2) *(GAS v2u*)(pOut + (u32)(((idx >> 6) * 1024 + role * 512 + (idx & 63) * 8 + lane * 4) * 2)) = v2u{cvtpk(v[0], v[1]), cvtpk(v[2], v[3])};
    if (bid == 255 && lane == 0) *(GAS u32*)ws = tag;
  }
}

extern "C" void kernel_launch(void* const* d_in, const int* in_sizes, int n_in, void* d_out, int out_size, void* d_ws, size_t ws_size, hipStream_t stream) {
  fused_decode<<<256, 576, 0, stream>>>((const u16*)d_in[0], (const u16*)d_in[1], (const u16*)d_in[2], (const u16*)d_in[3], (const u16*)d_in[4],
                                        (const u16*)d_in[5], (const u16*)d_in[6], (const u16*)d_in[7], (const u16*)d_in[8], (const u16*)d_in[9],
                                        (const u16*)d_in[10], (u16*)d_out, (char*)d_ws);
}
```

```cpp
#include <hip/hip_runtime.h>
#include <stdint.h>

typedef unsigned short u16;
typedef unsigned u32;
typedef unsigned long long u64;
#define GAS __attribute__((address_space(1)))
#define LAS __attribute__((address_space(3)))
typedef u32 v4u __attribute__((ext_vector_type(4)));
typedef u32 v2u __attribute__((ext_vector_type(2)));
typedef float f32x2 __attribute__((ext_vector_type(2)));
typedef float f32x4 __attribute__((ext_vector_type(4)));
typedef float f32x16 __attribute__((ext_vector_type(16)));
typedef __bf16 bf16x8 __attribute__((ext_vector_type(8)));
typedef __bf16 bf16x2 __attribute__((ext_vector_type(2)));
typedef short s16x4 __attribute__((ext_vector_type(4)));
#define RLX_AGENT __ATOMIC_RELAXED, __HIP_MEMORY_SCOPE_AGENT

constexpr int SK = 8192;
constexpr int NS = 8;
constexpr int TA = 8;
constexpr float C2 = 0.08838834764831845f * 1.4426950408889634f;
constexpr u32 SPIN_CAP = 1u << 16;

constexpr u32 QG_OFF = 4096;
constexpr u32 QA_OFF = QG_OFF + 131072;
constexpr u32 OA_OFF = QA_OFF + 262144;
constexpr u32 LA_OFF = OA_OFF + 524288;
constexpr u32 LB_OFF = LA_OFF + 8192;
constexpr u32 CX_OFF = LB_OFF + 32768;
constexpr u32 YG_OFF = CX_OFF + 262144;
constexpr u32 PB_OFF = YG_OFF + 524288;
constexpr u32 WS_USED = PB_OFF + 4194304;
#define AMPK 2
#define AMPR 0
constexpr int NSTAMP_AMP = 16;
#define STAMP(k) do { if ((k) == 0 || (k) == AMPK) st[k] = __builtin_amdgcn_s_memrealtime(); } while (0)

constexpr u32 EXTRA = 131072;
constexpr u32 L_XS = EXTRA, L_MRG = EXTRA, L_PART = EXTRA + 16384, L_XBUF = EXTRA + 24576, L_QIMG = EXTRA + 25600, L_MST = EXTRA + 26624;
constexpr u32 L_QB = EXTRA, L_SX = EXTRA, L_PF = EXTRA + 16384, L_BM = EXTRA + 24576, L_BL = EXTRA + 25600, L_OST = EXTRA;

__device__ __forceinline__ u32 cvtpk(float lo, float hi) { bf16x2 p = {(__bf16)lo, (__bf16)hi}; return __builtin_bit_cast(u32, p); }
__device__ __forceinline__ f32x4 mfma16(v4u a, v4u b, f32x4 c) { return __builtin_amdgcn_mfma_f32_16x16x32_bf16(__builtin_bit_cast(bf16x8, a), __builtin_bit_cast(bf16x8, b), c, 0, 0, 0); }
__device__ __forceinline__ f32x4 mfma16h(v2u a, v2u b, f32x4 c) { return __builtin_amdgcn_mfma_f32_16x16x16bf16_1k(__builtin_bit_cast(s16x4, a), __builtin_bit_cast(s16x4, b), c, 0, 0, 0); }
__device__ __forceinline__ f32x16 mfma32(v4u a, v4u b, f32x16 c) { return __builtin_amdgcn_mfma_f32_32x32x16_bf16(__builtin_bit_cast(bf16x8, a), __builtin_bit_cast(bf16x8, b), c, 0, 0, 0); }
__device__ __forceinline__ float ex2(float x) { return __builtin_amdgcn_exp2f(x); }
__device__ __forceinline__ float asf(u32 x) { return __builtin_bit_cast(float, x); }
__device__ __forceinline__ u32 asu(float x) { return __builtin_bit_cast(u32, x); }
template <int CTRL> __device__ __forceinline__ float dpp(float x) { return asf((u32)__builtin_amdgcn_mov_dpp((int)asu(x), CTRL, 0xf, 0xf, true)); }
__device__ __forceinline__ float x16max(float x) { auto s = __builtin_amdgcn_permlane16_swap(asu(x), asu(x), false, false); return fmaxf(asf(s[0]), asf(s[1])); }
__device__ __forceinline__ float x32max(float x) { auto s = __builtin_amdgcn_permlane32_swap(asu(x), asu(x), false, false); return fmaxf(asf(s[0]), asf(s[1])); }
__device__ __forceinline__ float x16sum(float x) { auto s = __builtin_amdgcn_permlane16_swap(asu(x), asu(x), false, false); return asf(s[0]) + asf(s[1]); }
__device__ __forceinline__ float x32sum(float x) { auto s = __builtin_amdgcn_permlane32_swap(asu(x), asu(x), false, false); return asf(s[0]) + asf(s[1]); }
__device__ __forceinline__ float row16max(float x) { x = fmaxf(x, dpp<0x128>(x)); x = fmaxf(x, dpp<0x124>(x)); x = fmaxf(x, dpp<0x122>(x)); return fmaxf(x, dpp<0x121>(x)); }
__device__ __forceinline__ float row16sum(float x) { x += dpp<0x128>(x); x += dpp<0x124>(x); x += dpp<0x122>(x); return x + dpp<0x121>(x); }

#define BAR() asm volatile("s_waitcnt lgkmcnt(0)\n\ts_barrier" ::: "memory")
#define VMWAIT(n) asm volatile("s_waitcnt vmcnt(" #n ")" ::: "memory")

template <bool PACED, bool NT, bool WAR = true>
__device__ __forceinline__ void dma4(u64 base, u32 o0, u32 o1, u32 o2, u32 o3, u32 lds) {
  if (WAR) asm volatile("s_waitcnt lgkmcnt(0)" ::: "memory");
#define DMA_BODY(W, P) \
  asm volatile("s_mov_b32 m0, %5\n\ts_nop 0\n\t" W "global_load_lds_dwordx4 %0, %4" P "\n\t" W "global_load_lds_dwordx4 %1, %4 offset:1024" P "\n\t" \
               W "global_load_lds_dwordx4 %2, %4 offset:2048" P "\n\t" W "global_load_lds_dwordx4 %3, %4 offset:3072" P \
               :: "v"(o0), "v"(o1), "v"(o2), "v"(o3), "s"(base), "s"(lds) : "memory")
  if (PACED && NT) DMA_BODY("s_waitcnt vmcnt(3)\n\t", " nt");
  else if (PACED) DMA_BODY("s_waitcnt vmcnt(3)\n\t", "");
  else if (NT) DMA_BODY("", " nt");
  else DMA_BODY("", "");
#undef DMA_BODY
}

struct Lane { u32 m0, m1, m2, m3; };
__device__ __forceinline__ f32x4 gemv4(const v4u (&W)[4], v2u xs, const Lane& L, f32x4 acc) {
#pragma unroll
  for (int i = 0; i < 4; ++i) {
    const u32 src = i < 2 ? xs.x : xs.y;
    const u32 pp = __builtin_amdgcn_perm(src, src, (i & 1) ? 0x03020302u : 0x01000100u);
    acc = mfma16(W[i], v4u{pp & L.m0, pp & L.m1, pp & L.m2, pp & L.m3}, acc);
  }
  return acc;
}
__device__ __forceinline__ void ldsW(v4u (&W)[4], LAS char* sm, u32 unit, int g, int c) {
#pragma unroll
  for (int i = 0; i < 4; ++i) W[i] = *(LAS v4u*)(sm + unit + (4 * g + i) * 256 + c * 16);
}
__device__ __forceinline__ float reduce8(LAS char* sm, int t) {
  float s = 0.f;
#pragma unroll
  for (int w = 0; w < 8; ++w) s += *(LAS float*)(sm + L_PART + w * 1024 + t * 4);
  return s;
}
__device__ __forceinline__ void st_gran(char* ws, u32 off, u32 val, u32 tag) { __hip_atomic_store((GAS u64*)(ws + off), ((u64)tag << 32) | val, RLX_AGENT); }
__device__ __forceinline__ u64 ld_gran(char* ws, u32 off) { return __hip_atomic_load((GAS u64*)(ws + off), RLX_AGENT); }
__device__ __forceinline__ v4u ld2(__amdgpu_buffer_rsrc_t rs, u32 off) { return __builtin_amdgcn_raw_buffer_load_b128(rs, off, 0, 16); }
__device__ __forceinline__ bool spin(u32& n) { __builtin_amdgcn_s_sleep(2); asm volatile("" ::: "memory"); return ++n > SPIN_CAP; }

__global__ void __launch_bounds__(576, 1)
fused_decode(const u16* __restrict__ Ax, const u16* __restrict__ Ak, const u16* __restrict__ Av, const u16* __restrict__ Awq, const u16* __restrict__ Awo,
             const u16* __restrict__ Bx, const u16* __restrict__ Bc, const u16* __restrict__ Bwq, const u16* __restrict__ Bwuk, const u16* __restrict__ Bwuv,
             const u16* __restrict__ Bwo, u16* __restrict__ out, char* ws) {
  __shared__ __attribute__((aligned(16))) char smem[163840];
  LAS char* sm = (LAS char*)smem;
  const u32 sm0 = (u32)(size_t)sm;
  u64 st[16];
#pragma unroll
  for (int k = 0; k < 16; ++k) st[k] = 0;
  STAMP(0);
  const int tid = threadIdx.x, lane = tid & 63, w = __builtin_amdgcn_readfirstlane(tid >> 6), g = lane >> 4, c = lane & 15;
  const int bid = blockIdx.x, xcd = bid & 7, role = bid >> 7, jj = (bid >> 3) & 15, qt = jj & 3, h = 4 * xcd + (jj >> 2);
  u64 pAx = (u64)Ax, pAk = (u64)Ak, pAv = (u64)Av, pAwq = (u64)Awq, pAwo = (u64)Awo, pBx = (u64)Bx, pBc = (u64)Bc, pBwq = (u64)Bwq, pBwuk = (u64)Bwuk, pBwuv = (u64)Bwuv, pBwo = (u64)Bwo, pOut = (u64)out;
  asm volatile("" : "+s"(pAx), "+s"(pAk), "+s"(pAv), "+s"(pAwq), "+s"(pAwo), "+s"(pBx), "+s"(pBc), "+s"(pBwq), "+s"(pBwuk), "+s"(pBwuv), "+s"(pBwo), "+s"(pOut));
  u32 tag = *(const __attribute__((address_space(4))) u32*)(u64)ws;
  const __amdgpu_buffer_rsrc_t rs = __builtin_amdgcn_make_buffer_rsrc(ws, 0, (int)WS_USED, 0x00020000);
  Lane L;
  { const int j0 = c & 7; const u32 hm = (j0 & 1) ? 0xFFFF0000u : 0x0000FFFFu;
    L.m0 = (j0 >> 1) == 0 ? hm : 0u; L.m1 = (j0 >> 1) == 1 ? hm : 0u; L.m2 = (j0 >> 1) == 2 ? hm : 0u; L.m3 = (j0 >> 1) == 3 ? hm : 0u; }
  const int tb = (tid >> 5) & 1, tcol = 32 * ((tid >> 6) & 3) + 8 * (tid & 3) + ((tid >> 2) & 7);
  v4u FW[4];

  if (role == 0) {
    const int b = jj >> 3, s = jj & 7;
    if (w == 8) {
      asm volatile("" : "+s"(tag)); tag += 1u;
      BAR(); BAR(); BAR();
      u32 n = 0;
      const u32 so = QA_OFF + (u32)(((4 * xcd + (c >> 2)) * 4 + (c & 3)) * 256 + b * 128) * 8;
      for (;;) { const u64 x = ld_gran(ws, so); if (__all((u32)(x >> 32) == tag) || spin(n)) break; }
      STAMP(11);
      for (;;) {
        v4u q[16]; bool ok = true;
#pragma unroll
        for (int k = 0; k < 16; ++k) q[k] = ld2(rs, QA_OFF + (u32)(((4 * xcd * 4 + k) * 256 + b * 128 + 2 * lane) * 8));
#pragma unroll
        for (int k = 0; k < 16; ++k) ok &= (q[k].y == tag) & (q[k].w == tag);
#pragma unroll
        for (int hh = 0; hh < 4; ++hh) {
          const float a0 = (asf(q[4 * hh].x) + asf(q[4 * hh + 1].x)) + (asf(q[4 * hh + 2].x) + asf(q[4 * hh + 3].x));
          const float a1 = (asf(q[4 * hh].z) + asf(q[4 * hh + 1].z)) + (asf(q[4 * hh + 2].z) + asf(q[4 * hh + 3].z));
          *(LAS u32*)(sm + L_QIMG + hh * 256 + lane * 4) = cvtpk(a0, a1);
        }
        if (__all(ok) || spin(n)) break;
      }
      STAMP(12);
      BAR();
      return;
    }
    const u32 ring = sm0 + w * 16384, ringo = w * 16384;
    {
      const int q4 = (lane >> 2) & 3, xb = (lane >> 1) & 1;
      const u64 src = (lane < 16 ? pBx : pAx) + (u32)(xb * 512 + 128 * (lane < 16 ? q4 : qt) + 16 * w + 8 * (lane & 1)) * 2;
      asm volatile("s_mov_b32 m0, %1\n\ts_nop 4\n\tglobal_load_lds_dwordx4 %0, off" :: "v"(src), "s"(sm0 + L_XS + w * 1024) : "memory");
    }
    const u32 wo0 = g * 8192 + c * 16, wo1 = wo0 + 31744, wo2 = wo0 + 2 * 31744, wo3 = wo0 + 3 * 31744;
    const u32 sw = (c ^ g) * 16;
    const u64 wq_b = pBwq + (u32)(16 * w * 8192 + h * 256);
    dma4<false, false, false>(wq_b + (u32)(qt * (128 * 8192)), wo0, wo1, wo2, wo3, ring + 4096);
    dma4<false, true, false>(pBwuk + (u32)((128 * qt + 16 * w) * 8192 + h * 256), g * 8192 + sw, g * 8192 + (sw ^ 64) + 31744, g * 8192 + (sw ^ 128) + 2 * 31744, g * 8192 + (sw ^ 192) + 3 * 31744, ring + 3 * 4096);
    dma4<false, false, false>(wq_b + (u32)(((qt + 1) & 3) * (128 * 8192)), wo0, wo1, wo2, wo3, ring + 2 * 4096);
    dma4<false, false, false>(wq_b + (u32)(((qt + 2) & 3) * (128 * 8192)), wo0, wo1, wo2, wo3, ring);
    const u32 xso = L_XS + w * 1024 + (c >> 3) * 32 + g * 8;
    const u32 ko0 = g * 256 + sw, ko1 = g * 256 + (sw ^ 64), ko2 = g * 256 + (sw ^ 128), ko3 = g * 256 + (sw ^ 192), vo = lane * 16;
    const u32 kv_off = (u32)((b * 8 + xcd) * SK + s * (128 * TA) + w * (16 * TA)) * 256;
    const u64 kb = pAk + kv_off, vb = pAv + kv_off;
    u64 fb = pAwo + (u32)((128 * h + 16 * w) * 1024 + qt * 256);
    asm volatile("" : "+s"(fb));
    f32x4 acc = {0.f, 0.f, 0.f, 0.f};
    v4u W[4];
    asm volatile("" : "+s"(tag)); tag += 1u;
    VMWAIT(12);
    ldsW(W, sm, ringo + 4096, g, c); acc = gemv4(W, *(LAS v2u*)(sm + xso + qt * 64), L, acc);
    dma4<false, false>(wq_b + (u32)(((qt + 3) & 3) * (128 * 8192)), wo0, wo1, wo2, wo3, ring + 4096);
    VMWAIT(8);
    ldsW(W, sm, ringo + 2 * 4096, g, c); acc = gemv4(W, *(LAS v2u*)(sm + xso + ((qt + 1) & 3) * 64), L, acc);
    dma4<false, true>(pAwq + (u32)((128 * qt + 16 * w) * 8192 + h * 256), wo0, wo1, wo2, wo3, ring + 2 * 4096);
    VMWAIT(8);
    ldsW(W, sm, ringo, g, c); acc = gemv4(W, *(LAS v2u*)(sm + xso + ((qt + 2) & 3) * 64), L, acc);
    dma4<false, true>(kb, ko0, ko1, ko2, ko3, ring);
    VMWAIT(8);
    ldsW(W, sm, ringo + 4096, g, c); acc = gemv4(W, *(LAS v2u*)(sm + xso + ((qt + 3) & 3) * 64), L, acc);
    *(LAS f32x4*)(sm + L_PART + w * 1024 + lane * 16) = acc;
    STAMP(1);
    BAR();
    if (tid < 256) *(LAS __bf16*)(sm + L_XBUF + (tb * 128 + tcol) * 2) = (__bf16)reduce8(sm, tid);
    BAR();
    {
      f32x4 qa = {0.f, 0.f, 0.f, 0.f};
#pragma unroll
      for (int k = 0; k < 4; ++k)
        qa = mfma16(*(LAS v4u*)(sm + ringo + 3 * 4096 + c * 256 + ((4 * k + g) ^ c) * 16), *(LAS v4u*)(sm + L_XBUF + (c & 1) * 256 + k * 64 + g * 16), qa);
      if (c < 2) {
        const int c0 = 128 * qt + 16 * w + 4 * g;
        const u32 G = ((c0 >> 3) * 32 + h) * 4 + ((c0 & 7) >> 1);
        __builtin_amdgcn_raw_buffer_store_b128(v4u{cvtpk(qa.x, qa.y), tag, cvtpk(qa.z, qa.w), tag}, rs, QG_OFF + (u32)c * 65536 + G * 8, 0, 16);
      }
    }
    STAMP(2);
    VMWAIT(5);
    ldsW(W, sm, ringo + 2 * 4096, g, c);
    acc = gemv4(W, *(LAS v2u*)(sm + xso + 256), L, f32x4{0.f, 0.f, 0.f, 0.f});
    *(LAS f32x4*)(sm + L_PART + w * 1024 + lane * 16) = acc;
    BAR();
    if (tid < 256) st_gran(ws, QA_OFF + (u32)((h * 4 + qt) * 256 + tb * 128 + tcol) * 8, asu(reduce8(sm, tid)), tag);
    STAMP(3);
    dma4<true, true>(vb, vo, vo, vo, vo, ring + 4096);
    dma4<true, true>(kb + 4096, ko0, ko1, ko2, ko3, ring + 2 * 4096);
    dma4<true, true>(vb + 4096, vo, vo, vo, vo, ring + 3 * 4096);
    STAMP(4);
    BAR();
    STAMP(5);
    v4u qf[4];
#pragma unroll
    for (int k = 0; k < 4; ++k) qf[k] = *(LAS v4u*)(sm + L_QIMG + (c & 3) * 256 + k * 64 + g * 16);
    const u32 pm0 = (c >> 2) == 0 ? 0x0000FFFFu : (c >> 2) == 1 ? 0xFFFF0000u : 0u, pm1 = (c >> 2) == 2 ? 0x0000FFFFu : (c >> 2) == 3 ? 0xFFFF0000u : 0u;
    float m = -INFINITY, l = 0.f;
    f32x4 o0 = {0.f, 0.f, 0.f, 0.f}, o1 = {0.f, 0.f, 0.f, 0.f};
    const u32 kr = ringo + c * 256, vr = ringo + 4096 + g * 1024 + c * 16;
#pragma unroll 1
    for (int t = 0; t < TA; ++t) {
      const u32 so = (t & 1) * 8192;
      f32x4 sa = {0.f, 0.f, 0.f, 0.f}, sb = {0.f, 0.f, 0.f, 0.f};
      sa = mfma16(*(LAS v4u*)(sm + kr + so + ((0 + g) ^ c) * 16), qf[0], sa);
      sb = mfma16(*(LAS v4u*)(sm + kr + so + ((4 + g) ^ c) * 16), qf[1], sb);
      sa = mfma16(*(LAS v4u*)(sm + kr + so + ((8 + g) ^ c) * 16), qf[2], sa);
      sb = mfma16(*(LAS v4u*)(sm + kr + so + ((12 + g) ^ c) * 16), qf[3], sb);
      v4u V[4];
#pragma unroll
      for (int i = 0; i < 4; ++i) V[i] = *(LAS v4u*)(sm + vr + so + i * 256);
      sa += sb;
      const float mn = fmaxf(m, x32max(x16max(fmaxf(fmaxf(sa.x, sa.y), fmaxf(sa.z, sa.w)))));
      const float al = ex2((m - mn) * C2), mc = mn * C2;
      m = mn;
      float p[4] = {ex2(fmaf(sa.x, C2, -mc)), ex2(fmaf(sa.y, C2, -mc)), ex2(fmaf(sa.z, C2, -mc)), ex2(fmaf(sa.w, C2, -mc))};
      l = fmaf(l, al, (p[0] + p[1]) + (p[2] + p[3]));
      o0 *= al; o1 *= al;
#pragma unroll
      for (int i = 0; i < 4; ++i) {
        const u32 pp = cvtpk(p[i], p[i]);
        const v2u y = {pp & pm0, pp & pm1};
        o0 = mfma16h(v2u{V[i].x, V[i].y}, y, o0);
        o1 = mfma16h(v2u{V[i].z, V[i].w}, y, o1);
      }
      if (t + 2 < TA) {
        dma4<true, true>(kb + (u32)((t + 2) * 4096), ko0, ko1, ko2, ko3, ring + so);
        dma4<true, true>(vb + (u32)((t + 2) * 4096), vo, vo, vo, vo, ring + so + 4096);
      } else if (t + 2 == TA) {
        const u32 f0 = g * 1024 + c * 16;
        dma4<true, true>(fb, f0, f0 + 3072, f0 + 2 * 3072, f0 + 3 * 3072, ring + so);
      }
    }
    STAMP(6);
    l = x32sum(x16sum(l));
    *(LAS f32x4*)(sm + L_MRG + w * 2048 + lane * 16) = o0;
    *(LAS f32x4*)(sm + L_MRG + w * 2048 + 1024 + lane * 16) = o1;
    if (lane < 4) { *(LAS float*)(sm + L_MST + lane * 32 + w * 4) = m; *(LAS float*)(sm + L_MST + 128 + lane * 32 + w * 4) = l; }
    BAR();
    if (tid < 256) {
      const int hh = (tid >> 1) & 3;
      const f32x4 ma = *(LAS f32x4*)(sm + L_MST + hh * 32), mb = *(LAS f32x4*)(sm + L_MST + hh * 32 + 16);
      const f32x4 la = *(LAS f32x4*)(sm + L_MST + 128 + hh * 32), lb = *(LAS f32x4*)(sm + L_MST + 128 + hh * 32 + 16);
      const float M = fmaxf(fmaxf(fmaxf(ma.x, ma.y), fmaxf(ma.z, ma.w)), fmaxf(fmaxf(mb.x, mb.y), fmaxf(mb.z, mb.w)));
      const float f[8] = {ex2((ma.x - M) * C2), ex2((ma.y - M) * C2), ex2((ma.z - M) * C2), ex2((ma.w - M) * C2),
                          ex2((mb.x - M) * C2), ex2((mb.y - M) * C2), ex2((mb.z - M) * C2), ex2((mb.w - M) * C2)};
      const float Ls = ((f[0] * la.x + f[1] * la.y) + (f[2] * la.z + f[3] * la.w)) + ((f[4] * lb.x + f[5] * lb.y) + (f[6] * lb.z + f[7] * lb.w));
      f32x2 a = {0.f, 0.f};
#pragma unroll
      for (int ww = 0; ww < 8; ++ww) a += f[ww] * *(LAS f32x2*)(sm + L_MRG + ww * 2048 + tid * 8);
      const float inv = 1.f / Ls;
      const u32 row = (u32)((4 * xcd + hh) * 2 + b) * 16 + s;
      const u32 idx = 32 * (tid >> 7) + ((tid >> 3) & 15) * 2 + (tid & 1);
      st_gran(ws, OA_OFF + (row * 64 + idx) * 8, cvtpk(a.x * inv, a.y * inv), tag);
      if ((tid & ~6) == 0) st_gran(ws, LA_OFF + row * 8, asu(fmaf(M, C2, __builtin_amdgcn_logf(Ls))), tag);
    }
    STAMP(7);
    {
      f32x4 oc = {0.f, 0.f, 0.f, 0.f};
      if (4 * w < 2 * NS) {
        const int r = 4 * w + g, rb = r / NS, rsp = r % NS;
        const u32 ro = OA_OFF + (u32)(((h * 2 + rb) * 16 + rsp) * 64 + 4 * c) * 8, lo = LA_OFF + (u32)((h * 2 + (g & 1)) * 16 + (c < NS ? c : 0)) * 8;
        v4u x0, x1; u64 ls; u32 n = 0;
        for (;;) {
          x0 = ld2(rs, ro); x1 = ld2(rs, ro + 16); ls = ld_gran(ws, lo);
          const bool ok = (x0.y == tag) & (x0.w == tag) & (x1.y == tag) & (x1.w == tag) & ((u32)(ls >> 32) == tag);
          if (__all(ok) || spin(n)) break;
        }
        const float lse = c < NS ? asf((u32)ls) : -INFINITY;
        const float e = ex2(lse - row16max(lse));
        float wt = e / row16sum(e);
        wt = __shfl(wt, rb * 16 + rsp);
        const u32 pp = (c >> 3) == rb ? cvtpk(wt, wt) : 0u;
        oc = mfma16(v4u{x0.x, x0.z, x1.x, x1.z}, v4u{pp & L.m0, pp & L.m1, pp & L.m2, pp & L.m3}, oc);
      }
      *(LAS f32x4*)(sm + L_PART + w * 1024 + lane * 16) = oc;
    }
    STAMP(8);
    VMWAIT(0);
    ldsW(FW, sm, ringo + (TA & 1) * 8192, g, c);
    BAR();
    if (tid < 256) {
      const int id = tcol >> 1, d = 8 * (4 * ((id >> 3) & 3) + 2 * (id & 1) + (tcol & 1)) + 4 * (id >> 5) + ((id >> 1) & 3);
      *(LAS __bf16*)(sm + L_XBUF + (tb * 128 + d) * 2) = (__bf16)reduce8(sm, tid);
    }
  } else {
    if (w == 8) return;
    const int b = xcd >> 2, s = (xcd & 3) * 16 + jj, h2 = lane >> 5, hd = lane & 31;
    {
      const u64 cb = pBc + (u32)((b * SK + s * 128 + 16 * w) * 1024);
      const u32 lo = lane * 16;
      asm volatile("s_nop 4" ::: "memory");
#pragma unroll
      for (int k = 0; k < 4; ++k) dma4<false, true, false>(cb + k * 4096, lo ^ (k * 16), lo ^ ((4 + k) * 16), lo ^ ((8 + k) * 16), lo ^ ((12 + k) * 16), sm0 + w * 16384 + k * 4096);
    }
    asm volatile("" : "+s"(tag)); tag += 1u;
    {
      u32 n = 0;
      for (;;) {
        v4u q[8]; bool ok = true;
#pragma unroll
        for (int k = 0; k < 8; ++k) q[k] = ld2(rs, QG_OFF + (u32)b * 65536 + (u32)(k * 512 + tid) * 16);
#pragma unroll
        for (int k = 0; k < 8; ++k) { ok &= (q[k].y == tag) & (q[k].w == tag); *(LAS v2u*)(sm + L_QB + (k * 512 + tid) * 8) = v2u{q[k].x, q[k].z}; }
        if (__all(ok) || spin(n)) break;
      }
    }
    VMWAIT(0);
    STAMP(1);
    v4u UW[4];
#pragma unroll
    for (int i = 0; i < 4; ++i) UW[i] = __builtin_nontemporal_load((const GAS v4u*)(pBwuv + (u32)((128 * qt + 16 * w + 4 * g + i) * 8192 + h * 256 + c * 16)));
#pragma unroll
    for (int i = 0; i < 4; ++i) FW[i] = __builtin_nontemporal_load((const GAS v4u*)(pBwo + (u32)((128 * h + 16 * w + 4 * g + i) * 1024 + qt * 256 + c * 16)));
    BAR();
    float sv[8];
    {
      const int kt = w & 3, ch = w >> 2;
      f32x16 sc;
#pragma unroll
      for (int k = 0; k < 16; ++k) sc[k] = 0.f;
      const u32 f = ((hd & 3) << 2) | ((hd >> 2) & 3);
      const u32 ca = (32 * kt + hd) * 1024 + ch * 512, cx = (h2 ^ f) * 16, qa = L_QB + ch * 16384 + lane * 16;
#pragma unroll
      for (int k = 0; k < 16; ++k)
        sc = mfma32(*(LAS v4u*)(sm + ca + (k >> 3) * 256 + (cx ^ ((k & 7) * 32))), *(LAS v4u*)(sm + qa + k * 1024), sc);
      STAMP(2);
      BAR();
      f32x4 s0, s1;
#pragma unroll
      for (int k = 0; k < 4; ++k) { s0[k] = ch ? sc[k] : sc[8 + k]; s1[k] = ch ? sc[4 + k] : sc[12 + k]; }
      *(LAS f32x4*)(sm + L_SX + w * 2048 + lane * 16) = s0;
      *(LAS f32x4*)(sm + L_SX + w * 2048 + 1024 + lane * 16) = s1;
      BAR();
      s0 = *(LAS f32x4*)(sm + L_SX + (w ^ 4) * 2048 + lane * 16);
      s1 = *(LAS f32x4*)(sm + L_SX + (w ^ 4) * 2048 + 1024 + lane * 16);
#pragma unroll
      for (int k = 0; k < 4; ++k) { sv[k] = s0[k] + (ch ? sc[8 + k] : sc[k]); sv[4 + k] = s1[k] + (ch ? sc[12 + k] : sc[4 + k]); }
      const float mw = x32max(fmaxf(fmaxf(fmaxf(sv[0], sv[1]), fmaxf(sv[2], sv[3])), fmaxf(fmaxf(sv[4], sv[5]), fmaxf(sv[6], sv[7]))));
      if (lane < 32) *(LAS float*)(sm + L_BM + hd * 32 + w * 4) = mw;
    }
    BAR();
    float mc;
    {
      const f32x4 ma = *(LAS f32x4*)(sm + L_BM + hd * 32), mb = *(LAS f32x4*)(sm + L_BM + hd * 32 + 16);
      mc = C2 * fmaxf(fmaxf(fmaxf(ma.x, ma.y), fmaxf(ma.z, ma.w)), fmaxf(fmaxf(mb.x, mb.y), fmaxf(mb.z, mb.w)));
#pragma unroll
      for (int k = 0; k < 8; ++k) sv[k] = ex2(fmaf(sv[k], C2, -mc));
      const float ls = x32sum(((sv[0] + sv[1]) + (sv[2] + sv[3])) + ((sv[4] + sv[5]) + (sv[6] + sv[7])));
      if (lane < 32) *(LAS float*)(sm + L_BL + hd * 32 + w * 4) = ls;
      *(LAS v4u*)(sm + L_PF + (2 * (w & 3) + (w >> 2)) * 1024 + lane * 16) = v4u{cvtpk(sv[0], sv[1]), cvtpk(sv[2], sv[3]), cvtpk(sv[4], sv[5]), cvtpk(sv[6], sv[7])};
    }
    BAR();
    f32x16 oa, ob;
#pragma unroll
    for (int k = 0; k < 16; ++k) { oa[k] = 0.f; ob[k] = 0.f; }
    {
      const int q = (lane >> 2) & 3, p = lane & 3, blk = (lane >> 4) & 1;
      const u32 base = (4 * h2 + q) * 1024 + (w >> 1) * 256 + 8 * (p & 1), lo4 = 8 * (w & 1) + 2 * blk + (p >> 1);
      const u32 a00 = base + 16 * (lo4 ^ ((q << 2) | h2)), a01 = base + 16 * ((lo4 + 4) ^ ((q << 2) | h2));
      const u32 a10 = base + 8192 + 16 * (lo4 ^ ((q << 2) | (2 + h2))), a11 = base + 8192 + 16 * ((lo4 + 4) ^ ((q << 2) | (2 + h2)));
#pragma unroll
      for (int ks = 0; ks < 8; ++ks) {
        const v4u pb = *(LAS v4u*)(sm + L_PF + ks * 1024 + lane * 16);
        const v2u t00 = __builtin_bit_cast(v2u, __builtin_amdgcn_ds_read_tr16_b64_v4i16((LAS s16x4*)(sm + a00 + ks * 16384)));
        const v2u t10 = __builtin_bit_cast(v2u, __builtin_amdgcn_ds_read_tr16_b64_v4i16((LAS s16x4*)(sm + a10 + ks * 16384)));
        const v2u t01 = __builtin_bit_cast(v2u, __builtin_amdgcn_ds_read_tr16_b64_v4i16((LAS s16x4*)(sm + a01 + ks * 16384)));
        const v2u t11 = __builtin_bit_cast(v2u, __builtin_amdgcn_ds_read_tr16_b64_v4i16((LAS s16x4*)(sm + a11 + ks * 16384)));
        oa = mfma32(v4u{t00.x, t00.y, t10.x, t10.y}, pb, oa);
        ob = mfma32(v4u{t01.x, t01.y, t11.x, t11.y}, pb, ob);
      }
    }
    float lse;
    {
      const f32x4 la = *(LAS f32x4*)(sm + L_BL + hd * 32), lb = *(LAS f32x4*)(sm + L_BL + hd * 32 + 16);
      const float Ls = ((la.x + la.y) + (la.z + la.w)) + ((lb.x + lb.y) + (lb.z + lb.w));
      lse = mc + __builtin_amdgcn_logf(Ls);
      const float inv = 1.f / Ls;
      STAMP(3);
      BAR();
#pragma unroll
      for (int k = 0; k < 4; ++k) {
        *(LAS v2u*)(sm + L_OST + hd * 1024 + ((8 * w + k) ^ (hd & 15)) * 16 + 8 * h2) = v2u{cvtpk(oa[4 * k] * inv, oa[4 * k + 1] * inv), cvtpk(oa[4 * k + 2] * inv, oa[4 * k + 3] * inv)};
        *(LAS v2u*)(sm + L_OST + hd * 1024 + ((8 * w + 4 + k) ^ (hd & 15)) * 16 + 8 * h2) = v2u{cvtpk(ob[4 * k] * inv, ob[4 * k + 1] * inv), cvtpk(ob[4 * k + 2] * inv, ob[4 * k + 3] * inv)};
      }
    }
    BAR();
#pragma unroll
    for (int i = 0; i < 4; ++i) {
      const int hh = 4 * w + i;
      const v4u v = *(LAS v4u*)(sm + L_OST + hh * 1024 + (lane ^ (hh & 15)) * 16);
      __builtin_amdgcn_raw_buffer_store_b128(v, rs, PB_OFF + (u32)(((hh * 4 + g) * 2 + b) * 64 + s) * 256 + c * 16, 0, 16);
    }
    VMWAIT(0);
    BAR();
    if (tid < 32) st_gran(ws, LB_OFF + (u32)((tid * 2 + b) * 64 + s) * 8, asu(lse), tag);
    STAMP(4);
    {
      u64 l0, l1; u32 n = 0;
      for (;;) {
        l0 = ld_gran(ws, LB_OFF + (u32)(h * 128 + lane) * 8); l1 = ld_gran(ws, LB_OFF + (u32)(h * 128 + 64 + lane) * 8);
        if (__all(((u32)(l0 >> 32) == tag) & ((u32)(l1 >> 32) == tag)) || spin(n)) break;
      }
      asm volatile("" ::: "memory");
      STAMP(5);
      v4u X[4];
#pragma unroll
      for (int i = 0; i < 4; ++i) X[i] = ld2(rs, PB_OFF + (u32)((h * 4 + qt) * 128 + 16 * w + 4 * g + i) * 256 + c * 16);
      const float ls = asf((u32)(w < 4 ? l0 : l1));
      const float e = ex2(ls - x32max(x16max(row16max(ls))));
      const float wt = e / x32sum(x16sum(row16sum(e)));
      const int sl = (16 * w + 4 * g) & 63;
      const bool mine = (c >> 3) == (w >> 2);
      const float w0 = __shfl(wt, sl), w1 = __shfl(wt, sl + 1), w2 = __shfl(wt, sl + 2), w3 = __shfl(wt, sl + 3);
      const v2u xs = mine ? v2u{cvtpk(w0, w1), cvtpk(w2, w3)} : v2u{0u, 0u};
      *(LAS f32x4*)(sm + L_PART + w * 1024 + lane * 16) = gemv4(X, xs, L, f32x4{0.f, 0.f, 0.f, 0.f});
    }
    BAR();
    if (tid < 256) *(LAS __bf16*)(sm + L_XBUF + (tb * 128 + tcol) * 2) = (__bf16)reduce8(sm, tid);
    BAR();
    *(LAS f32x4*)(sm + L_PART + w * 1024 + lane * 16) = gemv4(UW, *(LAS v2u*)(sm + L_XBUF + (c >> 3) * 256 + w * 32 + g * 8), L, f32x4{0.f, 0.f, 0.f, 0.f});
    BAR();
    if (tid < 256) {
      st_gran(ws, CX_OFF + (u32)((h * 4 + qt) * 256 + tid) * 8, asu(reduce8(sm, tid)), tag);
      STAMP(6);
      u64 x0, x1, x2, x3; u32 n = 0;
      for (;;) {
        x0 = ld_gran(ws, CX_OFF + (u32)(h * 1024 + tid) * 8); x1 = ld_gran(ws, CX_OFF + (u32)(h * 1024 + 256 + tid) * 8);
        x2 = ld_gran(ws, CX_OFF + (u32)(h * 1024 + 512 + tid) * 8); x3 = ld_gran(ws, CX_OFF + (u32)(h * 1024 + 768 + tid) * 8);
        if (__all(((u32)(x0 >> 32) == tag) & ((u32)(x1 >> 32) == tag) & ((u32)(x2 >> 32) == tag) & ((u32)(x3 >> 32) == tag)) || spin(n)) break;
      }
      *(LAS __bf16*)(sm + L_XBUF + 512 + (tb * 128 + tcol) * 2) = (__bf16)((asf((u32)x0) + asf((u32)x1)) + (asf((u32)x2) + asf((u32)x3)));
      STAMP(7);
    }
  }
  BAR();
  *(LAS f32x4*)(sm + L_PART + w * 1024 + lane * 16) = gemv4(FW, *(LAS v2u*)(sm + L_XBUF + role * 512 + (c >> 3) * 256 + w * 32 + g * 8), L, f32x4{0.f, 0.f, 0.f, 0.f});
  BAR();
  if (tid < 256) st_gran(ws, YG_OFF + (u32)((((role * 2 + tb) * 64 + 16 * qt + (tcol >> 3)) * 32 + h) * 8 + (tcol & 7)) * 8, asu(reduce8(sm, tid)), tag);
  STAMP(9);
  if (w == 7) {
    const int idx = bid & 127;
    const u32 yo = YG_OFF + (u32)((role * 128 + idx) * 256 + lane * 4) * 8;
    v4u y0, y1; u32 n = 0;
    for (;;) {
      y0 = ld2(rs, yo); y1 = ld2(rs, yo + 16);
      if (__all((y0.y == tag) & (y0.w == tag) & (y1.y == tag) & (y1.w == tag)) || spin(n)) break;
    }
    float v[4] = {asf(y0.x), asf(y0.z), asf(y1.x), asf(y1.z)};
#pragma unroll
    for (int k = 0; k < 4; ++k) { float x = v[k]; x += dpp<0x128>(x); x += dpp<0x124>(x); x += dpp<0x122>(x); v[k] = x32sum(x16sum(x)); }
    if (lane < 2) *(GAS v2u*)(pOut + (u32)(((idx >> 6) * 1024 + role * 512 + (idx & 63) * 8 + lane * 4) * 2)) = v2u{cvtpk(v[0], v[1]), cvtpk(v[2], v[3])};
    if (bid == 255 && lane == 0) *(GAS u32*)ws = tag;
    STAMP(10);
    if (role == AMPR || AMPK == 10) { const u64 until = __builtin_amdgcn_s_memrealtime() + 20 * (st[AMPK] - st[0]); while (__builtin_amdgcn_s_memrealtime() < until) __builtin_amdgcn_s_sleep(4); }
  }
}

extern "C" void kernel_launch(void* const* d_in, const int* in_sizes, int n_in, void* d_out, int out_size, void* d_ws, size_t ws_size, hipStream_t stream) {
  fused_decode<<<256, 576, 0, stream>>>((const u16*)d_in[0], (const u16*)d_in[1], (const u16*)d_in[2], (const u16*)d_in[3], (const u16*)d_in[4],
                                        (const u16*)d_in[5], (const u16*)d_in[6], (const u16*)d_in[7], (const u16*)d_in[8], (const u16*)d_in[9],
                                        (const u16*)d_in[10], (u16*)d_out, (char*)d_ws);
}
```

```cpp
#include <hip/hip_runtime.h>
#include <stdint.h>

typedef unsigned short u16;
typedef unsigned u32;
typedef unsigned long long u64;
#define GAS __attribute__((address_space(1)))
#define LAS __attribute__((address_space(3)))
typedef u32 v4u __attribute__((ext_vector_type(4)));
typedef u32 v2u __attribute__((ext_vector_type(2)));
typedef float f32x2 __attribute__((ext_vector_type(2)));
typedef float f32x4 __attribute__((ext_vector_type(4)));
typedef float f32x16 __attribute__((ext_vector_type(16)));
typedef __bf16 bf16x8 __attribute__((ext_vector_type(8)));
typedef __bf16 bf16x2 __attribute__((ext_vector_type(2)));
typedef short s16x4 __attribute__((ext_vector_type(4)));
#define RLX_AGENT __ATOMIC_RELAXED, __HIP_MEMORY_SCOPE_AGENT
#define RLX_WG __ATOMIC_RELAXED, __HIP_MEMORY_SCOPE_WORKGROUP

#define TILES_A 48
constexpr int SK = 8192;
constexpr int NA = TILES_A, NX = 128 - 2 * TILES_A;
static_assert(8 * NA + 4 * NX == 512 && NX >= 16 && NA >= 16, "tile split");
constexpr float C2 = 0.08838834764831845f * 1.4426950408889634f;
constexpr u32 SPIN_CAP = 1u << 17;

constexpr u32 QG_OFF = 4096;
constexpr u32 QA_OFF = QG_OFF + 131072;
constexpr u32 OA_OFF = QA_OFF + 262144;
constexpr u32 LB_OFF = OA_OFF + 32 * 24 * 640;
constexpr u32 CX_OFF = LB_OFF + 32768;
constexpr u32 YG_OFF = CX_OFF + 131072;
constexpr u32 PB_OFF = YG_OFF + 524288;
constexpr u32 WS_USED = PB_OFF + 4194304;
#define STAMP(k)
#define STAMP_OUT()

constexpr u32 EXTRA = 131072;
constexpr u32 L_PART = EXTRA, L_XBUF = EXTRA + 8192, L_FLAG = EXTRA + 15040, L_MST = EXTRA + 15104, L_QIMG = EXTRA + 15360, L_MRG = EXTRA + 16384, L_XS = EXTRA + 16384;
constexpr u32 L_QB = EXTRA, L_PF = EXTRA, L_BM = EXTRA + 9216, L_BL = EXTRA + 10240, L_LSE = EXTRA + 11264, L_PART2 = EXTRA + 16384;
constexpr u32 F_Q = L_FLAG, F_LB = L_FLAG + 4, F_DRAIN = L_FLAG + 8, F_M = L_FLAG + 12, F_OUT = L_FLAG + 16, F_FIN = L_FLAG + 32, L_LBV = EXTRA + 11392;
#define CODE_KB 21
#define XPOS1 0
#define XPOS2 1
#define XPOS3 3
#define OPOLL 1
#define OSENT 1
#define OSLEEP 1
#define YPOLL 1
#define YSLEEP 1
#define OTHR 10
#define FLATE 1
#define FSLEEP 4
#define STAG 3
#define QA_T 13000
#define SENTINEL 1

__device__ __forceinline__ u32 cvtpk(float lo, float hi) { bf16x2 p = {(__bf16)lo, (__bf16)hi}; return __builtin_bit_cast(u32, p); }
__device__ __forceinline__ f32x4 mfma16(v4u a, v4u b, f32x4 c) { return __builtin_amdgcn_mfma_f32_16x16x32_bf16(__builtin_bit_cast(bf16x8, a), __builtin_bit_cast(bf16x8, b), c, 0, 0, 0); }
__device__ __forceinline__ f32x4 mfma16h(v2u a, v2u b, f32x4 c) { return __builtin_amdgcn_mfma_f32_16x16x16bf16_1k(__builtin_bit_cast(s16x4, a), __builtin_bit_cast(s16x4, b), c, 0, 0, 0); }
__device__ __forceinline__ f32x16 mfma32(v4u a, v4u b, f32x16 c) { return __builtin_amdgcn_mfma_f32_32x32x16_bf16(__builtin_bit_cast(bf16x8, a), __builtin_bit_cast(bf16x8, b), c, 0, 0, 0); }
__device__ __forceinline__ float ex2(float x) { return __builtin_amdgcn_exp2f(x); }
__device__ __forceinline__ float rcp(float x) { return __builtin_amdgcn_rcpf(x); }
__device__ __forceinline__ float asf(u32 x) { return __builtin_bit_cast(float, x); }
__device__ __forceinline__ u32 asu(float x) { return __builtin_bit_cast(u32, x); }
template <int CTRL> __device__ __forceinline__ float dpp(float x) { return asf((u32)__builtin_amdgcn_mov_dpp((int)asu(x), CTRL, 0xf, 0xf, true)); }
__device__ __forceinline__ float x16max(float x) { auto s = __builtin_amdgcn_permlane16_swap(asu(x), asu(x), false, false); return fmaxf(asf(s[0]), asf(s[1])); }
__device__ __forceinline__ float x32max(float x) { auto s = __builtin_amdgcn_permlane32_swap(asu(x), asu(x), false, false); return fmaxf(asf(s[0]), asf(s[1])); }
__device__ __forceinline__ float x16sum(float x) { auto s = __builtin_amdgcn_permlane16_swap(asu(x), asu(x), false, false); return asf(s[0]) + asf(s[1]); }
__device__ __forceinline__ float x32sum(float x) { auto s = __builtin_amdgcn_permlane32_swap(asu(x), asu(x), false, false); return asf(s[0]) + asf(s[1]); }
__device__ __forceinline__ float row16max(float x) { x = fmaxf(x, dpp<0x128>(x)); x = fmaxf(x, dpp<0x124>(x)); x = fmaxf(x, dpp<0x122>(x)); return fmaxf(x, dpp<0x121>(x)); }
__device__ __forceinline__ float row16sum(float x) { x += dpp<0x128>(x); x += dpp<0x124>(x); x += dpp<0x122>(x); return x + dpp<0x121>(x); }
__device__ __forceinline__ float max4(f32x4 a) { return fmaxf(fmaxf(a.x, a.y), fmaxf(a.z, a.w)); }
__device__ __forceinline__ float sum4(f32x4 a) { return (a.x + a.y) + (a.z + a.w); }
#define ZERO4 f32x4{0.f, 0.f, 0.f, 0.f}

#define BAR() asm volatile("s_waitcnt lgkmcnt(0)\n\ts_barrier" ::: "memory")
#define VMWAIT(n) asm volatile("s_waitcnt vmcnt(" #n ")" ::: "memory")

template <int PACED, bool NT, bool WAR = true>
__device__ __forceinline__ void dma4(u64 base, u32 o0, u32 o1, u32 o2, u32 o3, u32 lds) {
  if (WAR) asm volatile("s_waitcnt lgkmcnt(0)" ::: "memory");
#define DMA_BODY(W, P) \
  asm volatile("s_mov_b32 m0, %5\n\ts_nop 0\n\t" W "global_load_lds_dwordx4 %0, %4" P "\n\t" W "global_load_lds_dwordx4 %1, %4 offset:1024" P "\n\t" \
               W "global_load_lds_dwordx4 %2, %4 offset:2048" P "\n\t" W "global_load_lds_dwordx4 %3, %4 offset:3072" P \
               :: "v"(o0), "v"(o1), "v"(o2), "v"(o3), "s"(base), "s"(lds) : "memory")
  if (PACED == 8) DMA_BODY("s_waitcnt vmcnt(7)\n\t", " nt");
  else if (PACED && NT) DMA_BODY("s_waitcnt vmcnt(3)\n\t", " nt");
  else if (PACED) DMA_BODY("s_waitcnt vmcnt(3)\n\t", "");
  else if (NT) DMA_BODY("", " nt");
  else DMA_BODY("", "");
#undef DMA_BODY
}

struct Lane { u32 m0, m1, m2, m3; };
__device__ __forceinline__ Lane make_lane(int c) {
  const int j0 = c & 7; const u32 hm = (j0 & 1) ? 0xFFFF0000u : 0x0000FFFFu;
  return Lane{(j0 >> 1) == 0 ? hm : 0u, (j0 >> 1) == 1 ? hm : 0u, (j0 >> 1) == 2 ? hm : 0u, (j0 >> 1) == 3 ? hm : 0u};
}
__device__ __forceinline__ v4u diag(v2u xs, int i, const Lane& L) {
  const u32 src = i < 2 ? xs.x : xs.y;
  const u32 pp = __builtin_amdgcn_perm(src, src, (i & 1) ? 0x03020302u : 0x01000100u);
  return v4u{pp & L.m0, pp & L.m1, pp & L.m2, pp & L.m3};
}
__device__ __forceinline__ f32x4 gemv4(const v4u* W, v2u xs, const Lane& L, f32x4 acc) {
#pragma unroll
  for (int i = 0; i < 4; ++i) acc = mfma16(W[i], diag(xs, i, L), acc);
  return acc;
}
__device__ __forceinline__ void ldsW(v4u (&W)[4], LAS char* sm, u32 unit, int g, int c) {
#pragma unroll
  for (int i = 0; i < 4; ++i) W[i] = *(LAS v4u*)(sm + unit + (4 * g + i) * 256 + c * 16);
}
template <int STRIDE> __device__ __forceinline__ float reduce8(LAS char* sm, u32 off) {
  float v[8];
  asm volatile("" : "+v"(off));
#pragma unroll
  for (int w = 0; w < 8; ++w) v[w] = *(LAS float*)(sm + off + w * STRIDE);
  __builtin_amdgcn_sched_barrier(0);
  return ((v[0] + v[1]) + (v[2] + v[3])) + ((v[4] + v[5]) + (v[6] + v[7]));
}
__device__ __forceinline__ void st_gran(char* ws, u32 off, u32 val, u32 tag) { __hip_atomic_store((GAS u64*)(ws + off), ((u64)tag << 32) | val, RLX_AGENT); }
__device__ __forceinline__ u64 ld_gran(char* ws, u32 off) { return __hip_atomic_load((GAS u64*)(ws + off), RLX_AGENT); }
__device__ __forceinline__ v4u ld2(__amdgpu_buffer_rsrc_t rs, u32 off) { return __builtin_amdgcn_raw_buffer_load_b128(rs, off, 0, 16); }
__device__ __forceinline__ bool spin(u32& n) { asm volatile("s_sleep 1" ::: "memory"); return ++n > SPIN_CAP; }
__device__ __forceinline__ bool spinl(u32& n) { asm volatile("s_sleep 6" ::: "memory"); return ++n > SPIN_CAP; }
__device__ __forceinline__ void sleep_until(u64 t0, u32 cyc) {
  _Pragma("nounroll") for (u32 n = 0; n < 4096 && __builtin_amdgcn_s_memtime() - t0 < cyc; ++n) asm volatile("s_sleep 2" ::: "memory");
}
__device__ __forceinline__ void lds_add(LAS char* sm, u32 off, int lane) {
  asm volatile("s_waitcnt lgkmcnt(0)" ::: "memory");
  if (lane == 0) __hip_atomic_fetch_add((LAS u32*)(sm + off), 1u, RLX_WG);
}
__device__ __forceinline__ void lds_wait(LAS char* sm, u32 off, u32 want) {
  for (u32 n = 0; n < (SPIN_CAP << 4); ++n) {
    if ((u32)__builtin_amdgcn_readfirstlane(__hip_atomic_load((LAS u32*)(sm + off), RLX_WG)) >= want) break;
  }
  asm volatile("" ::: "memory");
}
__device__ __forceinline__ v4u gather_qa(__amdgpu_buffer_rsrc_t rs, int xcd, int b, int lane, u32 tag) {
  v4u r;
  _Pragma("nounroll") for (u32 n = 0;;) {
    v4u q[16]; bool ok = true;
#pragma unroll
    for (int k = 0; k < 16; ++k) q[k] = ld2(rs, QA_OFF + (u32)(((16 * xcd + k) * 256 + b * 128 + 2 * lane) * 8));
#pragma unroll
    for (int k = 0; k < 16; ++k) ok &= (q[k].y == tag) & (q[k].w == tag);
#pragma unroll
    for (int hh = 0; hh < 4; ++hh)
      r[hh] = cvtpk((asf(q[4 * hh].x) + asf(q[4 * hh + 1].x)) + (asf(q[4 * hh + 2].x) + asf(q[4 * hh + 3].x)),
                    (asf(q[4 * hh].z) + asf(q[4 * hh + 1].z)) + (asf(q[4 * hh + 2].z) + asf(q[4 * hh + 3].z)));
    if (__all(ok) || spinl(n)) break;
  }
  return r;
}
__device__ __forceinline__ void put_qa(LAS char* sm, v4u r, int lane) {
#pragma unroll
  for (int hh = 0; hh < 4; ++hh) *(LAS u32*)(sm + L_QIMG + hh * 256 + lane * 4) = r[hh];
  lds_add(sm, F_Q, lane);
}
__device__ __forceinline__ void final_reduce(LAS char* sm, u32 flag, int phase, char* ws, u64 pOut, int which, int idx, int lane, u32 tag) {
  const u32 yo = YG_OFF + (u32)((which * 128 + idx) * 256 + lane) * 8;
  const u32 oo = (u32)(((idx >> 6) * 1024 + which * 512 + (idx & 63) * 8 + (lane & 7)) * 2);
  if (phase >= YPOLL) return;
  _Pragma("nounroll") for (int k = 0; k < phase; ++k) asm volatile("s_sleep %0" :: "i"(STAG) : "memory");
  u64 y0, y1, y2, y3;
  _Pragma("nounroll") for (u32 n = 0;;) {
    y0 = ld_gran(ws, yo); y1 = ld_gran(ws, yo + 512); y2 = ld_gran(ws, yo + 1024); y3 = ld_gran(ws, yo + 1536);
    if (__all(((u32)(y0 >> 32) == tag) & ((u32)(y1 >> 32) == tag) & ((u32)(y2 >> 32) == tag) & ((u32)(y3 >> 32) == tag)) || ++n > SPIN_CAP) break;
    if (__builtin_amdgcn_readfirstlane(__hip_atomic_load((LAS u32*)(sm + flag), RLX_WG))) return;
    asm volatile("s_sleep %0" :: "i"(YSLEEP) : "memory");
  }
  float x = (asf((u32)y0) + asf((u32)y1)) + (asf((u32)y2) + asf((u32)y3));
  x += dpp<0x128>(x);
  x = x32sum(x16sum(x));
  if (lane < 8) *(GAS __bf16*)(pOut + oo) = (__bf16)x;
  if (lane == 0) __hip_atomic_store((LAS u32*)(sm + flag), 1u, RLX_WG);
}

__global__ void __launch_bounds__(576, 1)
fused_decode(const u16* __restrict__ Bwq, const u16* __restrict__ Bc, char* ws, const u16* __restrict__ Bx, const u16* __restrict__ Ax, const u16* __restrict__ Awq,
             const u16* __restrict__ Bwuk, const u16* __restrict__ Ak, const u16* __restrict__ Av, const u16* __restrict__ Awo, const u16* __restrict__ Bwuv,
             const u16* __restrict__ Bwo, u16* __restrict__ out) {
  __shared__ __attribute__((aligned(16))) char smem[163840];
  u64 pc0; asm volatile("s_getpc_b64 %0" : "=s"(pc0));
  LAS char* sm = (LAS char*)smem;
  const u32 sm0 = (u32)(size_t)sm;
  STAMP(0);
  const int tid = threadIdx.x, lane = tid & 63, w = __builtin_amdgcn_readfirstlane(tid >> 6), g = lane >> 4, c = lane & 15;
  const int bid = blockIdx.x, xcd = bid & 7, isB = bid >> 7, jj = (bid >> 3) & 15, qt = jj & 3, h = 4 * xcd + (jj >> 2);
  u64 pAx = (u64)Ax, pAk = (u64)Ak, pAv = (u64)Av, pAwq = (u64)Awq, pAwo = (u64)Awo, pBx = (u64)Bx, pBc = (u64)Bc, pBwq = (u64)Bwq, pBwuk = (u64)Bwuk, pBwuv = (u64)Bwuv, pBwo = (u64)Bwo, pOut = (u64)out;
  const u32 ring = sm0 + w * 16384, ringo = w * 16384;
  const u32 wrow = (u32)(16 * w * 8192 + h * 256);
  const u32 wo0 = g * 8192 + c * 16, wo1 = wo0 + 31744, wo2 = wo0 + 2 * 31744, wo3 = wo0 + 3 * 31744, lo16 = lane * 16;
  const int bB = xcd >> 2, sB = (xcd & 3) * 16 + jj;
  const u64 cb = pBc + (u32)((bB * SK + sB * 128 + 4 * w) * 1024);
  const u32 co0 = lo16 ^ ((w & 3) * 16), cl = sm0 + w * 4096;
  if (w < 8) {
    asm volatile("s_nop 4" ::: "memory");
    dma4<false, false, false>(isB ? cb : pBwq + (wrow + (u32)qt * (128 * 8192)), isB ? co0 : wo0, isB ? co0 ^ 64 : wo1, isB ? co0 ^ 128 : wo2, isB ? co0 ^ 192 : wo3, isB ? cl : ring + 4096);
  }
  asm volatile("" : "+s"(pAx), "+s"(pAk), "+s"(pAv), "+s"(pAwo), "+s"(pBx), "+s"(pAwq), "+s"(pBwuk), "+s"(pBwuv), "+s"(pBwo), "+s"(pOut));
  u32 tag = *(const __attribute__((address_space(4))) u32*)(u64)ws;
  const __amdgpu_buffer_rsrc_t rs = __builtin_amdgcn_make_buffer_rsrc(ws, 0, (int)WS_USED, 0x00020000);
  const int isT = isB & ~jj & 1;
  const int bA = jj >> 3, sA = isB ? 8 + ((jj >> 1) & 3) : (jj & 7);

  if (w == 8) {
    const u64 ht0 = __builtin_amdgcn_s_memtime();
    asm volatile("" : "+s"(tag)); tag += 1u;
    __builtin_amdgcn_s_setprio(3);
    if (CODE_KB && jj == 0) {
      const u64 pc = pc0 & ~255ull;
      v4u acc = {0u, 0u, 0u, 0u};
#pragma unroll
      for (int k = 0; k < CODE_KB; ++k) acc ^= *(const GAS v4u*)(pc + (u32)(k * 1024 + lo16));
      asm volatile("" :: "v"(acc));
    }
    { const u32 touch = *(volatile GAS u32*)pOut; asm volatile("" :: "v"(touch)); }
    if (!isB) {
      if (lane < 16) *(LAS u32*)(sm + L_FLAG + lane * 4) = 0u;
      BAR(); BAR();
      sleep_until(ht0, QA_T);
      put_qa(sm, gather_qa(rs, xcd, bA, lane, tag), lane);
      STAMP(12);
    } else {
      BAR();
      v4u qa = {0u, 0u, 0u, 0u};
      if (!isT) { sleep_until(ht0, QA_T); qa = gather_qa(rs, xcd, bA, lane, tag); STAMP(12); }
      if (SENTINEL) {
        const u32 so = QG_OFF + (u32)bB * 65536 + (u32)((8 * (lane >> 5) + 7) * 32 + (lane & 31)) * 64;
        _Pragma("nounroll") for (u32 n = 0;;) { const u64 x = ld_gran(ws, so), y = ld_gran(ws, so + 8 * 4096); if (__all(((u32)(x >> 32) == tag) & ((u32)(y >> 32) == tag)) || spin(n)) break; }
      }
      STAMP(16);
      BAR(); BAR(); BAR(); BAR();
      if (lane < 16) *(LAS u32*)(sm + L_FLAG + lane * 4) = 0u;
      BAR(); BAR(); BAR();
      if (!isT) put_qa(sm, qa, lane);
      lds_wait(sm, F_DRAIN, 8);
      if (lane < 32) st_gran(ws, LB_OFF + (u32)((bB * 64 + sB) * 32 + lane) * 8, *(LAS u32*)(sm + L_LSE + lane * 4), tag);
      STAMP(15);
      if (isT) {
        u64 l0, l1;
        _Pragma("nounroll") for (u32 n = 0;;) {
          l0 = ld_gran(ws, LB_OFF + (u32)(lane * 32 + h) * 8); l1 = ld_gran(ws, LB_OFF + (u32)((64 + lane) * 32 + h) * 8);
          if (__all(((u32)(l0 >> 32) == tag) & ((u32)(l1 >> 32) == tag)) || spin(n)) break;
        }
        const float f0 = asf((u32)l0), f1 = asf((u32)l1);
        const float e0 = ex2(f0 - x32max(x16max(row16max(f0)))), e1 = ex2(f1 - x32max(x16max(row16max(f1))));
        *(LAS __bf16*)(sm + L_LBV + lane * 2) = (__bf16)(e0 * rcp(x32sum(x16sum(row16sum(e0)))));
        *(LAS __bf16*)(sm + L_LBV + 128 + lane * 2) = (__bf16)(e1 * rcp(x32sum(x16sum(row16sum(e1)))));
        lds_add(sm, F_LB, lane);
      }
    }
    STAMP_OUT();
    return;
  }

  const u32 sw = (c ^ g) * 16;
  const u32 ko0 = g * 256 + sw, ko1 = g * 256 + (sw ^ 64), ko2 = g * 256 + (sw ^ 128), ko3 = g * 256 + (sw ^ 192);
  const int nt = isB ? NX : NA, T = (nt >> 3) + (w < (nt & 7));
  const u32 kv_off = (u32)((bA * 8 + xcd) * SK + 16 * ((isB ? 8 * NA + NX * (sA - 8) : NA * sA) + w)) * 256;
  u64 kb = pAk + kv_off, vb = pAv + kv_off, fb = pAwo + (u32)((128 * h + 16 * w) * 1024 + qt * 256);
  asm volatile("" : "+s"(kb), "+s"(vb), "+s"(fb));
  const int tb = (tid >> 5) & 1, tcol = 32 * ((tid >> 6) & 3) + (tid & 31), tr = (tid & 0xE0) | ((tid & 7) << 2) | ((tid >> 3) & 3);
  const Lane L = make_lane(c);

  if (!isB) {
    {
      const u64 src = pBx + (u32)(((lane >> 1) & 1) * 512 + 128 * ((lane >> 2) & 3) + 16 * w + 8 * (lane & 1)) * 2;
      asm volatile("s_mov_b32 m0, %1\n\ts_nop 4\n\tglobal_load_lds_dwordx4 %0, off" :: "v"(src), "s"(sm0 + L_XS + w * 1024) : "memory");
    }
    const u64 wq_b = pBwq + wrow;
    const u32 xso = L_XS + w * 1024 + (c >> 3) * 32 + g * 8;
    f32x4 acc = ZERO4;
    v4u W[4];
    asm volatile("" : "+s"(tag)); tag += 1u;
    dma4<false, true, false>(pBwuk + (wrow + (u32)qt * (128 * 8192)), g * 8192 + sw, g * 8192 + (sw ^ 64) + 31744, g * 8192 + (sw ^ 128) + 2 * 31744, g * 8192 + (sw ^ 192) + 3 * 31744, ring + 3 * 4096);
    VMWAIT(4);
    STAMP(13);
    dma4<false, false, false>(wq_b + (u32)(((qt + 1) & 3) * (128 * 8192)), wo0, wo1, wo2, wo3, ring + 2 * 4096);
    dma4<false, false, false>(wq_b + (u32)(((qt + 2) & 3) * (128 * 8192)), wo0, wo1, wo2, wo3, ring);
    ldsW(W, sm, ringo + 4096, g, c); acc = gemv4(W, *(LAS v2u*)(sm + xso + qt * 64), L, acc);
    dma4<false, false>(wq_b + (u32)(((qt + 3) & 3) * (128 * 8192)), wo0, wo1, wo2, wo3, ring + 4096);
    VMWAIT(8);
    STAMP(14);
    ldsW(W, sm, ringo + 2 * 4096, g, c); acc = gemv4(W, *(LAS v2u*)(sm + xso + ((qt + 1) & 3) * 64), L, acc);
    VMWAIT(4);
    ldsW(W, sm, ringo, g, c); acc = gemv4(W, *(LAS v2u*)(sm + xso + ((qt + 2) & 3) * 64), L, acc);
    dma4<true, true>(kb, ko0, ko1, ko2, ko3, ring);
    ldsW(W, sm, ringo + 4096, g, c); acc = gemv4(W, *(LAS v2u*)(sm + xso + ((qt + 3) & 3) * 64), L, acc);
    *(LAS f32x4*)(sm + L_PART + w * 1024 + lo16) = acc;
    STAMP(1);
    BAR();
    STAMP(16);
    if (tid < 256) *(LAS __bf16*)(sm + L_XBUF + (tb * 128 + tcol) * 2) = (__bf16)reduce8<1024>(sm, L_PART + tr * 4);
    BAR();
    STAMP(17);
    {
      f32x4 qa = ZERO4;
#pragma unroll
      for (int k = 0; k < 4; ++k)
        qa = mfma16(*(LAS v4u*)(sm + ringo + 3 * 4096 + c * 256 + ((4 * k + g) ^ c) * 16), *(LAS v4u*)(sm + L_XBUF + (c & 1) * 256 + k * 64 + g * 16), qa);
      if (c < 2) {
        __builtin_amdgcn_raw_buffer_store_b128(v4u{cvtpk(qa.x, qa.y), tag, cvtpk(qa.z, qa.w), tag}, rs, QG_OFF + (u32)c * 65536 + (u32)(((8 * qt + w) * 32 + h) * 4 + g) * 16, 0, 16);
      }
    }
    STAMP(2);
  } else {
    const int h2 = lane >> 5, hd = lane & 31;
    v2u xs;
    asm volatile("global_load_dwordx2 %0, %1, %2" : "=v"(xs) : "v"((u32)(((c >> 3) * 512 + 128 * qt + 16 * w + 4 * g) * 2)), "s"(pAx) : "memory");
    dma4<true, true, false>(cb + 32768, co0, co0 ^ 64, co0 ^ 128, co0 ^ 192, cl + 32768);
    asm volatile("" : "+v"(xs));
    asm volatile("" : "+s"(tag)); tag += 1u;
    dma4<true, true, false>(pAwq + (wrow + (u32)qt * (128 * 8192)), wo0, wo1, wo2, wo3, sm0 + EXTRA + w * 4096);
    dma4<true, true, false>(cb + 2 * 32768, co0, co0 ^ 64, co0 ^ 128, co0 ^ 192, cl + 2 * 32768);
    const int half = (jj >> 1) & 1;
    const u64 ub = isT ? pBwuv + (u32)((256 * half + 32 * w) * 8192 + h * 256) : kb;
    const u32 uo = isT ? g * 32768 + c * 16 : ko0, us = isT ? 8192 : 1024, ux = isT ? 0 : 64;
    v4u UW[8], FW[8];
#pragma unroll
    for (int i = 0; i < 4; ++i) UW[i] = __builtin_nontemporal_load((const GAS v4u*)(ub + (u32)((uo ^ (ux * i)) + us * i)));
    {
      v4u W[4];
      ldsW(W, sm, EXTRA + w * 4096, g, c);
      const f32x4 acc = gemv4(W, xs, L, ZERO4);
      *(LAS f32x4*)(sm + EXTRA + w * 4096 + lo16) = acc;
    }
    BAR();
    if (tid < 256) st_gran(ws, QA_OFF + (u32)((h * 4 + qt) * 256 + tb * 128 + tcol) * 8, asu(reduce8<4096>(sm, EXTRA + tr * 4)), tag);
    STAMP(14);
    dma4<true, true, false>(cb + 3 * 32768, co0, co0 ^ 64, co0 ^ 128, co0 ^ 192, cl + 3 * 32768);
    BAR();
    const u64 ub2 = isT ? ub + 16 * 8192 : vb, fb1 = isT ? pBwo + (u32)((128 * h + 16 * w) * 1024 + half * 512) : kb + 32768, fb2 = fb1 + 256;
    const u32 uo2 = isT ? uo : lo16, fo1 = isT ? g * 4096 + c * 16 : ko0, fo2 = fo1;
#define LOAD_U2() { _Pragma("unroll") for (int i = 0; i < 4; ++i) UW[4 + i] = __builtin_nontemporal_load((const GAS v4u*)(ub2 + (u32)(uo2 + us * i))); }
#define LOAD_F1() { _Pragma("unroll") for (int i = 0; i < 4; ++i) FW[i] = __builtin_nontemporal_load((const GAS v4u*)(fb1 + (u32)((fo1 ^ (ux * i)) + 1024 * i))); }
#define LOAD_F2() if (isT) { _Pragma("unroll") for (int i = 0; i < 4; ++i) FW[4 + i] = __builtin_nontemporal_load((const GAS v4u*)(fb2 + (u32)(fo2 + 1024 * i))); }
#define LOAD_AT(P) { if (XPOS1 == P) LOAD_U2(); if (XPOS2 == P) LOAD_F1(); if (XPOS3 == P) LOAD_F2(); asm volatile("" ::: "memory"); }
    {
      const u32 qg = QG_OFF + (u32)bB * 65536 + (u32)tid * 16, ql = L_QB + (tid >> 7) * 1024 + ((tid >> 1) & 1) * 512 + ((tid >> 2) & 31) * 16 + (tid & 1) * 8;
      v4u q[8]; bool ok = true;
#pragma unroll
      for (int k = 0; k < 8; ++k) q[k] = ld2(rs, qg + ((k + jj) & 7) * 8192);
      __builtin_amdgcn_sched_barrier(0);
      __builtin_amdgcn_s_barrier();
      __builtin_amdgcn_sched_barrier(0);
      if (XPOS1 == 0) LOAD_U2(); if (XPOS2 == 0) LOAD_F1(); if (XPOS3 == 0) LOAD_F2();
      __builtin_amdgcn_sched_barrier(0);
#pragma unroll
      for (int k = 0; k < 8; ++k) { ok &= (q[k].y == tag) & (q[k].w == tag); *(LAS v2u*)(sm + ql + ((k + jj) & 7) * 4096) = v2u{q[k].x, q[k].z}; }
      _Pragma("nounroll") for (u32 n = 0; !__all(ok) && !spinl(n);) {
        ok = true;
#pragma unroll 1
        for (int k = 0; k < 8; ++k) { const v4u x = ld2(rs, qg + k * 8192); ok &= (x.y == tag) & (x.w == tag); *(LAS v2u*)(sm + ql + k * 4096) = v2u{x.x, x.z}; }
      }
    }
    STAMP(1);
    BAR();
    STAMP(8);
    LOAD_AT(1)
    f32x16 sc;
#pragma unroll
    for (int k = 0; k < 16; ++k) sc[k] = 0.f;
    float mw = 0.f;
    if (w < 4) {
      const u32 ca = (32 * w + hd) * 1024, cx = (h2 ^ (((hd & 3) << 2) | ((hd >> 2) & 3))) * 16;
      u32 qo = L_QB + lo16;
      asm volatile("" : "+v"(qo));
      v4u fa[3][2], fq[3][2];
#define QK_LOAD(B) _Pragma("unroll") for (int k = 0; k < 2; ++k) { fa[(B) % 3][k] = *(LAS v4u*)(sm + ca + ((B) >> 2) * 256 + (cx ^ ((2 * ((B) & 3) + k) * 32))); fq[(B) % 3][k] = *(LAS v4u*)(sm + qo + (2 * (B) + k) * 1024); }
      QK_LOAD(0) QK_LOAD(1)
#pragma unroll
      for (int bb = 0; bb < 16; ++bb) {
        if (bb < 14) { QK_LOAD(bb + 2) }
        __builtin_amdgcn_sched_barrier(0);
#pragma unroll
        for (int k = 0; k < 2; ++k) sc = mfma32(fa[bb % 3][k], fq[bb % 3][k], sc);
        __builtin_amdgcn_sched_barrier(0);
      }
#undef QK_LOAD
      mw = sc[0];
#pragma unroll
      for (int k = 1; k < 16; ++k) mw = fmaxf(mw, sc[k]);
      mw = x32max(mw);
    }
    STAMP(2);
    BAR();
    LOAD_AT(2)
    if (w < 4 && lane < 32) *(LAS float*)(sm + L_BM + hd * 16 + w * 4) = mw;
    BAR();
    if (w < 4) {
      const float M = C2 * max4(*(LAS f32x4*)(sm + L_BM + hd * 16));
      float ls = 0.f;
#pragma unroll
      for (int k = 0; k < 16; ++k) { sc[k] = ex2(fmaf(sc[k], C2, -M)); ls += sc[k]; }
      *(LAS v4u*)(sm + L_PF + w * 2048 + lo16) = v4u{cvtpk(sc[0], sc[1]), cvtpk(sc[2], sc[3]), cvtpk(sc[4], sc[5]), cvtpk(sc[6], sc[7])};
      *(LAS v4u*)(sm + L_PF + w * 2048 + 1024 + lo16) = v4u{cvtpk(sc[8], sc[9]), cvtpk(sc[10], sc[11]), cvtpk(sc[12], sc[13]), cvtpk(sc[14], sc[15])};
      ls = x32sum(ls);
      if (lane < 32) *(LAS float*)(sm + L_BL + hd * 16 + w * 4) = ls;
    }
    BAR();
    STAMP(11);
    LOAD_AT(3)
    f32x16 oa, ob;
#pragma unroll
    for (int k = 0; k < 16; ++k) { oa[k] = 0.f; ob[k] = 0.f; }
    {
      const int q = (lane >> 2) & 3, p = lane & 3, blk = (lane >> 4) & 1;
      const u32 base = (4 * h2 + q) * 1024 + (w >> 1) * 256 + 8 * (p & 1), lo4 = 8 * (w & 1) + 2 * blk + (p >> 1);
      const u32 a00 = base + 16 * (lo4 ^ ((q << 2) | h2)), a01 = base + 16 * ((lo4 + 4) ^ ((q << 2) | h2));
      const u32 a10 = base + 8192 + 16 * (lo4 ^ ((q << 2) | (2 + h2))), a11 = base + 8192 + 16 * ((lo4 + 4) ^ ((q << 2) | (2 + h2)));
      u32 po = L_PF + lo16;
      asm volatile("" : "+v"(po));
      v4u pb[2]; v2u tt[2][4];
#define TRR(A) __builtin_bit_cast(v2u, __builtin_amdgcn_ds_read_tr16_b64_v4i16((LAS s16x4*)(sm + (A))))
#define PV_LOAD(K, S) { pb[S] = *(LAS v4u*)(sm + po + (K) * 1024); tt[S][0] = TRR(a00 + (K) * 16384); tt[S][1] = TRR(a10 + (K) * 16384); tt[S][2] = TRR(a01 + (K) * 16384); tt[S][3] = TRR(a11 + (K) * 16384); }
      PV_LOAD(0, 0)
#pragma unroll
      for (int ks = 0; ks < 8; ++ks) {
        if (ks < 7) PV_LOAD(ks + 1, (ks + 1) & 1)
        __builtin_amdgcn_sched_barrier(0);
        oa = mfma32(v4u{tt[ks & 1][0].x, tt[ks & 1][0].y, tt[ks & 1][1].x, tt[ks & 1][1].y}, pb[ks & 1], oa);
        ob = mfma32(v4u{tt[ks & 1][2].x, tt[ks & 1][2].y, tt[ks & 1][3].x, tt[ks & 1][3].y}, pb[ks & 1], ob);
        __builtin_amdgcn_sched_barrier(0);
      }
#undef PV_LOAD
#undef TRR
    }
    STAMP(3);
    {
      const float Ls = sum4(*(LAS f32x4*)(sm + L_BL + hd * 16));
      const float inv = rcp(Ls);
      if (tid < 32) *(LAS float*)(sm + L_LSE + tid * 4) = fmaf(C2, max4(*(LAS f32x4*)(sm + L_BM + hd * 16)), __builtin_amdgcn_logf(Ls));
      const u32 so = hd * 1024 + (w >> 1) * 256 + 8 * h2, sx = (8 * (w & 1)) ^ (((hd & 3) << 2) | ((hd >> 2) & 3));
#pragma unroll
      for (int k = 0; k < 4; ++k) {
        *(LAS v2u*)(sm + so + (sx ^ k) * 16) = v2u{cvtpk(oa[4 * k] * inv, oa[4 * k + 1] * inv), cvtpk(oa[4 * k + 2] * inv, oa[4 * k + 3] * inv)};
        *(LAS v2u*)(sm + so + (sx ^ (4 + k)) * 16) = v2u{cvtpk(ob[4 * k] * inv, ob[4 * k + 1] * inv), cvtpk(ob[4 * k + 2] * inv, ob[4 * k + 3] * inv)};
      }
      asm volatile("s_waitcnt lgkmcnt(0)" ::: "memory");
#pragma unroll
      for (int i = 0; i < 4; ++i) {
        const int hh = 8 * i + (lane >> 3);
        const v4u v = *(LAS v4u*)(sm + hh * 1024 + (w >> 1) * 256 + ((8 * (w & 1) + (lane & 7)) ^ (((hh & 3) << 2) | ((hh >> 2) & 3))) * 16);
        __builtin_amdgcn_raw_buffer_store_b128(v, rs, PB_OFF + (u32)(((hh * 2 + (w >> 2)) * 2 + bB) * 64 + sB) * 512 + (w & 3) * 128 + (lane & 7) * 16, 0, 16);
      }
    }
    STAMP(13);
    BAR();
    if (isT) {
      VMWAIT(0);
      lds_add(sm, F_DRAIN, lane);
      lds_wait(sm, F_LB, 1);
      STAMP(17);
      v4u X[8];
#pragma unroll
      for (int i = 0; i < 8; ++i) X[i] = ld2(rs, PB_OFF + (u32)((h * 2 + half) * 128 + 16 * w + 4 * g + (i & 3)) * 512 + (i >> 2) * 256 + c * 16);
      v2u xw = *(LAS v2u*)(sm + L_LBV + w * 32 + g * 8);
      if ((c >> 3) != (w >> 2)) xw = v2u{0u, 0u};
      *(LAS f32x4*)(sm + L_PART + w * 1024 + lo16) = gemv4(X, xw, L, ZERO4);
      *(LAS f32x4*)(sm + L_PART2 + w * 1024 + lo16) = gemv4(X + 4, xw, L, ZERO4);
      const u32 po = (tid < 256 ? L_PART : L_PART2) + tr * 4;
      STAMP(4);
      BAR();
      *(LAS __bf16*)(sm + L_XBUF + (tb * 256 + (tid >> 8) * 128 + tcol) * 2) = (__bf16)reduce8<1024>(sm, po);
      BAR();
      const u32 xo = L_XBUF + (c >> 3) * 512 + w * 64 + g * 8;
      *(LAS f32x4*)(sm + L_PART + w * 1024 + lo16) = gemv4(UW + 4, *(LAS v2u*)(sm + xo + 32), L, gemv4(UW, *(LAS v2u*)(sm + xo), L, ZERO4));
      BAR();
      if (tid < 256) {
        const float own = reduce8<1024>(sm, po);
        st_gran(ws, CX_OFF + (u32)((h * 2 + half) * 256 + tid) * 8, asu(own), tag);
        STAMP(19);
        u64 x;
        _Pragma("nounroll") for (u32 n = 0;;) { x = ld_gran(ws, CX_OFF + (u32)((h * 2 + (half ^ 1)) * 256 + tid) * 8); if (__all((u32)(x >> 32) == tag) || spin(n)) break; }
        *(LAS __bf16*)(sm + L_XBUF + (tb * 128 + tcol) * 2) = (__bf16)(half ? asf((u32)x) + own : own + asf((u32)x));
        STAMP(20);
      }
      BAR();
      const v2u xc = *(LAS v2u*)(sm + L_XBUF + (c >> 3) * 256 + w * 32 + g * 8);
      *(LAS f32x4*)(sm + L_PART + w * 1024 + lo16) = gemv4(FW, xc, L, ZERO4);
      *(LAS f32x4*)(sm + L_PART2 + w * 1024 + lo16) = gemv4(FW + 4, xc, L, ZERO4);
      BAR();
      st_gran(ws, YG_OFF + (u32)((((2 + tb) * 64 + 32 * half + 16 * (tid >> 8) + (tcol >> 3)) * 32 + h) * 8 + (tcol & 7)) * 8, asu(reduce8<1024>(sm, po)), tag);
      STAMP(9);
      final_reduce(sm, F_OUT + (w & 1) * 4, w >> 1, ws, pOut, 1, (xcd * 8 + (jj >> 1)) * 2 + (w & 1), lane, tag); STAMP(10);
      STAMP_OUT();
      return;
    }
    dma4<false, true, false>(vb + 32768, lo16, lo16, lo16, lo16, ring + 3 * 4096);
#pragma unroll
    for (int i = 0; i < 8; ++i) *(LAS v4u*)(sm + ringo + i * 1024 + lo16) = UW[i];
#pragma unroll
    for (int i = 0; i < 4; ++i) *(LAS v4u*)(sm + ringo + 8192 + i * 1024 + lo16) = FW[i];
    VMWAIT(4);
    lds_add(sm, F_DRAIN, lane);
  }
  v4u WO[4];
  if (!isB) {
    dma4<true, true>(vb, lo16, lo16, lo16, lo16, ring + 4096);
    dma4<true, true>(kb + 32768, ko0, ko1, ko2, ko3, ring + 2 * 4096);
    dma4<true, true>(vb + 32768, lo16, lo16, lo16, lo16, ring + 3 * 4096);
    if (!FLATE) VMWAIT(2);
#pragma unroll
    for (int i = 0; i < 4; ++i) if (!FLATE) WO[i] = __builtin_nontemporal_load((const GAS v4u*)(fb + (u32)(g * 1024 + ((c ^ (4 * g) ^ i) * 16) + i * 4096)));
    asm volatile("" ::: "memory");
  }
  STAMP(4);
  lds_wait(sm, F_Q, 1);
  STAMP(5);
  float Mw;
  {
    v4u qf[4];
#pragma unroll
    for (int k = 0; k < 4; ++k) qf[k] = *(LAS v4u*)(sm + L_QIMG + (c & 3) * 256 + k * 64 + g * 16);
    const u32 pm0 = (c >> 2) == 0 ? 0x0000FFFFu : (c >> 2) == 1 ? 0xFFFF0000u : 0u, pm1 = (c >> 2) == 2 ? 0x0000FFFFu : (c >> 2) == 3 ? 0xFFFF0000u : 0u;
    float m = -INFINITY, l = 0.f;
    f32x4 o0 = ZERO4, o1 = ZERO4;
    const u32 kr = ringo + c * 256, vr = ringo + 4096 + g * 1024 + c * 16;
    v2u y[4];
#define QK_STEP(SO) { \
      const f32x4 sa = mfma16(*(LAS v4u*)(sm + kr + (SO) + ((8 + g) ^ c) * 16), qf[2], mfma16(*(LAS v4u*)(sm + kr + (SO) + ((0 + g) ^ c) * 16), qf[0], ZERO4)) \
                     + mfma16(*(LAS v4u*)(sm + kr + (SO) + ((12 + g) ^ c) * 16), qf[3], mfma16(*(LAS v4u*)(sm + kr + (SO) + ((4 + g) ^ c) * 16), qf[1], ZERO4)); \
      const float mn = fmaxf(m, x32max(x16max(max4(sa)))); \
      const float al = ex2((m - mn) * C2), mc = mn * C2; \
      m = mn; \
      const float p[4] = {ex2(fmaf(sa.x, C2, -mc)), ex2(fmaf(sa.y, C2, -mc)), ex2(fmaf(sa.z, C2, -mc)), ex2(fmaf(sa.w, C2, -mc))}; \
      l = fmaf(l, al, (p[0] + p[1]) + (p[2] + p[3])); \
      o0 *= al; o1 *= al; \
      _Pragma("unroll") for (int i = 0; i < 4; ++i) { const u32 pp = cvtpk(p[i], p[i]); y[i] = v2u{pp & pm0, pp & pm1}; } }
#define PV_STEP(SO) _Pragma("unroll") for (int i = 0; i < 4; ++i) { \
      const v4u V = *(LAS v4u*)(sm + vr + (SO) + i * 256); \
      o0 = mfma16h(v2u{V.x, V.y}, y[i], o0); o1 = mfma16h(v2u{V.z, V.w}, y[i], o1); }
#pragma unroll 1
    for (int t = -1; t < T - 1; ++t) {
      const u32 so = (t & 1) * 8192;
      if (t >= 0) {
        if (t + 2 < T) dma4<8, true>(kb + (u32)((t + 2) * 32768), ko0, ko1, ko2, ko3, ring + so);
        else VMWAIT(4);
        PV_STEP(so)
      }
      QK_STEP(so ^ 8192)
      if (t >= 0 && t + 2 < T) dma4<8, true>(vb + (u32)((t + 2) * 32768), lo16, lo16, lo16, lo16, ring + so + 4096);
    }
    if (lane < 4) *(LAS float*)(sm + L_MST + lane * 32 + w * 4) = m;
    lds_add(sm, F_M, lane);
    l = x32sum(x16sum(l));
    u32 mo = L_MRG + w * 2048 + (c & 3) * 512 + ((4 * g + (c >> 2)) ^ (2 * (c & 3))) * 16, lo = L_MST + 128 + lane * 32 + w * 4, so = ((T - 1) & 1) * 8192;
    asm volatile("" : "+v"(mo), "+v"(lo), "+v"(l));
    lds_wait(sm, F_M, 8);
    Mw = fmaxf(max4(*(LAS f32x4*)(sm + L_MST + (c & 3) * 32)), max4(*(LAS f32x4*)(sm + L_MST + (c & 3) * 32 + 16)));
    float f = ex2((m - Mw) * C2);
    asm volatile("" : "+v"(f), "+v"(Mw));
    VMWAIT(0);
    PV_STEP(so)
    STAMP(6);
    *(LAS f32x4*)(sm + mo) = o0 * f;
    *(LAS f32x4*)(sm + mo + 256) = o1 * f;
    if (lane < 4) *(LAS float*)(sm + lo) = l * f;
#undef QK_STEP
#undef PV_STEP
  }
  {
    u32 ro = L_MRG + w * 512 + ((lane & 31) ^ (2 * w)) * 16, go = OA_OFF + (u32)((4 * xcd + w) * 24 + bA * 12 + sA) * 640 + lane * 16;
    const float Mc = C2 * asf(__builtin_amdgcn_readlane(asu(Mw), w & 3));
    asm volatile("" : "+v"(ro), "+v"(go));
    BAR();
    if (w < 4) {
      STAMP(18);
      f32x4 v[8];
#pragma unroll
      for (int ww = 0; ww < 8; ++ww) v[ww] = *(LAS f32x4*)(sm + ro + ww * 2048);
      const f32x4 la = *(LAS f32x4*)(sm + L_MST + 128 + w * 32), lb = *(LAS f32x4*)(sm + L_MST + 128 + w * 32 + 16);
      __builtin_amdgcn_sched_barrier(0);
      const float Ls = sum4(la + lb), inv = rcp(Ls);
      const f32x4 a = (((v[0] + v[1]) + (v[2] + v[3])) + ((v[4] + v[5]) + (v[6] + v[7]))) * inv;
      const u32 p0 = lane < 32 ? cvtpk(a.x, a.y) : asu(Mc + __builtin_amdgcn_logf(Ls));
      if (lane < 40) __builtin_amdgcn_raw_buffer_store_b128(v4u{p0, tag, cvtpk(a.z, a.w), tag}, rs, go, 0, 16);
      STAMP(7);
    }
  }
  if (isB) { STAMP_OUT(); return; }
  if (!FLATE) {
#pragma unroll
    for (int i = 0; i < 4; ++i) *(LAS v4u*)(sm + w * 4096 + i * 1024 + lo16) = WO[i];
    lds_add(sm, F_FIN, lane);
  }
  if (w < 4) {
    const int ob = w & 1;
    const u32 ro = OA_OFF + (u32)(h * 24 + 12 * ob + g) * 640;
    v4u x[6]; u64 ls[3];
    if ((w >> 1) >= OPOLL) { STAMP_OUT(); return; }
    if (w >> 1) asm volatile("s_sleep %0" :: "i"(2 * STAG) : "memory");
    if (OSENT) {
      const u32 so = OA_OFF + (u32)(h * 24 + 12 * ob + (lane < 12 ? lane : 0)) * 640 + 512;
      _Pragma("nounroll") for (u32 n = 0;;) {
        const u64 z = ld_gran(ws, so);
        if (__builtin_popcount((u32)__ballot((u32)(z >> 32) == tag) & 0xFFFu) >= OTHR || ++n > SPIN_CAP) break;
        asm volatile("s_sleep %0" :: "i"(OSLEEP) : "memory");
      }
      STAMP(11);
    }
    _Pragma("nounroll") for (u32 n = 0;;) {
      bool ok = true;
#pragma unroll
      for (int k = 0; k < 3; ++k) { x[2 * k] = ld2(rs, ro + k * 2560 + c * 32); x[2 * k + 1] = ld2(rs, ro + k * 2560 + c * 32 + 16); ls[k] = ld_gran(ws, ro + k * 2560 + 512); }
#pragma unroll
      for (int k = 0; k < 3; ++k) ok &= (x[2 * k].y == tag) & (x[2 * k].w == tag) & (x[2 * k + 1].y == tag) & (x[2 * k + 1].w == tag) & ((u32)(ls[k] >> 32) == tag);
      if (__all(ok) || ++n > SPIN_CAP) break;
      if (__builtin_amdgcn_readfirstlane(__hip_atomic_load((LAS u32*)(sm + F_FIN + 8 + 4 * ob), RLX_WG))) { STAMP_OUT(); return; }
      asm volatile("s_sleep %0" :: "i"(OSLEEP) : "memory");
    }
    STAMP(8);
    const float M = x32max(x16max(fmaxf(fmaxf(asf((u32)ls[0]), asf((u32)ls[1])), asf((u32)ls[2]))));
    f32x4 oc = ZERO4; float lw = 0.f;
#pragma unroll
    for (int k = 0; k < 3; ++k) {
      const float e = ex2(asf((u32)ls[k]) - M);
      const u32 pp = cvtpk(e, e);
      lw += asf(pp << 16);
      oc = mfma16(v4u{x[2 * k].x, x[2 * k].z, x[2 * k + 1].x, x[2 * k + 1].z}, v4u{pp & L.m0, pp & L.m1, pp & L.m2, pp & L.m3}, oc);
    }
    const float inv = rcp(x32sum(x16sum(lw)));
    if (c < 8) {
      const u32 xo = L_XBUF + ob * 256 + (16 * ((c >> 1) & 1) + 8 * (c & 1) + 4 * (g >> 1) + (c >> 2)) * 2;
#pragma unroll
      for (int r = 0; r < 4; ++r) *(LAS __bf16*)(sm + xo + (32 * ((2 * (g & 1) + (r >> 1))) + 2 * (r & 1)) * 2) = (__bf16)(oc[r] * inv);
    }
    lds_add(sm, F_FIN + 8 + 4 * ob, lane);
    STAMP_OUT();
    return;
  }
  {
    const int q = (lane >> 2) & 3, p = lane & 3;
    if (FLATE) {
      const u32 f0 = g * 1024, fx = (c ^ (4 * g)) * 16, fl = sm0 + (w - 4) * 8192;
      const u64 fb2 = fb - (u32)((8 - w) * 16384);
      asm volatile("s_sleep %0" :: "i"(FSLEEP) : "memory");
      dma4<false, true, false>(fb2, f0 + fx, f0 + 3072 + (fx ^ 16), f0 + 2 * 3072 + (fx ^ 32), f0 + 3 * 3072 + (fx ^ 48), fl);
      dma4<false, true, false>(fb2 + 16384, f0 + fx, f0 + 3072 + (fx ^ 16), f0 + 2 * 3072 + (fx ^ 32), f0 + 3 * 3072 + (fx ^ 48), fl + 4096);
      VMWAIT(0);
      lds_add(sm, F_FIN, lane);
    }
    lds_wait(sm, F_FIN, FLATE ? 4 : 8);
    v4u wt[4][2];
#pragma unroll
    for (int k = 0; k < 4; ++k) {
      const u32 ra = (32 * k + 8 * g + q) * 256 + 8 * (p & 1);
#pragma unroll
      for (int e = 0; e < 2; ++e) {
        const u32 ch = 4 * (w - 4) + 2 * e + (p >> 1);
        const v2u t0 = __builtin_bit_cast(v2u, __builtin_amdgcn_ds_read_tr16_b64_v4i16((LAS s16x4*)(sm + ra + (ch ^ ((q << 2) | (2 * (g & 1)))) * 16)));
        const v2u t1 = __builtin_bit_cast(v2u, __builtin_amdgcn_ds_read_tr16_b64_v4i16((LAS s16x4*)(sm + ra + 1024 + (ch ^ ((q << 2) | (2 * (g & 1) + 1))) * 16)));
        wt[k][e] = v4u{t0.x, t0.y, t1.x, t1.y};
        asm volatile("" : "+v"(wt[k][e]));
      }
    }
    u32 xa = L_XBUF + (c & 1) * 256 + g * 16, yo = YG_OFF + (u32)((((g & 1) * 64 + 16 * qt + 4 * (w - 4) + 2 * (g >> 1) + (c >> 3)) * 32 + h) * 8 + (c & 7)) * 8;
    asm volatile("" : "+v"(xa), "+v"(yo));
    _Pragma("nounroll") for (u32 n = 0; n < (SPIN_CAP << 4); ++n) {
      const u64 f12 = __hip_atomic_load((LAS u64*)(sm + F_FIN + 8), RLX_WG);
      if (__builtin_amdgcn_readfirstlane(((u32)f12 > 0) & ((u32)(f12 >> 32) > 0))) break;
    }
    asm volatile("" ::: "memory");
    STAMP(19);
    v4u xo[4];
#pragma unroll
    for (int k = 0; k < 4; ++k) xo[k] = *(LAS v4u*)(sm + xa + k * 64);
    __builtin_amdgcn_sched_barrier(0);
    f32x4 y0 = ZERO4, y1 = ZERO4;
#pragma unroll
    for (int k = 0; k < 4; ++k) { y0 = mfma16(xo[k], wt[k][0], y0); y1 = mfma16(xo[k], wt[k][1], y1); }
    st_gran(ws, yo, asu(g == 0 ? y0.x : g == 1 ? y0.y : g == 2 ? y1.x : y1.y), tag);
  }
  STAMP(9);
  final_reduce(sm, F_OUT, w - 4, ws, pOut, 0, bid, lane, tag);
  if (bid == 0 && w == 4 && lane == 0) *(GAS u32*)ws = tag;
  STAMP(10);
  STAMP_OUT();
}

extern "C" void kernel_launch(void* const* d_in, const int* in_sizes, int n_in, void* d_out, int out_size, void* d_ws, size_t ws_size, hipStream_t stream) {
  fused_decode<<<256, 576, 0, stream>>>((const u16*)d_in[7], (const u16*)d_in[6], (char*)d_ws, (const u16*)d_in[5], (const u16*)d_in[0], (const u16*)d_in[3],
                                        (const u16*)d_in[8], (const u16*)d_in[1], (const u16*)d_in[2], (const u16*)d_in[4], (const u16*)d_in[9],
                                        (const u16*)d_in[10], (u16*)d_out);
}
```

```cpp
#include <hip/hip_runtime.h>
#include <stdint.h>

typedef unsigned short u16;
typedef unsigned u32;
typedef unsigned long long u64;
#define GAS __attribute__((address_space(1)))
#define LAS __attribute__((address_space(3)))
typedef u32 v4u __attribute__((ext_vector_type(4)));
typedef u32 v2u __attribute__((ext_vector_type(2)));
typedef float f32x2 __attribute__((ext_vector_type(2)));
typedef float f32x4 __attribute__((ext_vector_type(4)));
typedef float f32x16 __attribute__((ext_vector_type(16)));
typedef __bf16 bf16x8 __attribute__((ext_vector_type(8)));
typedef __bf16 bf16x2 __attribute__((ext_vector_type(2)));
typedef short s16x4 __attribute__((ext_vector_type(4)));
#define RLX_AGENT __ATOMIC_RELAXED, __HIP_MEMORY_SCOPE_AGENT
#define RLX_WG __ATOMIC_RELAXED, __HIP_MEMORY_SCOPE_WORKGROUP

#define TILES_A 48
constexpr int SK = 8192;
constexpr int NA = TILES_A, NX = 128 - 2 * TILES_A;
static_assert(8 * NA + 4 * NX == 512 && NX >= 16 && NA >= 16, "tile split");
constexpr float C2 = 0.08838834764831845f * 1.4426950408889634f;
constexpr u32 SPIN_CAP = 1u << 17;

constexpr u32 QG_OFF = 4096;
constexpr u32 QA_OFF = QG_OFF + 131072;
constexpr u32 OA_OFF = QA_OFF + 262144;
constexpr u32 LB_OFF = OA_OFF + 32 * 24 * 640;
constexpr u32 CX_OFF = LB_OFF + 32768;
constexpr u32 YG_OFF = CX_OFF + 131072;
constexpr u32 PB_OFF = YG_OFF + 524288;
constexpr u32 WS_USED = PB_OFF + 4194304;
#define STAMP(k) do { if ((k) == 0 || (k) == 21) st[k] = __builtin_amdgcn_s_memrealtime(); } while (0)
#define STAMP_OUT()

constexpr u32 EXTRA = 131072;
constexpr u32 L_PART = EXTRA, L_XBUF = EXTRA + 8192, L_FLAG = EXTRA + 15040, L_MST = EXTRA + 15104, L_QIMG = EXTRA + 15360, L_MRG = EXTRA + 16384, L_XS = EXTRA + 16384;
constexpr u32 L_QB = EXTRA, L_PF = EXTRA, L_BM = EXTRA + 9216, L_BL = EXTRA + 10240, L_LSE = EXTRA + 11264, L_PART2 = EXTRA + 16384;
constexpr u32 F_Q = L_FLAG, F_LB = L_FLAG + 4, F_DRAIN = L_FLAG + 8, F_M = L_FLAG + 12, F_OUT = L_FLAG + 16, F_FIN = L_FLAG + 32, L_LBV = EXTRA + 11392;
#define CODE_KB 21
#define XPOS1 0
#define XPOS2 1
#define XPOS3 3
#define OPOLL 1
#define OSENT 1
#define OSLEEP 1
#define YPOLL 1
#define YSLEEP 1
#define OTHR 10
#define FLATE 1
#define FSLEEP 4
#define STAG 3
#define QA_T 13000
#define SENTINEL 1

__device__ __forceinline__ u32 cvtpk(float lo, float hi) { bf16x2 p = {(__bf16)lo, (__bf16)hi}; return __builtin_bit_cast(u32, p); }
__device__ __forceinline__ f32x4 mfma16(v4u a, v4u b, f32x4 c) { return __builtin_amdgcn_mfma_f32_16x16x32_bf16(__builtin_bit_cast(bf16x8, a), __builtin_bit_cast(bf16x8, b), c, 0, 0, 0); }
__device__ __forceinline__ f32x4 mfma16h(v2u a, v2u b, f32x4 c) { return __builtin_amdgcn_mfma_f32_16x16x16bf16_1k(__builtin_bit_cast(s16x4, a), __builtin_bit_cast(s16x4, b), c, 0, 0, 0); }
__device__ __forceinline__ f32x16 mfma32(v4u a, v4u b, f32x16 c) { return __builtin_amdgcn_mfma_f32_32x32x16_bf16(__builtin_bit_cast(bf16x8, a), __builtin_bit_cast(bf16x8, b), c, 0, 0, 0); }
__device__ __forceinline__ float ex2(float x) { return __builtin_amdgcn_exp2f(x); }
__device__ __forceinline__ float rcp(float x) { return __builtin_amdgcn_rcpf(x); }
__device__ __forceinline__ float asf(u32 x) { return __builtin_bit_cast(float, x); }
__device__ __forceinline__ u32 asu(float x) { return __builtin_bit_cast(u32, x); }
template <int CTRL> __device__ __forceinline__ float dpp(float x) { return asf((u32)__builtin_amdgcn_mov_dpp((int)asu(x), CTRL, 0xf, 0xf, true)); }
__device__ __forceinline__ float x16max(float x) { auto s = __builtin_amdgcn_permlane16_swap(asu(x), asu(x), false, false); return fmaxf(asf(s[0]), asf(s[1])); }
__device__ __forceinline__ float x32max(float x) { auto s = __builtin_amdgcn_permlane32_swap(asu(x), asu(x), false, false); return fmaxf(asf(s[0]), asf(s[1])); }
__device__ __forceinline__ float x16sum(float x) { auto s = __builtin_amdgcn_permlane16_swap(asu(x), asu(x), false, false); return asf(s[0]) + asf(s[1]); }
__device__ __forceinline__ float x32sum(float x) { auto s = __builtin_amdgcn_permlane32_swap(asu(x), asu(x), false, false); return asf(s[0]) + asf(s[1]); }
__device__ __forceinline__ float row16max(float x) { x = fmaxf(x, dpp<0x128>(x)); x = fmaxf(x, dpp<0x124>(x)); x = fmaxf(x, dpp<0x122>(x)); return fmaxf(x, dpp<0x121>(x)); }
__device__ __forceinline__ float row16sum(float x) { x += dpp<0x128>(x); x += dpp<0x124>(x); x += dpp<0x122>(x); return x + dpp<0x121>(x); }
__device__ __forceinline__ float max4(f32x4 a) { return fmaxf(fmaxf(a.x, a.y), fmaxf(a.z, a.w)); }
__device__ __forceinline__ float sum4(f32x4 a) { return (a.x + a.y) + (a.z + a.w); }
#define ZERO4 f32x4{0.f, 0.f, 0.f, 0.f}

#define BAR() asm volatile("s_waitcnt lgkmcnt(0)\n\ts_barrier" ::: "memory")
#define VMWAIT(n) asm volatile("s_waitcnt vmcnt(" #n ")" ::: "memory")

template <int PACED, bool NT, bool WAR = true>
__device__ __forceinline__ void dma4(u64 base, u32 o0, u32 o1, u32 o2, u32 o3, u32 lds) {
  if (WAR) asm volatile("s_waitcnt lgkmcnt(0)" ::: "memory");
#define DMA_BODY(W, P) \
  asm volatile("s_mov_b32 m0, %5\n\ts_nop 0\n\t" W "global_load_lds_dwordx4 %0, %4" P "\n\t" W "global_load_lds_dwordx4 %1, %4 offset:1024" P "\n\t" \
               W "global_load_lds_dwordx4 %2, %4 offset:2048" P "\n\t" W "global_load_lds_dwordx4 %3, %4 offset:3072" P \
               :: "v"(o0), "v"(o1), "v"(o2), "v"(o3), "s"(base), "s"(lds) : "memory")
  if (PACED == 8) DMA_BODY("s_waitcnt vmcnt(7)\n\t", " nt");
  else if (PACED && NT) DMA_BODY("s_waitcnt vmcnt(3)\n\t", " nt");
  else if (PACED) DMA_BODY("s_waitcnt vmcnt(3)\n\t", "");
  else if (NT) DMA_BODY("", " nt");
  else DMA_BODY("", "");
#undef DMA_BODY
}

struct Lane { u32 m0, m1, m2, m3; };
__device__ __forceinline__ Lane make_lane(int c) {
  const int j0 = c & 7; const u32 hm = (j0 & 1) ? 0xFFFF0000u : 0x0000FFFFu;
  return Lane{(j0 >> 1) == 0 ? hm : 0u, (j0 >> 1) == 1 ? hm : 0u, (j0 >> 1) == 2 ? hm : 0u, (j0 >> 1) == 3 ? hm : 0u};
}
__device__ __forceinline__ v4u diag(v2u xs, int i, const Lane& L) {
  const u32 src = i < 2 ? xs.x : xs.y;
  const u32 pp = __builtin_amdgcn_perm(src, src, (i & 1) ? 0x03020302u : 0x01000100u);
  return v4u{pp & L.m0, pp & L.m1, pp & L.m2, pp & L.m3};
}
__device__ __forceinline__ f32x4 gemv4(const v4u* W, v2u xs, const Lane& L, f32x4 acc) {
#pragma unroll
  for (int i = 0; i < 4; ++i) acc = mfma16(W[i], diag(xs, i, L), acc);
  return acc;
}
__device__ __forceinline__ void ldsW(v4u (&W)[4], LAS char* sm, u32 unit, int g, int c) {
#pragma unroll
  for (int i = 0; i < 4; ++i) W[i] = *(LAS v4u*)(sm + unit + (4 * g + i) * 256 + c * 16);
}
template <int STRIDE> __device__ __forceinline__ float reduce8(LAS char* sm, u32 off) {
  float v[8];
  asm volatile("" : "+v"(off));
#pragma unroll
  for (int w = 0; w < 8; ++w) v[w] = *(LAS float*)(sm + off + w * STRIDE);
  __builtin_amdgcn_sched_barrier(0);
  return ((v[0] + v[1]) + (v[2] + v[3])) + ((v[4] + v[5]) + (v[6] + v[7]));
}
__device__ __forceinline__ void st_gran(char* ws, u32 off, u32 val, u32 tag) { __hip_atomic_store((GAS u64*)(ws + off), ((u64)tag << 32) | val, RLX_AGENT); }
__device__ __forceinline__ u64 ld_gran(char* ws, u32 off) { return __hip_atomic_load((GAS u64*)(ws + off), RLX_AGENT); }
__device__ __forceinline__ v4u ld2(__amdgpu_buffer_rsrc_t rs, u32 off) { return __builtin_amdgcn_raw_buffer_load_b128(rs, off, 0, 16); }
__device__ __forceinline__ bool spin(u32& n) { asm volatile("s_sleep 1" ::: "memory"); return ++n > SPIN_CAP; }
__device__ __forceinline__ bool spinl(u32& n) { asm volatile("s_sleep 6" ::: "memory"); return ++n > SPIN_CAP; }
__device__ __forceinline__ void sleep_until(u64 t0, u32 cyc) {
  _Pragma("nounroll") for (u32 n = 0; n < 4096 && __builtin_amdgcn_s_memtime() - t0 < cyc; ++n) asm volatile("s_sleep 2" ::: "memory");
}
__device__ __forceinline__ void lds_add(LAS char* sm, u32 off, int lane) {
  asm volatile("s_waitcnt lgkmcnt(0)" ::: "memory");
  if (lane == 0) __hip_atomic_fetch_add((LAS u32*)(sm + off), 1u, RLX_WG);
}
__device__ __forceinline__ void lds_wait(LAS char* sm, u32 off, u32 want) {
  for (u32 n = 0; n < (SPIN_CAP << 4); ++n) {
    if ((u32)__builtin_amdgcn_readfirstlane(__hip_atomic_load((LAS u32*)(sm + off), RLX_WG)) >= want) break;
  }
  asm volatile("" ::: "memory");
}
__device__ __forceinline__ v4u gather_qa(__amdgpu_buffer_rsrc_t rs, int xcd, int b, int lane, u32 tag) {
  v4u r;
  _Pragma("nounroll") for (u32 n = 0;;) {
    v4u q[16]; bool ok = true;
#pragma unroll
    for (int k = 0; k < 16; ++k) q[k] = ld2(rs, QA_OFF + (u32)(((16 * xcd + k) * 256 + b * 128 + 2 * lane) * 8));
#pragma unroll
    for (int k = 0; k < 16; ++k) ok &= (q[k].y == tag) & (q[k].w == tag);
#pragma unroll
    for (int hh = 0; hh < 4; ++hh)
      r[hh] = cvtpk((asf(q[4 * hh].x) + asf(q[4 * hh + 1].x)) + (asf(q[4 * hh + 2].x) + asf(q[4 * hh + 3].x)),
                    (asf(q[4 * hh].z) + asf(q[4 * hh + 1].z)) + (asf(q[4 * hh + 2].z) + asf(q[4 * hh + 3].z)));
    if (__all(ok) || spinl(n)) break;
  }
  return r;
}
__device__ __forceinline__ void put_qa(LAS char* sm, v4u r, int lane) {
#pragma unroll
  for (int hh = 0; hh < 4; ++hh) *(LAS u32*)(sm + L_QIMG + hh * 256 + lane * 4) = r[hh];
  lds_add(sm, F_Q, lane);
}
__device__ __forceinline__ void final_reduce(LAS char* sm, u32 flag, int phase, char* ws, u64 pOut, int which, int idx, int lane, u32 tag) {
  const u32 yo = YG_OFF + (u32)((which * 128 + idx) * 256 + lane) * 8;
  const u32 oo = (u32)(((idx >> 6) * 1024 + which * 512 + (idx & 63) * 8 + (lane & 7)) * 2);
  if (phase >= YPOLL) return;
  _Pragma("nounroll") for (int k = 0; k < phase; ++k) asm volatile("s_sleep %0" :: "i"(STAG) : "memory");
  u64 y0, y1, y2, y3;
  _Pragma("nounroll") for (u32 n = 0;;) {
    y0 = ld_gran(ws, yo); y1 = ld_gran(ws, yo + 512); y2 = ld_gran(ws, yo + 1024); y3 = ld_gran(ws, yo + 1536);
    if (__all(((u32)(y0 >> 32) == tag) & ((u32)(y1 >> 32) == tag) & ((u32)(y2 >> 32) == tag) & ((u32)(y3 >> 32) == tag)) || ++n > SPIN_CAP) break;
    if (__builtin_amdgcn_readfirstlane(__hip_atomic_load((LAS u32*)(sm + flag), RLX_WG))) return;
    asm volatile("s_sleep %0" :: "i"(YSLEEP) : "memory");
  }
  float x = (asf((u32)y0) + asf((u32)y1)) + (asf((u32)y2) + asf((u32)y3));
  x += dpp<0x128>(x);
  x = x32sum(x16sum(x));
  if (lane < 8) *(GAS __bf16*)(pOut + oo) = (__bf16)x;
  if (lane == 0) __hip_atomic_store((LAS u32*)(sm + flag), 1u, RLX_WG);
}

__global__ void __launch_bounds__(576, 1)
fused_decode(const u16* __restrict__ Bwq, const u16* __restrict__ Bc, char* ws, const u16* __restrict__ Bx, const u16* __restrict__ Ax, const u16* __restrict__ Awq,
             const u16* __restrict__ Bwuk, const u16* __restrict__ Ak, const u16* __restrict__ Av, const u16* __restrict__ Awo, const u16* __restrict__ Bwuv,
             const u16* __restrict__ Bwo, u16* __restrict__ out) {
  __shared__ __attribute__((aligned(16))) char smem[163840];
  u64 pc0; asm volatile("s_getpc_b64 %0" : "=s"(pc0));
  LAS char* sm = (LAS char*)smem;
  const u32 sm0 = (u32)(size_t)sm;
  u64 st[24];
#pragma unroll
  for (int k = 0; k < 24; ++k) st[k] = 0;
  STAMP(0);
  const int tid = threadIdx.x, lane = tid & 63, w = __builtin_amdgcn_readfirstlane(tid >> 6), g = lane >> 4, c = lane & 15;
  const int bid = blockIdx.x, xcd = bid & 7, isB = bid >> 7, jj = (bid >> 3) & 15, qt = jj & 3, h = 4 * xcd + (jj >> 2);
  u64 pAx = (u64)Ax, pAk = (u64)Ak, pAv = (u64)Av, pAwq = (u64)Awq, pAwo = (u64)Awo, pBx = (u64)Bx, pBc = (u64)Bc, pBwq = (u64)Bwq, pBwuk = (u64)Bwuk, pBwuv = (u64)Bwuv, pBwo = (u64)Bwo, pOut = (u64)out;
  const u32 ring = sm0 + w * 16384, ringo = w * 16384;
  const u32 wrow = (u32)(16 * w * 8192 + h * 256);
  const u32 wo0 = g * 8192 + c * 16, wo1 = wo0 + 31744, wo2 = wo0 + 2 * 31744, wo3 = wo0 + 3 * 31744, lo16 = lane * 16;
  const int bB = xcd >> 2, sB = (xcd & 3) * 16 + jj;
  const u64 cb = pBc + (u32)((bB * SK + sB * 128 + 4 * w) * 1024);
  const u32 co0 = lo16 ^ ((w & 3) * 16), cl = sm0 + w * 4096;
  if (w < 8) {
    asm volatile("s_nop 4" ::: "memory");
    dma4<false, false, false>(isB ? cb : pBwq + (wrow + (u32)qt * (128 * 8192)), isB ? co0 : wo0, isB ? co0 ^ 64 : wo1, isB ? co0 ^ 128 : wo2, isB ? co0 ^ 192 : wo3, isB ? cl : ring + 4096);
  }
  STAMP(21);
  asm volatile("" : "+s"(pAx), "+s"(pAk), "+s"(pAv), "+s"(pAwo), "+s"(pBx), "+s"(pAwq), "+s"(pBwuk), "+s"(pBwuv), "+s"(pBwo), "+s"(pOut));
  u32 tag = *(const __attribute__((address_space(4))) u32*)(u64)ws;
  const __amdgpu_buffer_rsrc_t rs = __builtin_amdgcn_make_buffer_rsrc(ws, 0, (int)WS_USED, 0x00020000);
  const int isT = isB & ~jj & 1;
  const int bA = jj >> 3, sA = isB ? 8 + ((jj >> 1) & 3) : (jj & 7);

  if (w == 8) {
    const u64 ht0 = __builtin_amdgcn_s_memtime();
    asm volatile("" : "+s"(tag)); tag += 1u;
    __builtin_amdgcn_s_setprio(3);
    if (CODE_KB && jj == 0) {
      const u64 pc = pc0 & ~255ull;
      v4u acc = {0u, 0u, 0u, 0u};
#pragma unroll
      for (int k = 0; k < CODE_KB; ++k) acc ^= *(const GAS v4u*)(pc + (u32)(k * 1024 + lo16));
      asm volatile("" :: "v"(acc));
    }
    { const u32 touch = *(volatile GAS u32*)pOut; asm volatile("" :: "v"(touch)); }
    if (!isB) {
      if (lane < 16) *(LAS u32*)(sm + L_FLAG + lane * 4) = 0u;
      BAR(); BAR();
      sleep_until(ht0, QA_T);
      put_qa(sm, gather_qa(rs, xcd, bA, lane, tag), lane);
      STAMP(12);
    } else {
      BAR();
      v4u qa = {0u, 0u, 0u, 0u};
      if (!isT) { sleep_until(ht0, QA_T); qa = gather_qa(rs, xcd, bA, lane, tag); STAMP(12); }
      if (SENTINEL) {
        const u32 so = QG_OFF + (u32)bB * 65536 + (u32)((8 * (lane >> 5) + 7) * 32 + (lane & 31)) * 64;
        _Pragma("nounroll") for (u32 n = 0;;) { const u64 x = ld_gran(ws, so), y = ld_gran(ws, so + 8 * 4096); if (__all(((u32)(x >> 32) == tag) & ((u32)(y >> 32) == tag)) || spin(n)) break; }
      }
      STAMP(16);
      BAR(); BAR(); BAR(); BAR();
      if (lane < 16) *(LAS u32*)(sm + L_FLAG + lane * 4) = 0u;
      BAR(); BAR(); BAR();
      if (!isT) put_qa(sm, qa, lane);
      lds_wait(sm, F_DRAIN, 8);
      if (lane < 32) st_gran(ws, LB_OFF + (u32)((bB * 64 + sB) * 32 + lane) * 8, *(LAS u32*)(sm + L_LSE + lane * 4), tag);
      STAMP(15);
      if (isT) {
        u64 l0, l1;
        _Pragma("nounroll") for (u32 n = 0;;) {
          l0 = ld_gran(ws, LB_OFF + (u32)(lane * 32 + h) * 8); l1 = ld_gran(ws, LB_OFF + (u32)((64 + lane) * 32 + h) * 8);
          if (__all(((u32)(l0 >> 32) == tag) & ((u32)(l1 >> 32) == tag)) || spin(n)) break;
        }
        const float f0 = asf((u32)l0), f1 = asf((u32)l1);
        const float e0 = ex2(f0 - x32max(x16max(row16max(f0)))), e1 = ex2(f1 - x32max(x16max(row16max(f1))));
        *(LAS __bf16*)(sm + L_LBV + lane * 2) = (__bf16)(e0 * rcp(x32sum(x16sum(row16sum(e0)))));
        *(LAS __bf16*)(sm + L_LBV + 128 + lane * 2) = (__bf16)(e1 * rcp(x32sum(x16sum(row16sum(e1)))));
        lds_add(sm, F_LB, lane);
      }
    }
    STAMP_OUT();
    return;
  }

  const u32 sw = (c ^ g) * 16;
  const u32 ko0 = g * 256 + sw, ko1 = g * 256 + (sw ^ 64), ko2 = g * 256 + (sw ^ 128), ko3 = g * 256 + (sw ^ 192);
  const int nt = isB ? NX : NA, T = (nt >> 3) + (w < (nt & 7));
  const u32 kv_off = (u32)((bA * 8 + xcd) * SK + 16 * ((isB ? 8 * NA + NX * (sA - 8) : NA * sA) + w)) * 256;
  u64 kb = pAk + kv_off, vb = pAv + kv_off, fb = pAwo + (u32)((128 * h + 16 * w) * 1024 + qt * 256);
  asm volatile("" : "+s"(kb), "+s"(vb), "+s"(fb));
  const int tb = (tid >> 5) & 1, tcol = 32 * ((tid >> 6) & 3) + (tid & 31), tr = (tid & 0xE0) | ((tid & 7) << 2) | ((tid >> 3) & 3);
  const Lane L = make_lane(c);

  if (!isB) {
    {
      const u64 src = pBx + (u32)(((lane >> 1) & 1) * 512 + 128 * ((lane >> 2) & 3) + 16 * w + 8 * (lane & 1)) * 2;
      asm volatile("s_mov_b32 m0, %1\n\ts_nop 4\n\tglobal_load_lds_dwordx4 %0, off" :: "v"(src), "s"(sm0 + L_XS + w * 1024) : "memory");
    }
    const u64 wq_b = pBwq + wrow;
    const u32 xso = L_XS + w * 1024 + (c >> 3) * 32 + g * 8;
    f32x4 acc = ZERO4;
    v4u W[4];
    asm volatile("" : "+s"(tag)); tag += 1u;
    dma4<false, true, false>(pBwuk + (wrow + (u32)qt * (128 * 8192)), g * 8192 + sw, g * 8192 + (sw ^ 64) + 31744, g * 8192 + (sw ^ 128) + 2 * 31744, g * 8192 + (sw ^ 192) + 3 * 31744, ring + 3 * 4096);
    VMWAIT(4);
    STAMP(13);
    dma4<false, false, false>(wq_b + (u32)(((qt + 1) & 3) * (128 * 8192)), wo0, wo1, wo2, wo3, ring + 2 * 4096);
    dma4<false, false, false>(wq_b + (u32)(((qt + 2) & 3) * (128 * 8192)), wo0, wo1, wo2, wo3, ring);
    ldsW(W, sm, ringo + 4096, g, c); acc = gemv4(W, *(LAS v2u*)(sm + xso + qt * 64), L, acc);
    dma4<false, false>(wq_b + (u32)(((qt + 3) & 3) * (128 * 8192)), wo0, wo1, wo2, wo3, ring + 4096);
    VMWAIT(8);
    STAMP(14);
    ldsW(W, sm, ringo + 2 * 4096, g, c); acc = gemv4(W, *(LAS v2u*)(sm + xso + ((qt + 1) & 3) * 64), L, acc);
    VMWAIT(4);
    ldsW(W, sm, ringo, g, c); acc = gemv4(W, *(LAS v2u*)(sm + xso + ((qt + 2) & 3) * 64), L, acc);
    dma4<true, true>(kb, ko0, ko1, ko2, ko3, ring);
    ldsW(W, sm, ringo + 4096, g, c); acc = gemv4(W, *(LAS v2u*)(sm + xso + ((qt + 3) & 3) * 64), L, acc);
    *(LAS f32x4*)(sm + L_PART + w * 1024 + lo16) = acc;
    STAMP(1);
    BAR();
    STAMP(16);
    if (tid < 256) *(LAS __bf16*)(sm + L_XBUF + (tb * 128 + tcol) * 2) = (__bf16)reduce8<1024>(sm, L_PART + tr * 4);
    BAR();
    STAMP(17);
    {
      f32x4 qa = ZERO4;
#pragma unroll
      for (int k = 0; k < 4; ++k)
        qa = mfma16(*(LAS v4u*)(sm + ringo + 3 * 4096 + c * 256 + ((4 * k + g) ^ c) * 16), *(LAS v4u*)(sm + L_XBUF + (c & 1) * 256 + k * 64 + g * 16), qa);
      if (c < 2) {
        __builtin_amdgcn_raw_buffer_store_b128(v4u{cvtpk(qa.x, qa.y), tag, cvtpk(qa.z, qa.w), tag}, rs, QG_OFF + (u32)c * 65536 + (u32)(((8 * qt + w) * 32 + h) * 4 + g) * 16, 0, 16);
      }
    }
    STAMP(2);
  } else {
    const int h2 = lane >> 5, hd = lane & 31;
    v2u xs;
    asm volatile("global_load_dwordx2 %0, %1, %2" : "=v"(xs) : "v"((u32)(((c >> 3) * 512 + 128 * qt + 16 * w + 4 * g) * 2)), "s"(pAx) : "memory");
    dma4<true, true, false>(cb + 32768, co0, co0 ^ 64, co0 ^ 128, co0 ^ 192, cl + 32768);
    asm volatile("" : "+v"(xs));
    asm volatile("" : "+s"(tag)); tag += 1u;
    dma4<true, true, false>(pAwq + (wrow + (u32)qt * (128 * 8192)), wo0, wo1, wo2, wo3, sm0 + EXTRA + w * 4096);
    dma4<true, true, false>(cb + 2 * 32768, co0, co0 ^ 64, co0 ^ 128, co0 ^ 192, cl + 2 * 32768);
    const int half = (jj >> 1) & 1;
    const u64 ub = isT ? pBwuv + (u32)((256 * half + 32 * w) * 8192 + h * 256) : kb;
    const u32 uo = isT ? g * 32768 + c * 16 : ko0, us = isT ? 8192 : 1024, ux = isT ? 0 : 64;
    v4u UW[8], FW[8];
#pragma unroll
    for (int i = 0; i < 4; ++i) UW[i] = __builtin_nontemporal_load((const GAS v4u*)(ub + (u32)((uo ^ (ux * i)) + us * i)));
    {
      v4u W[4];
      ldsW(W, sm, EXTRA + w * 4096, g, c);
      const f32x4 acc = gemv4(W, xs, L, ZERO4);
      *(LAS f32x4*)(sm + EXTRA + w * 4096 + lo16) = acc;
    }
    BAR();
    if (tid < 256) st_gran(ws, QA_OFF + (u32)((h * 4 + qt) * 256 + tb * 128 + tcol) * 8, asu(reduce8<4096>(sm, EXTRA + tr * 4)), tag);
    STAMP(14);
    dma4<true, true, false>(cb + 3 * 32768, co0, co0 ^ 64, co0 ^ 128, co0 ^ 192, cl + 3 * 32768);
    BAR();
    const u64 ub2 = isT ? ub + 16 * 8192 : vb, fb1 = isT ? pBwo + (u32)((128 * h + 16 * w) * 1024 + half * 512) : kb + 32768, fb2 = fb1 + 256;
    const u32 uo2 = isT ? uo : lo16, fo1 = isT ? g * 4096 + c * 16 : ko0, fo2 = fo1;
#define LOAD_U2() { _Pragma("unroll") for (int i = 0; i < 4; ++i) UW[4 + i] = __builtin_nontemporal_load((const GAS v4u*)(ub2 + (u32)(uo2 + us * i))); }
#define LOAD_F1() { _Pragma("unroll") for (int i = 0; i < 4; ++i) FW[i] = __builtin_nontemporal_load((const GAS v4u*)(fb1 + (u32)((fo1 ^ (ux * i)) + 1024 * i))); }
#define LOAD_F2() if (isT) { _Pragma("unroll") for (int i = 0; i < 4; ++i) FW[4 + i] = __builtin_nontemporal_load((const GAS v4u*)(fb2 + (u32)(fo2 + 1024 * i))); }
#define LOAD_AT(P) { if (XPOS1 == P) LOAD_U2(); if (XPOS2 == P) LOAD_F1(); if (XPOS3 == P) LOAD_F2(); asm volatile("" ::: "memory"); }
    {
      const u32 qg = QG_OFF + (u32)bB * 65536 + (u32)tid * 16, ql = L_QB + (tid >> 7) * 1024 + ((tid >> 1) & 1) * 512 + ((tid >> 2) & 31) * 16 + (tid & 1) * 8;
      v4u q[8]; bool ok = true;
#pragma unroll
      for (int k = 0; k < 8; ++k) q[k] = ld2(rs, qg + ((k + jj) & 7) * 8192);
      __builtin_amdgcn_sched_barrier(0);
      __builtin_amdgcn_s_barrier();
      __builtin_amdgcn_sched_barrier(0);
      if (XPOS1 == 0) LOAD_U2(); if (XPOS2 == 0) LOAD_F1(); if (XPOS3 == 0) LOAD_F2();
      __builtin_amdgcn_sched_barrier(0);
#pragma unroll
      for (int k = 0; k < 8; ++k) { ok &= (q[k].y == tag) & (q[k].w == tag); *(LAS v2u*)(sm + ql + ((k + jj) & 7) * 4096) = v2u{q[k].x, q[k].z}; }
      _Pragma("nounroll") for (u32 n = 0; !__all(ok) && !spinl(n);) {
        ok = true;
#pragma unroll 1
        for (int k = 0; k < 8; ++k) { const v4u x = ld2(rs, qg + k * 8192); ok &= (x.y == tag) & (x.w == tag); *(LAS v2u*)(sm + ql + k * 4096) = v2u{x.x, x.z}; }
      }
    }
    STAMP(1);
    BAR();
    STAMP(8);
    LOAD_AT(1)
    f32x16 sc;
#pragma unroll
    for (int k = 0; k < 16; ++k) sc[k] = 0.f;
    float mw = 0.f;
    if (w < 4) {
      const u32 ca = (32 * w + hd) * 1024, cx = (h2 ^ (((hd & 3) << 2) | ((hd >> 2) & 3))) * 16;
      u32 qo = L_QB + lo16;
      asm volatile("" : "+v"(qo));
      v4u fa[3][2], fq[3][2];
#define QK_LOAD(B) _Pragma("unroll") for (int k = 0; k < 2; ++k) { fa[(B) % 3][k] = *(LAS v4u*)(sm + ca + ((B) >> 2) * 256 + (cx ^ ((2 * ((B) & 3) + k) * 32))); fq[(B) % 3][k] = *(LAS v4u*)(sm + qo + (2 * (B) + k) * 1024); }
      QK_LOAD(0) QK_LOAD(1)
#pragma unroll
      for (int bb = 0; bb < 16; ++bb) {
        if (bb < 14) { QK_LOAD(bb + 2) }
        __builtin_amdgcn_sched_barrier(0);
#pragma unroll
        for (int k = 0; k < 2; ++k) sc = mfma32(fa[bb % 3][k], fq[bb % 3][k], sc);
        __builtin_amdgcn_sched_barrier(0);
      }
#undef QK_LOAD
      mw = sc[0];
#pragma unroll
      for (int k = 1; k < 16; ++k) mw = fmaxf(mw, sc[k]);
      mw = x32max(mw);
    }
    STAMP(2);
    BAR();
    LOAD_AT(2)
    if (w < 4 && lane < 32) *(LAS float*)(sm + L_BM + hd * 16 + w * 4) = mw;
    BAR();
    if (w < 4) {
      const float M = C2 * max4(*(LAS f32x4*)(sm + L_BM + hd * 16));
      float ls = 0.f;
#pragma unroll
      for (int k = 0; k < 16; ++k) { sc[k] = ex2(fmaf(sc[k], C2, -M)); ls += sc[k]; }
      *(LAS v4u*)(sm + L_PF + w * 2048 + lo16) = v4u{cvtpk(sc[0], sc[1]), cvtpk(sc[2], sc[3]), cvtpk(sc[4], sc[5]), cvtpk(sc[6], sc[7])};
      *(LAS v4u*)(sm + L_PF + w * 2048 + 1024 + lo16) = v4u{cvtpk(sc[8], sc[9]), cvtpk(sc[10], sc[11]), cvtpk(sc[12], sc[13]), cvtpk(sc[14], sc[15])};
      ls = x32sum(ls);
      if (lane < 32) *(LAS float*)(sm + L_BL + hd * 16 + w * 4) = ls;
    }
    BAR();
    STAMP(11);
    LOAD_AT(3)
    f32x16 oa, ob;
#pragma unroll
    for (int k = 0; k < 16; ++k) { oa[k] = 0.f; ob[k] = 0.f; }
    {
      const int q = (lane >> 2) & 3, p = lane & 3, blk = (lane >> 4) & 1;
      const u32 base = (4 * h2 + q) * 1024 + (w >> 1) * 256 + 8 * (p & 1), lo4 = 8 * (w & 1) + 2 * blk + (p >> 1);
      const u32 a00 = base + 16 * (lo4 ^ ((q << 2) | h2)), a01 = base + 16 * ((lo4 + 4) ^ ((q << 2) | h2));
      const u32 a10 = base + 8192 + 16 * (lo4 ^ ((q << 2) | (2 + h2))), a11 = base + 8192 + 16 * ((lo4 + 4) ^ ((q << 2) | (2 + h2)));
      u32 po = L_PF + lo16;
      asm volatile("" : "+v"(po));
      v4u pb[2]; v2u tt[2][4];
#define TRR(A) __builtin_bit_cast(v2u, __builtin_amdgcn_ds_read_tr16_b64_v4i16((LAS s16x4*)(sm + (A))))
#define PV_LOAD(K, S) { pb[S] = *(LAS v4u*)(sm + po + (K) * 1024); tt[S][0] = TRR(a00 + (K) * 16384); tt[S][1] = TRR(a10 + (K) * 16384); tt[S][2] = TRR(a01 + (K) * 16384); tt[S][3] = TRR(a11 + (K) * 16384); }
      PV_LOAD(0, 0)
#pragma unroll
      for (int ks = 0; ks < 8; ++ks) {
        if (ks < 7) PV_LOAD(ks + 1, (ks + 1) & 1)
        __builtin_amdgcn_sched_barrier(0);
        oa = mfma32(v4u{tt[ks & 1][0].x, tt[ks & 1][0].y, tt[ks & 1][1].x, tt[ks & 1][1].y}, pb[ks & 1], oa);
        ob = mfma32(v4u{tt[ks & 1][2].x, tt[ks & 1][2].y, tt[ks & 1][3].x, tt[ks & 1][3].y}, pb[ks & 1], ob);
        __builtin_amdgcn_sched_barrier(0);
      }
#undef PV_LOAD
#undef TRR
    }
    STAMP(3);
    {
      const float Ls = sum4(*(LAS f32x4*)(sm + L_BL + hd * 16));
      const float inv = rcp(Ls);
      if (tid < 32) *(LAS float*)(sm + L_LSE + tid * 4) = fmaf(C2, max4(*(LAS f32x4*)(sm + L_BM + hd * 16)), __builtin_amdgcn_logf(Ls));
      const u32 so = hd * 1024 + (w >> 1) * 256 + 8 * h2, sx = (8 * (w & 1)) ^ (((hd & 3) << 2) | ((hd >> 2) & 3));
#pragma unroll
      for (int k = 0; k < 4; ++k) {
        *(LAS v2u*)(sm + so + (sx ^ k) * 16) = v2u{cvtpk(oa[4 * k] * inv, oa[4 * k + 1] * inv), cvtpk(oa[4 * k + 2] * inv, oa[4 * k + 3] * inv)};
        *(LAS v2u*)(sm + so + (sx ^ (4 + k)) * 16) = v2u{cvtpk(ob[4 * k] * inv, ob[4 * k + 1] * inv), cvtpk(ob[4 * k + 2] * inv, ob[4 * k + 3] * inv)};
      }
      asm volatile("s_waitcnt lgkmcnt(0)" ::: "memory");
#pragma unroll
      for (int i = 0; i < 4; ++i) {
        const int hh = 8 * i + (lane >> 3);
        const v4u v = *(LAS v4u*)(sm + hh * 1024 + (w >> 1) * 256 + ((8 * (w & 1) + (lane & 7)) ^ (((hh & 3) << 2) | ((hh >> 2) & 3))) * 16);
        __builtin_amdgcn_raw_buffer_store_b128(v, rs, PB_OFF + (u32)(((hh * 2 + (w >> 2)) * 2 + bB) * 64 + sB) * 512 + (w & 3) * 128 + (lane & 7) * 16, 0, 16);
      }
    }
    STAMP(13);
    BAR();
    if (isT) {
      VMWAIT(0);
      lds_add(sm, F_DRAIN, lane);
      lds_wait(sm, F_LB, 1);
      STAMP(17);
      v4u X[8];
#pragma unroll
      for (int i = 0; i < 8; ++i) X[i] = ld2(rs, PB_OFF + (u32)((h * 2 + half) * 128 + 16 * w + 4 * g + (i & 3)) * 512 + (i >> 2) * 256 + c * 16);
      v2u xw = *(LAS v2u*)(sm + L_LBV + w * 32 + g * 8);
      if ((c >> 3) != (w >> 2)) xw = v2u{0u, 0u};
      *(LAS f32x4*)(sm + L_PART + w * 1024 + lo16) = gemv4(X, xw, L, ZERO4);
      *(LAS f32x4*)(sm + L_PART2 + w * 1024 + lo16) = gemv4(X + 4, xw, L, ZERO4);
      const u32 po = (tid < 256 ? L_PART : L_PART2) + tr * 4;
      STAMP(4);
      BAR();
      *(LAS __bf16*)(sm + L_XBUF + (tb * 256 + (tid >> 8) * 128 + tcol) * 2) = (__bf16)reduce8<1024>(sm, po);
      BAR();
      const u32 xo = L_XBUF + (c >> 3) * 512 + w * 64 + g * 8;
      *(LAS f32x4*)(sm + L_PART + w * 1024 + lo16) = gemv4(UW + 4, *(LAS v2u*)(sm + xo + 32), L, gemv4(UW, *(LAS v2u*)(sm + xo), L, ZERO4));
      BAR();
      if (tid < 256) {
        const float own = reduce8<1024>(sm, po);
        st_gran(ws, CX_OFF + (u32)((h * 2 + half) * 256 + tid) * 8, asu(own), tag);
        STAMP(19);
        u64 x;
        _Pragma("nounroll") for (u32 n = 0;;) { x = ld_gran(ws, CX_OFF + (u32)((h * 2 + (half ^ 1)) * 256 + tid) * 8); if (__all((u32)(x >> 32) == tag) || spin(n)) break; }
        *(LAS __bf16*)(sm + L_XBUF + (tb * 128 + tcol) * 2) = (__bf16)(half ? asf((u32)x) + own : own + asf((u32)x));
        STAMP(20);
      }
      BAR();
      const v2u xc = *(LAS v2u*)(sm + L_XBUF + (c >> 3) * 256 + w * 32 + g * 8);
      *(LAS f32x4*)(sm + L_PART + w * 1024 + lo16) = gemv4(FW, xc, L, ZERO4);
      *(LAS f32x4*)(sm + L_PART2 + w * 1024 + lo16) = gemv4(FW + 4, xc, L, ZERO4);
      BAR();
      st_gran(ws, YG_OFF + (u32)((((2 + tb) * 64 + 32 * half + 16 * (tid >> 8) + (tcol >> 3)) * 32 + h) * 8 + (tcol & 7)) * 8, asu(reduce8<1024>(sm, po)), tag);
      STAMP(9);
      final_reduce(sm, F_OUT + (w & 1) * 4, w >> 1, ws, pOut, 1, (xcd * 8 + (jj >> 1)) * 2 + (w & 1), lane, tag); STAMP(10);
      STAMP_OUT();
      return;
    }
    dma4<false, true, false>(vb + 32768, lo16, lo16, lo16, lo16, ring + 3 * 4096);
#pragma unroll
    for (int i = 0; i < 8; ++i) *(LAS v4u*)(sm + ringo + i * 1024 + lo16) = UW[i];
#pragma unroll
    for (int i = 0; i < 4; ++i) *(LAS v4u*)(sm + ringo + 8192 + i * 1024 + lo16) = FW[i];
    VMWAIT(4);
    lds_add(sm, F_DRAIN, lane);
  }
  v4u WO[4];
  if (!isB) {
    dma4<true, true>(vb, lo16, lo16, lo16, lo16, ring + 4096);
    dma4<true, true>(kb + 32768, ko0, ko1, ko2, ko3, ring + 2 * 4096);
    dma4<true, true>(vb + 32768, lo16, lo16, lo16, lo16, ring + 3 * 4096);
    if (!FLATE) VMWAIT(2);
#pragma unroll
    for (int i = 0; i < 4; ++i) if (!FLATE) WO[i] = __builtin_nontemporal_load((const GAS v4u*)(fb + (u32)(g * 1024 + ((c ^ (4 * g) ^ i) * 16) + i * 4096)));
    asm volatile("" ::: "memory");
  }
  STAMP(4);
  lds_wait(sm, F_Q, 1);
  STAMP(5);
  float Mw;
  {
    v4u qf[4];
#pragma unroll
    for (int k = 0; k < 4; ++k) qf[k] = *(LAS v4u*)(sm + L_QIMG + (c & 3) * 256 + k * 64 + g * 16);
    const u32 pm0 = (c >> 2) == 0 ? 0x0000FFFFu : (c >> 2) == 1 ? 0xFFFF0000u : 0u, pm1 = (c >> 2) == 2 ? 0x0000FFFFu : (c >> 2) == 3 ? 0xFFFF0000u : 0u;
    float m = -INFINITY, l = 0.f;
    f32x4 o0 = ZERO4, o1 = ZERO4;
    const u32 kr = ringo + c * 256, vr = ringo + 4096 + g * 1024 + c * 16;
    v2u y[4];
#define QK_STEP(SO) { \
      const f32x4 sa = mfma16(*(LAS v4u*)(sm + kr + (SO) + ((8 + g) ^ c) * 16), qf[2], mfma16(*(LAS v4u*)(sm + kr + (SO) + ((0 + g) ^ c) * 16), qf[0], ZERO4)) \
                     + mfma16(*(LAS v4u*)(sm + kr + (SO) + ((12 + g) ^ c) * 16), qf[3], mfma16(*(LAS v4u*)(sm + kr + (SO) + ((4 + g) ^ c) * 16), qf[1], ZERO4)); \
      const float mn = fmaxf(m, x32max(x16max(max4(sa)))); \
      const float al = ex2((m - mn) * C2), mc = mn * C2; \
      m = mn; \
      const float p[4] = {ex2(fmaf(sa.x, C2, -mc)), ex2(fmaf(sa.y, C2, -mc)), ex2(fmaf(sa.z, C2, -mc)), ex2(fmaf(sa.w, C2, -mc))}; \
      l = fmaf(l, al, (p[0] + p[1]) + (p[2] + p[3])); \
      o0 *= al; o1 *= al; \
      _Pragma("unroll") for (int i = 0; i < 4; ++i) { const u32 pp = cvtpk(p[i], p[i]); y[i] = v2u{pp & pm0, pp & pm1}; } }
#define PV_STEP(SO) _Pragma("unroll") for (int i = 0; i < 4; ++i) { \
      const v4u V = *(LAS v4u*)(sm + vr + (SO) + i * 256); \
      o0 = mfma16h(v2u{V.x, V.y}, y[i], o0); o1 = mfma16h(v2u{V.z, V.w}, y[i], o1); }
#pragma unroll 1
    for (int t = -1; t < T - 1; ++t) {
      const u32 so = (t & 1) * 8192;
      if (t >= 0) {
        if (t + 2 < T) dma4<8, true>(kb + (u32)((t + 2) * 32768), ko0, ko1, ko2, ko3, ring + so);
        else VMWAIT(4);
        PV_STEP(so)
      }
      QK_STEP(so ^ 8192)
      if (t >= 0 && t + 2 < T) dma4<8, true>(vb + (u32)((t + 2) * 32768), lo16, lo16, lo16, lo16, ring + so + 4096);
    }
    if (lane < 4) *(LAS float*)(sm + L_MST + lane * 32 + w * 4) = m;
    lds_add(sm, F_M, lane);
    l = x32sum(x16sum(l));
    u32 mo = L_MRG + w * 2048 + (c & 3) * 512 + ((4 * g + (c >> 2)) ^ (2 * (c & 3))) * 16, lo = L_MST + 128 + lane * 32 + w * 4, so = ((T - 1) & 1) * 8192;
    asm volatile("" : "+v"(mo), "+v"(lo), "+v"(l));
    lds_wait(sm, F_M, 8);
    Mw = fmaxf(max4(*(LAS f32x4*)(sm + L_MST + (c & 3) * 32)), max4(*(LAS f32x4*)(sm + L_MST + (c & 3) * 32 + 16)));
    float f = ex2((m - Mw) * C2);
    asm volatile("" : "+v"(f), "+v"(Mw));
    VMWAIT(0);
    PV_STEP(so)
    STAMP(6);
    *(LAS f32x4*)(sm + mo) = o0 * f;
    *(LAS f32x4*)(sm + mo + 256) = o1 * f;
    if (lane < 4) *(LAS float*)(sm + lo) = l * f;
#undef QK_STEP
#undef PV_STEP
  }
  {
    u32 ro = L_MRG + w * 512 + ((lane & 31) ^ (2 * w)) * 16, go = OA_OFF + (u32)((4 * xcd + w) * 24 + bA * 12 + sA) * 640 + lane * 16;
    const float Mc = C2 * asf(__builtin_amdgcn_readlane(asu(Mw), w & 3));
    asm volatile("" : "+v"(ro), "+v"(go));
    BAR();
    if (w < 4) {
      STAMP(18);
      f32x4 v[8];
#pragma unroll
      for (int ww = 0; ww < 8; ++ww) v[ww] = *(LAS f32x4*)(sm + ro + ww * 2048);
      const f32x4 la = *(LAS f32x4*)(sm + L_MST + 128 + w * 32), lb = *(LAS f32x4*)(sm + L_MST + 128 + w * 32 + 16);
      __builtin_amdgcn_sched_barrier(0);
      const float Ls = sum4(la + lb), inv = rcp(Ls);
      const f32x4 a = (((v[0] + v[1]) + (v[2] + v[3])) + ((v[4] + v[5]) + (v[6] + v[7]))) * inv;
      const u32 p0 = lane < 32 ? cvtpk(a.x, a.y) : asu(Mc + __builtin_amdgcn_logf(Ls));
      if (lane < 40) __builtin_amdgcn_raw_buffer_store_b128(v4u{p0, tag, cvtpk(a.z, a.w), tag}, rs, go, 0, 16);
      STAMP(7);
    }
  }
  if (isB) { STAMP_OUT(); return; }
  if (!FLATE) {
#pragma unroll
    for (int i = 0; i < 4; ++i) *(LAS v4u*)(sm + w * 4096 + i * 1024 + lo16) = WO[i];
    lds_add(sm, F_FIN, lane);
  }
  if (w < 4) {
    const int ob = w & 1;
    const u32 ro = OA_OFF + (u32)(h * 24 + 12 * ob + g) * 640;
    v4u x[6]; u64 ls[3];
    if ((w >> 1) >= OPOLL) { STAMP_OUT(); return; }
    if (w >> 1) asm volatile("s_sleep %0" :: "i"(2 * STAG) : "memory");
    if (OSENT) {
      const u32 so = OA_OFF + (u32)(h * 24 + 12 * ob + (lane < 12 ? lane : 0)) * 640 + 512;
      _Pragma("nounroll") for (u32 n = 0;;) {
        const u64 z = ld_gran(ws, so);
        if (__builtin_popcount((u32)__ballot((u32)(z >> 32) == tag) & 0xFFFu) >= OTHR || ++n > SPIN_CAP) break;
        asm volatile("s_sleep %0" :: "i"(OSLEEP) : "memory");
      }
      STAMP(11);
    }
    _Pragma("nounroll") for (u32 n = 0;;) {
      bool ok = true;
#pragma unroll
      for (int k = 0; k < 3; ++k) { x[2 * k] = ld2(rs, ro + k * 2560 + c * 32); x[2 * k + 1] = ld2(rs, ro + k * 2560 + c * 32 + 16); ls[k] = ld_gran(ws, ro + k * 2560 + 512); }
#pragma unroll
      for (int k = 0; k < 3; ++k) ok &= (x[2 * k].y == tag) & (x[2 * k].w == tag) & (x[2 * k + 1].y == tag) & (x[2 * k + 1].w == tag) & ((u32)(ls[k] >> 32) == tag);
      if (__all(ok) || ++n > SPIN_CAP) break;
      if (__builtin_amdgcn_readfirstlane(__hip_atomic_load((LAS u32*)(sm + F_FIN + 8 + 4 * ob), RLX_WG))) { STAMP_OUT(); return; }
      asm volatile("s_sleep %0" :: "i"(OSLEEP) : "memory");
    }
    STAMP(8);
    const float M = x32max(x16max(fmaxf(fmaxf(asf((u32)ls[0]), asf((u32)ls[1])), asf((u32)ls[2]))));
    f32x4 oc = ZERO4; float lw = 0.f;
#pragma unroll
    for (int k = 0; k < 3; ++k) {
      const float e = ex2(asf((u32)ls[k]) - M);
      const u32 pp = cvtpk(e, e);
      lw += asf(pp << 16);
      oc = mfma16(v4u{x[2 * k].x, x[2 * k].z, x[2 * k + 1].x, x[2 * k + 1].z}, v4u{pp & L.m0, pp & L.m1, pp & L.m2, pp & L.m3}, oc);
    }
    const float inv = rcp(x32sum(x16sum(lw)));
    if (c < 8) {
      const u32 xo = L_XBUF + ob * 256 + (16 * ((c >> 1) & 1) + 8 * (c & 1) + 4 * (g >> 1) + (c >> 2)) * 2;
#pragma unroll
      for (int r = 0; r < 4; ++r) *(LAS __bf16*)(sm + xo + (32 * ((2 * (g & 1) + (r >> 1))) + 2 * (r & 1)) * 2) = (__bf16)(oc[r] * inv);
    }
    lds_add(sm, F_FIN + 8 + 4 * ob, lane);
    STAMP_OUT();
    return;
  }
  {
    const int q = (lane >> 2) & 3, p = lane & 3;
    if (FLATE) {
      const u32 f0 = g * 1024, fx = (c ^ (4 * g)) * 16, fl = sm0 + (w - 4) * 8192;
      const u64 fb2 = fb - (u32)((8 - w) * 16384);
      asm volatile("s_sleep %0" :: "i"(FSLEEP) : "memory");
      dma4<false, true, false>(fb2, f0 + fx, f0 + 3072 + (fx ^ 16), f0 + 2 * 3072 + (fx ^ 32), f0 + 3 * 3072 + (fx ^ 48), fl);
      dma4<false, true, false>(fb2 + 16384, f0 + fx, f0 + 3072 + (fx ^ 16), f0 + 2 * 3072 + (fx ^ 32), f0 + 3 * 3072 + (fx ^ 48), fl + 4096);
      VMWAIT(0);
      lds_add(sm, F_FIN, lane);
    }
    lds_wait(sm, F_FIN, FLATE ? 4 : 8);
    v4u wt[4][2];
#pragma unroll
    for (int k = 0; k < 4; ++k) {
      const u32 ra = (32 * k + 8 * g + q) * 256 + 8 * (p & 1);
#pragma unroll
      for (int e = 0; e < 2; ++e) {
        const u32 ch = 4 * (w - 4) + 2 * e + (p >> 1);
        const v2u t0 = __builtin_bit_cast(v2u, __builtin_amdgcn_ds_read_tr16_b64_v4i16((LAS s16x4*)(sm + ra + (ch ^ ((q << 2) | (2 * (g & 1)))) * 16)));
        const v2u t1 = __builtin_bit_cast(v2u, __builtin_amdgcn_ds_read_tr16_b64_v4i16((LAS s16x4*)(sm + ra + 1024 + (ch ^ ((q << 2) | (2 * (g & 1) + 1))) * 16)));
        wt[k][e] = v4u{t0.x, t0.y, t1.x, t1.y};
        asm volatile("" : "+v"(wt[k][e]));
      }
    }
    u32 xa = L_XBUF + (c & 1) * 256 + g * 16, yo = YG_OFF + (u32)((((g & 1) * 64 + 16 * qt + 4 * (w - 4) + 2 * (g >> 1) + (c >> 3)) * 32 + h) * 8 + (c & 7)) * 8;
    asm volatile("" : "+v"(xa), "+v"(yo));
    _Pragma("nounroll") for (u32 n = 0; n < (SPIN_CAP << 4); ++n) {
      const u64 f12 = __hip_atomic_load((LAS u64*)(sm + F_FIN + 8), RLX_WG);
      if (__builtin_amdgcn_readfirstlane(((u32)f12 > 0) & ((u32)(f12 >> 32) > 0))) break;
    }
    asm volatile("" ::: "memory");
    STAMP(19);
    v4u xo[4];
#pragma unroll
    for (int k = 0; k < 4; ++k) xo[k] = *(LAS v4u*)(sm + xa + k * 64);
    __builtin_amdgcn_sched_barrier(0);
    f32x4 y0 = ZERO4, y1 = ZERO4;
#pragma unroll
    for (int k = 0; k < 4; ++k) { y0 = mfma16(xo[k], wt[k][0], y0); y1 = mfma16(xo[k], wt[k][1], y1); }
    st_gran(ws, yo, asu(g == 0 ? y0.x : g == 1 ? y0.y : g == 2 ? y1.x : y1.y), tag);
  }
  STAMP(9);
  final_reduce(sm, F_OUT, w - 4, ws, pOut, 0, bid, lane, tag);
  if (bid == 0 && w == 4 && lane == 0) *(GAS u32*)ws = tag;
  STAMP(10);
  if (w == 4) { const u64 until = __builtin_amdgcn_s_memrealtime() + 100 * (st[21] - st[0]); while (__builtin_amdgcn_s_memrealtime() < until) __builtin_amdgcn_s_sleep(4); }
}

extern "C" void kernel_launch(void* const* d_in, const int* in_sizes, int n_in, void* d_out, int out_size, void* d_ws, size_t ws_size, hipStream_t stream) {
  fused_decode<<<256, 576, 0, stream>>>((const u16*)d_in[7], (const u16*)d_in[6], (char*)d_ws, (const u16*)d_in[5], (const u16*)d_in[0], (const u16*)d_in[3],
                                        (const u16*)d_in[8], (const u16*)d_in[1], (const u16*)d_in[2], (const u16*)d_in[4], (const u16*)d_in[9],
                                        (const u16*)d_in[10], (u16*)d_out);
}
```

```cpp
#include <hip/hip_runtime.h>
#include <stdint.h>

typedef unsigned short u16;
typedef unsigned u32;
typedef unsigned long long u64;
#define GAS __attribute__((address_space(1)))
#define LAS __attribute__((address_space(3)))
typedef u32 v4u __attribute__((ext_vector_type(4)));
typedef u32 v2u __attribute__((ext_vector_type(2)));
typedef float f32x2 __attribute__((ext_vector_type(2)));
typedef float f32x4 __attribute__((ext_vector_type(4)));
typedef float f32x16 __attribute__((ext_vector_type(16)));
typedef __bf16 bf16x8 __attribute__((ext_vector_type(8)));
typedef __bf16 bf16x2 __attribute__((ext_vector_type(2)));
typedef short s16x4 __attribute__((ext_vector_type(4)));
#define RLX_AGENT __ATOMIC_RELAXED, __HIP_MEMORY_SCOPE_AGENT
#define RLX_WG __ATOMIC_RELAXED, __HIP_MEMORY_SCOPE_WORKGROUP

#define TILES_A 45
constexpr int SK = 8192;
#define TILES_T 8
constexpr int NA = TILES_A, NT = TILES_T, NX = 128 - 2 * NA - NT;
static_assert(8 * NA + 4 * NX + 4 * NT == 512 && NX >= 16 && NA >= 16 && NT == 8, "tile split");
constexpr float C2 = 0.08838834764831845f * 1.4426950408889634f;
constexpr u32 SPIN_CAP = 1u << 17;

constexpr u32 QG_OFF = 4096;
constexpr u32 QA_OFF = QG_OFF + 131072;
constexpr u32 OA_OFF = QA_OFF + 262144;
constexpr u32 LB_OFF = OA_OFF + 32 * 32 * 640;
constexpr u32 CX_OFF = LB_OFF + 32768;
constexpr u32 YG_OFF = CX_OFF + 131072;
constexpr u32 PB_OFF = YG_OFF + 524288;
constexpr u32 WS_USED = PB_OFF + 4194304;
#define STAMP(k)
#define STAMP_OUT()

constexpr u32 EXTRA = 131072;
constexpr u32 L_PART = EXTRA, L_XBUF = EXTRA + 8192, L_FLAG = EXTRA + 15040, L_MST = EXTRA + 15104, L_QIMG = EXTRA + 15360, L_MRG = EXTRA + 16384, L_XS = EXTRA + 16384;
constexpr u32 L_QB = EXTRA, L_PF = EXTRA, L_BM = EXTRA + 9216, L_BL = EXTRA + 10240, L_LSE = EXTRA + 11264, L_PARTB = 0;
constexpr u32 F_Q = L_FLAG, F_LB = L_FLAG + 4, F_DRAIN = L_FLAG + 8, F_M = L_FLAG + 12, F_OUT = L_FLAG + 16, F_FIN = L_FLAG + 32, L_LBV = EXTRA + 11392;
#define CODE_KB 21
#define XPOS1 -1
#define XPOS2 0
#define XPOS3 2
#define OPOLL 1
#define OSENT 1
#define OSLEEP 1
#define YPOLL 1
#define YSLEEP 1
#define OTHR 14
#define FPOS 0
static_assert(OSENT == 1, "F_FIN accounting");
#define FSLEEP 4
#define QS_T 17000
#define OSTAG 9
#define UPOS 3
#define LSLEEP 1
#define LDELAY 40
#define YDELAY 15
#define ODELAY 0
#define CDELAY 0
#define SDELAY 0
#define STAG 9
#define QA_T 14000
#define SENTINEL 1

__device__ __forceinline__ u32 cvtpk(float lo, float hi) { bf16x2 p = {(__bf16)lo, (__bf16)hi}; return __builtin_bit_cast(u32, p); }
__device__ __forceinline__ f32x4 mfma16(v4u a, v4u b, f32x4 c) { return __builtin_amdgcn_mfma_f32_16x16x32_bf16(__builtin_bit_cast(bf16x8, a), __builtin_bit_cast(bf16x8, b), c, 0, 0, 0); }
__device__ __forceinline__ f32x4 mfma16h(v2u a, v2u b, f32x4 c) { return __builtin_amdgcn_mfma_f32_16x16x16bf16_1k(__builtin_bit_cast(s16x4, a), __builtin_bit_cast(s16x4, b), c, 0, 0, 0); }
__device__ __forceinline__ f32x16 mfma32(v4u a, v4u b, f32x16 c) { return __builtin_amdgcn_mfma_f32_32x32x16_bf16(__builtin_bit_cast(bf16x8, a), __builtin_bit_cast(bf16x8, b), c, 0, 0, 0); }
__device__ __forceinline__ float ex2(float x) { return __builtin_amdgcn_exp2f(x); }
__device__ __forceinline__ float rcp(float x) { return __builtin_amdgcn_rcpf(x); }
__device__ __forceinline__ float asf(u32 x) { return __builtin_bit_cast(float, x); }
__device__ __forceinline__ u32 asu(float x) { return __builtin_bit_cast(u32, x); }
template <int CTRL> __device__ __forceinline__ float dpp(float x) { return asf((u32)__builtin_amdgcn_mov_dpp((int)asu(x), CTRL, 0xf, 0xf, true)); }
__device__ __forceinline__ float x16max(float x) { auto s = __builtin_amdgcn_permlane16_swap(asu(x), asu(x), false, false); return fmaxf(asf(s[0]), asf(s[1])); }
__device__ __forceinline__ float x32max(float x) { auto s = __builtin_amdgcn_permlane32_swap(asu(x), asu(x), false, false); return fmaxf(asf(s[0]), asf(s[1])); }
__device__ __forceinline__ float x16sum(float x) { auto s = __builtin_amdgcn_permlane16_swap(asu(x), asu(x), false, false); return asf(s[0]) + asf(s[1]); }
__device__ __forceinline__ float x32sum(float x) { auto s = __builtin_amdgcn_permlane32_swap(asu(x), asu(x), false, false); return asf(s[0]) + asf(s[1]); }
__device__ __forceinline__ float row16max(float x) { x = fmaxf(x, dpp<0x128>(x)); x = fmaxf(x, dpp<0x124>(x)); x = fmaxf(x, dpp<0x122>(x)); return fmaxf(x, dpp<0x121>(x)); }
__device__ __forceinline__ float row16sum(float x) { x += dpp<0x128>(x); x += dpp<0x124>(x); x += dpp<0x122>(x); return x + dpp<0x121>(x); }
__device__ __forceinline__ float max4(f32x4 a) { return fmaxf(fmaxf(a.x, a.y), fmaxf(a.z, a.w)); }
__device__ __forceinline__ float sum4(f32x4 a) { return (a.x + a.y) + (a.z + a.w); }
#define ZERO4 f32x4{0.f, 0.f, 0.f, 0.f}

#define BAR() asm volatile("s_waitcnt lgkmcnt(0)\n\ts_barrier" ::: "memory")
#define VMWAIT(n) asm volatile("s_waitcnt vmcnt(" #n ")" ::: "memory")

template <int PACED, bool NT, bool WAR = true>
__device__ __forceinline__ void dma4(u64 base, u32 o0, u32 o1, u32 o2, u32 o3, u32 lds) {
  if (WAR) asm volatile("s_waitcnt lgkmcnt(0)" ::: "memory");
#define DMA_BODY(W, P) \
  asm volatile("s_mov_b32 m0, %5\n\ts_nop 0\n\t" W "global_load_lds_dwordx4 %0, %4" P "\n\t" W "global_load_lds_dwordx4 %1, %4 offset:1024" P "\n\t" \
               W "global_load_lds_dwordx4 %2, %4 offset:2048" P "\n\t" W "global_load_lds_dwordx4 %3, %4 offset:3072" P \
               :: "v"(o0), "v"(o1), "v"(o2), "v"(o3), "s"(base), "s"(lds) : "memory")
  if (PACED == 8) DMA_BODY("s_waitcnt vmcnt(7)\n\t", " nt");
  else if (PACED && NT) DMA_BODY("s_waitcnt vmcnt(3)\n\t", " nt");
  else if (PACED) DMA_BODY("s_waitcnt vmcnt(3)\n\t", "");
  else if (NT) DMA_BODY("", " nt");
  else DMA_BODY("", "");
#undef DMA_BODY
}

struct Lane { u32 m0, m1, m2, m3; };
__device__ __forceinline__ Lane make_lane(int c) {
  const int j0 = c & 7; const u32 hm = (j0 & 1) ? 0xFFFF0000u : 0x0000FFFFu;
  return Lane{(j0 >> 1) == 0 ? hm : 0u, (j0 >> 1) == 1 ? hm : 0u, (j0 >> 1) == 2 ? hm : 0u, (j0 >> 1) == 3 ? hm : 0u};
}
__device__ __forceinline__ v4u diag(v2u xs, int i, const Lane& L) {
  const u32 src = i < 2 ? xs.x : xs.y;
  const u32 pp = __builtin_amdgcn_perm(src, src, (i & 1) ? 0x03020302u : 0x01000100u);
  return v4u{pp & L.m0, pp & L.m1, pp & L.m2, pp & L.m3};
}
__device__ __forceinline__ f32x4 gemv4(const v4u* W, v2u xs, const Lane& L, f32x4 acc) {
#pragma unroll
  for (int i = 0; i < 4; ++i) acc = mfma16(W[i], diag(xs, i, L), acc);
  return acc;
}
__device__ __forceinline__ void ldsW(v4u (&W)[4], LAS char* sm, u32 unit, int g, int c) {
#pragma unroll
  for (int i = 0; i < 4; ++i) W[i] = *(LAS v4u*)(sm + unit + (4 * g + i) * 256 + c * 16);
}
template <int STRIDE> __device__ __forceinline__ float reduce8(LAS char* sm, u32 off) {
  float v[8];
  asm volatile("" : "+v"(off));
#pragma unroll
  for (int w = 0; w < 8; ++w) v[w] = *(LAS float*)(sm + off + w * STRIDE);
  __builtin_amdgcn_sched_barrier(0);
  return ((v[0] + v[1]) + (v[2] + v[3])) + ((v[4] + v[5]) + (v[6] + v[7]));
}
__device__ __forceinline__ void st_gran(char* ws, u32 off, u32 val, u32 tag) { __hip_atomic_store((GAS u64*)(ws + off), ((u64)tag << 32) | val, RLX_AGENT); }
__device__ __forceinline__ u64 ld_gran(char* ws, u32 off) { return __hip_atomic_load((GAS u64*)(ws + off), RLX_AGENT); }
__device__ __forceinline__ v4u ld2(__amdgpu_buffer_rsrc_t rs, u32 off) { return __builtin_amdgcn_raw_buffer_load_b128(rs, off, 0, 16); }
__device__ __forceinline__ bool spin(u32& n) { asm volatile("s_sleep 1" ::: "memory"); return ++n > SPIN_CAP; }
__device__ __forceinline__ bool spinx(u32& n) { asm volatile("s_sleep %0" :: "i"(LSLEEP) : "memory"); return ++n > SPIN_CAP; }
__device__ __forceinline__ bool spinl(u32& n) { asm volatile("s_sleep 6" ::: "memory"); return ++n > SPIN_CAP; }
__device__ __forceinline__ void sleep_until(u64 t0, u32 cyc) {
  _Pragma("nounroll") for (u32 n = 0; n < 4096 && __builtin_amdgcn_s_memtime() - t0 < cyc; ++n) asm volatile("s_sleep 2" ::: "memory");
}
__device__ __forceinline__ void lds_add(LAS char* sm, u32 off, int lane) {
  asm volatile("s_waitcnt lgkmcnt(0)" ::: "memory");
  if (lane == 0) __hip_atomic_fetch_add((LAS u32*)(sm + off), 1u, RLX_WG);
}
__device__ __forceinline__ void lds_wait(LAS char* sm, u32 off, u32 want) {
  for (u32 n = 0; n < (SPIN_CAP << 4); ++n) {
    if ((u32)__builtin_amdgcn_readfirstlane(__hip_atomic_load((LAS u32*)(sm + off), RLX_WG)) >= want) break;
  }
  asm volatile("" ::: "memory");
}
__device__ __forceinline__ void qa_issue(v4u (&q)[16], __amdgpu_buffer_rsrc_t rs, int xcd, int b, int lane) {
#pragma unroll
  for (int k = 0; k < 16; ++k) q[k] = ld2(rs, QA_OFF + (u32)(((16 * xcd + k) * 256 + b * 128 + 2 * lane) * 8));
}
__device__ __forceinline__ v4u qa_finish(v4u (&q)[16], __amdgpu_buffer_rsrc_t rs, int xcd, int b, int lane, u32 tag) {
  _Pragma("nounroll") for (u32 n = 0;;) {
    bool ok = true;
#pragma unroll
    for (int k = 0; k < 16; ++k) ok &= (q[k].y == tag) & (q[k].w == tag);
    if (__all(ok) || spinl(n)) break;
    qa_issue(q, rs, xcd, b, lane);
  }
  v4u r;
#pragma unroll
  for (int hh = 0; hh < 4; ++hh)
    r[hh] = cvtpk((asf(q[4 * hh].x) + asf(q[4 * hh + 1].x)) + (asf(q[4 * hh + 2].x) + asf(q[4 * hh + 3].x)),
                  (asf(q[4 * hh].z) + asf(q[4 * hh + 1].z)) + (asf(q[4 * hh + 2].z) + asf(q[4 * hh + 3].z)));
  return r;
}
__device__ __forceinline__ void put_qa(LAS char* sm, v4u r, int lane) {
#pragma unroll
  for (int hh = 0; hh < 4; ++hh) *(LAS u32*)(sm + L_QIMG + hh * 256 + lane * 4) = r[hh];
  lds_add(sm, F_Q, lane);
}
__device__ __forceinline__ void final_reduce(LAS char* sm, u32 flag, int phase, char* ws, u64 pOut, int which, int idx, int lane, u32 tag) {
  const u32 yo = YG_OFF + (u32)((which * 128 + idx) * 256 + lane) * 8;
  const u32 oo = (u32)(((idx >> 6) * 1024 + which * 512 + (idx & 63) * 8 + (lane & 7)) * 2);
  if (phase >= YPOLL) return;
  if (YDELAY) asm volatile("s_sleep %0" :: "i"(YDELAY) : "memory");
  _Pragma("nounroll") for (int k = 0; k < phase; ++k) asm volatile("s_sleep %0" :: "i"(STAG) : "memory");
  u64 y0, y1, y2, y3;
  _Pragma("nounroll") for (u32 n = 0;;) {
    y0 = ld_gran(ws, yo); y1 = ld_gran(ws, yo + 512); y2 = ld_gran(ws, yo + 1024); y3 = ld_gran(ws, yo + 1536);
    if (__all(((u32)(y0 >> 32) == tag) & ((u32)(y1 >> 32) == tag) & ((u32)(y2 >> 32) == tag) & ((u32)(y3 >> 32) == tag)) || ++n > SPIN_CAP) break;
    if (__builtin_amdgcn_readfirstlane(__hip_atomic_load((LAS u32*)(sm + flag), RLX_WG))) return;
    asm volatile("s_sleep %0" :: "i"(YSLEEP) : "memory");
  }
  float x = (asf((u32)y0) + asf((u32)y1)) + (asf((u32)y2) + asf((u32)y3));
  x += dpp<0x128>(x);
  x = x32sum(x16sum(x));
  if (lane < 8) *(GAS __bf16*)(pOut + oo) = (__bf16)x;
  if (lane == 0) __hip_atomic_store((LAS u32*)(sm + flag), 1u, RLX_WG);
}

__global__ void __launch_bounds__(576, 1)
fused_decode(const u16* __restrict__ Bwq, const u16* __restrict__ Bc, char* ws, const u16* __restrict__ Bx, const u16* __restrict__ Ax, const u16* __restrict__ Awq,
             const u16* __restrict__ Bwuk, const u16* __restrict__ Ak, const u16* __restrict__ Av, const u16* __restrict__ Awo, const u16* __restrict__ Bwuv,
             const u16* __restrict__ Bwo, u16* __restrict__ out) {
  __shared__ __attribute__((aligned(16))) char smem[163840];
  u64 pc0; asm volatile("s_getpc_b64 %0" : "=s"(pc0));
  const u64 ht0 = __builtin_amdgcn_s_memtime();
  LAS char* sm = (LAS char*)smem;
  const u32 sm0 = (u32)(size_t)sm;
  STAMP(0);
  const int tid = threadIdx.x, lane = tid & 63, w = __builtin_amdgcn_readfirstlane(tid >> 6), g = lane >> 4, c = lane & 15;
  const int bid = blockIdx.x, xcd = bid & 7, isB = bid >> 7, jj = (bid >> 3) & 15, qt = jj & 3, h = 4 * xcd + (jj >> 2);
  u64 pAx = (u64)Ax, pAk = (u64)Ak, pAv = (u64)Av, pAwq = (u64)Awq, pAwo = (u64)Awo, pBx = (u64)Bx, pBc = (u64)Bc, pBwq = (u64)Bwq, pBwuk = (u64)Bwuk, pBwuv = (u64)Bwuv, pBwo = (u64)Bwo, pOut = (u64)out;
  const u32 ring = sm0 + w * 16384, ringo = w * 16384;
  const u32 wrow = (u32)(16 * w * 8192 + h * 256);
  const u32 wo0 = g * 8192 + c * 16, wo1 = wo0 + 31744, wo2 = wo0 + 2 * 31744, wo3 = wo0 + 3 * 31744, lo16 = lane * 16;
  const int bB = xcd >> 2, sB = (xcd & 3) * 16 + jj;
  const u64 cb = pBc + (u32)((bB * SK + sB * 128 + 4 * w) * 1024);
  const u32 co0 = lo16 ^ ((w & 3) * 16), cl = sm0 + w * 4096;
  if (w < 8) {
    asm volatile("s_nop 4" ::: "memory");
    dma4<false, false, false>(isB ? cb : pBwq + (wrow + (u32)qt * (128 * 8192)), isB ? co0 : wo0, isB ? co0 ^ 64 : wo1, isB ? co0 ^ 128 : wo2, isB ? co0 ^ 192 : wo3, isB ? cl : ring + 4096);
  }
  asm volatile("" : "+s"(pAx), "+s"(pAk), "+s"(pAv), "+s"(pAwo), "+s"(pBx), "+s"(pAwq), "+s"(pBwuk), "+s"(pBwuv), "+s"(pBwo), "+s"(pOut));
  u32 tag = *(const __attribute__((address_space(4))) u32*)(u64)ws;
  const __amdgpu_buffer_rsrc_t rs = __builtin_amdgcn_make_buffer_rsrc(ws, 0, (int)WS_USED, 0x00020000);
  const int isT = isB & ~jj & 1;
  const int bA = jj >> 3, sA = isB ? 8 + 4 * isT + ((jj >> 1) & 3) : (jj & 7);

  if (w == 8) {
    asm volatile("" : "+s"(tag)); tag += 1u;
    __builtin_amdgcn_s_setprio(3);
    if (CODE_KB && jj == 0) {
      const u64 pc = pc0 & ~255ull;
      v4u acc = {0u, 0u, 0u, 0u};
#pragma unroll
      for (int k = 0; k < CODE_KB; ++k) acc ^= *(const GAS v4u*)(pc + (u32)(k * 1024 + lo16));
      asm volatile("" :: "v"(acc));
    }
    { const u32 touch = *(volatile GAS u32*)pOut; asm volatile("" :: "v"(touch)); }
    if (!isB) {
      if (lane < 16) *(LAS u32*)(sm + L_FLAG + lane * 4) = 0u;
      BAR(); BAR();
      sleep_until(ht0, QA_T);
      v4u q[16];
      qa_issue(q, rs, xcd, bA, lane);
      put_qa(sm, qa_finish(q, rs, xcd, bA, lane, tag), lane);
      STAMP(12);
    } else {
      BAR();
      v4u q[16];
      sleep_until(ht0, QA_T + 1000);
      qa_issue(q, rs, xcd, bA, lane);
      if (SENTINEL) {
        if (SDELAY) sleep_until(ht0, SDELAY);
        const u32 so = QG_OFF + (u32)bB * 65536 + (u32)((8 * (lane >> 5) + 7) * 32 + (lane & 31)) * 64;
        _Pragma("nounroll") for (u32 n = 0;;) { const u64 x = ld_gran(ws, so), y = ld_gran(ws, so + 8 * 4096); if (__all(((u32)(x >> 32) == tag) & ((u32)(y >> 32) == tag)) || spin(n)) break; }
      }
      else sleep_until(ht0, QS_T);
      STAMP(16);
      BAR(); BAR(); BAR(); BAR();
      if (lane < 16) *(LAS u32*)(sm + L_FLAG + lane * 4) = 0u;
      BAR(); BAR(); BAR();
      put_qa(sm, qa_finish(q, rs, xcd, bA, lane, tag), lane);
      lds_wait(sm, F_DRAIN, 8);
      if (lane < 32) st_gran(ws, LB_OFF + (u32)((bB * 64 + sB) * 32 + lane) * 8, *(LAS u32*)(sm + L_LSE + lane * 4), tag);
      STAMP(15);
      if (isT) {
        u64 l0, l1;
        if (LDELAY) asm volatile("s_sleep %0" :: "i"(LDELAY) : "memory");
        _Pragma("nounroll") for (u32 n = 0;;) {
          l0 = ld_gran(ws, LB_OFF + (u32)(lane * 32 + h) * 8); l1 = ld_gran(ws, LB_OFF + (u32)((64 + lane) * 32 + h) * 8);
          if (__all(((u32)(l0 >> 32) == tag) & ((u32)(l1 >> 32) == tag)) || spinx(n)) break;
        }
        const float f0 = asf((u32)l0), f1 = asf((u32)l1);
        const float e0 = ex2(f0 - x32max(x16max(row16max(f0)))), e1 = ex2(f1 - x32max(x16max(row16max(f1))));
        *(LAS __bf16*)(sm + L_LBV + lane * 2) = (__bf16)(e0 * rcp(x32sum(x16sum(row16sum(e0)))));
        *(LAS __bf16*)(sm + L_LBV + 128 + lane * 2) = (__bf16)(e1 * rcp(x32sum(x16sum(row16sum(e1)))));
        asm volatile("s_waitcnt lgkmcnt(0)" ::: "memory");
        STAMP(17);
      }
    }
    STAMP_OUT();
    return;
  }

  const u32 sw = (c ^ g) * 16;
  const u32 ko0 = g * 256 + sw, ko1 = g * 256 + (sw ^ 64), ko2 = g * 256 + (sw ^ 128), ko3 = g * 256 + (sw ^ 192);
  const int nt = isT ? NT : isB ? NX : NA, T = (nt >> 3) + (w < (nt & 7));
  const u32 kv_off = (u32)((bA * 8 + xcd) * SK + 16 * ((isT ? 8 * NA + 4 * NX + NT * (sA - 12) : isB ? 8 * NA + NX * (sA - 8) : NA * sA) + w)) * 256;
  u64 kb = pAk + kv_off, vb = pAv + kv_off, fb = pAwo + (u32)((128 * h + 16 * w) * 1024 + qt * 256);
  asm volatile("" : "+s"(kb), "+s"(vb), "+s"(fb));
  const int tb = (tid >> 5) & 1, tcol = 32 * ((tid >> 6) & 3) + (tid & 31), tr = (tid & 0xE0) | ((tid & 7) << 2) | ((tid >> 3) & 3);
  const Lane L = make_lane(c);

  const int half = (jj >> 1) & 1;
  v4u UW[8], X[8];
#define LOAD_FW() { _Pragma("unroll") for (int i = 0; i < 8; ++i) FW[i] = __builtin_nontemporal_load((const GAS v4u*)(pBwo + (u32)((128 * h + 16 * w + 4 * g + (i & 3)) * 1024 + half * 512 + (i >> 2) * 256 + c * 16))); }
  if (!isB) {
    {
      const u64 src = pBx + (u32)(((lane >> 1) & 1) * 512 + 128 * ((lane >> 2) & 3) + 16 * w + 8 * (lane & 1)) * 2;
      asm volatile("s_mov_b32 m0, %1\n\ts_nop 4\n\tglobal_load_lds_dwordx4 %0, off" :: "v"(src), "s"(sm0 + L_XS + w * 1024) : "memory");
    }
    const u64 wq_b = pBwq + wrow;
    const u32 xso = L_XS + w * 1024 + (c >> 3) * 32 + g * 8;
    f32x4 acc = ZERO4;
    v4u W[4];
    asm volatile("" : "+s"(tag)); tag += 1u;
    dma4<false, true, false>(pBwuk + (wrow + (u32)qt * (128 * 8192)), g * 8192 + sw, g * 8192 + (sw ^ 64) + 31744, g * 8192 + (sw ^ 128) + 2 * 31744, g * 8192 + (sw ^ 192) + 3 * 31744, ring + 3 * 4096);
    VMWAIT(4);
    STAMP(13);
    dma4<false, false, false>(wq_b + (u32)(((qt + 1) & 3) * (128 * 8192)), wo0, wo1, wo2, wo3, ring + 2 * 4096);
    dma4<false, false, false>(wq_b + (u32)(((qt + 2) & 3) * (128 * 8192)), wo0, wo1, wo2, wo3, ring);
    ldsW(W, sm, ringo + 4096, g, c); acc = gemv4(W, *(LAS v2u*)(sm + xso + qt * 64), L, acc);
    dma4<false, false>(wq_b + (u32)(((qt + 3) & 3) * (128 * 8192)), wo0, wo1, wo2, wo3, ring + 4096);
    VMWAIT(8);
    STAMP(14);
    ldsW(W, sm, ringo + 2 * 4096, g, c); acc = gemv4(W, *(LAS v2u*)(sm + xso + ((qt + 1) & 3) * 64), L, acc);
    VMWAIT(4);
    ldsW(W, sm, ringo, g, c); acc = gemv4(W, *(LAS v2u*)(sm + xso + ((qt + 2) & 3) * 64), L, acc);
    dma4<true, true>(kb, ko0, ko1, ko2, ko3, ring);
    ldsW(W, sm, ringo + 4096, g, c); acc = gemv4(W, *(LAS v2u*)(sm + xso + ((qt + 3) & 3) * 64), L, acc);
    *(LAS f32x4*)(sm + L_PART + w * 1024 + lo16) = acc;
    STAMP(1);
    BAR();
    STAMP(16);
    if (tid < 256) *(LAS __bf16*)(sm + L_XBUF + (tb * 128 + tcol) * 2) = (__bf16)reduce8<1024>(sm, L_PART + tr * 4);
    BAR();
    STAMP(17);
    {
      f32x4 qa = ZERO4;
#pragma unroll
      for (int k = 0; k < 4; ++k)
        qa = mfma16(*(LAS v4u*)(sm + ringo + 3 * 4096 + c * 256 + ((4 * k + g) ^ c) * 16), *(LAS v4u*)(sm + L_XBUF + (c & 1) * 256 + k * 64 + g * 16), qa);
      if (c < 2) {
        __builtin_amdgcn_raw_buffer_store_b128(v4u{cvtpk(qa.x, qa.y), tag, cvtpk(qa.z, qa.w), tag}, rs, QG_OFF + (u32)c * 65536 + (u32)(((8 * qt + w) * 32 + h) * 4 + g) * 16, 0, 16);
      }
    }
    STAMP(2);
  } else {
    const int h2 = lane >> 5, hd = lane & 31;
    v2u xs;
    asm volatile("global_load_dwordx2 %0, %1, %2" : "=v"(xs) : "v"((u32)(((c >> 3) * 512 + 128 * qt + 16 * w + 4 * g) * 2)), "s"(pAx) : "memory");
    dma4<true, true, false>(cb + 32768, co0, co0 ^ 64, co0 ^ 128, co0 ^ 192, cl + 32768);
    asm volatile("" : "+v"(xs));
    asm volatile("" : "+s"(tag)); tag += 1u;
    dma4<true, true, false>(pAwq + (wrow + (u32)qt * (128 * 8192)), wo0, wo1, wo2, wo3, sm0 + EXTRA + w * 4096);
    dma4<true, true, false>(cb + 2 * 32768, co0, co0 ^ 64, co0 ^ 128, co0 ^ 192, cl + 2 * 32768);
    v4u KV[8];
#pragma unroll
    for (int i = 0; i < 4; ++i) KV[i] = __builtin_nontemporal_load((const GAS v4u*)(kb + (u32)((ko0 ^ (64 * i)) + 1024 * i)));
#define LOAD_V0() { _Pragma("unroll") for (int i = 0; i < 4; ++i) KV[4 + i] = __builtin_nontemporal_load((const GAS v4u*)(vb + (u32)(lo16 + 1024 * i))); }
    const u64 ub = isT ? pBwuv + (u32)((256 * half + 32 * w) * 8192 + h * 256) : kb + 32768;
    const u32 uo = isT ? g * 32768 + c * 16 : ko0, us = isT ? 8192 : 1024, ux = isT ? 0 : 64;
#define LOAD_K1() { _Pragma("unroll") for (int i = 0; i < 4; ++i) UW[i] = __builtin_nontemporal_load((const GAS v4u*)(ub + (u32)((uo ^ (ux * i)) + us * i))); }
#define LOAD_AT(P) { if (XPOS1 == P) LOAD_V0(); if (XPOS2 == P) LOAD_K1(); asm volatile("" ::: "memory"); }
    {
      v4u W[4];
      ldsW(W, sm, EXTRA + w * 4096, g, c);
      const f32x4 acc = gemv4(W, xs, L, ZERO4);
      *(LAS f32x4*)(sm + EXTRA + w * 4096 + lo16) = acc;
    }
    BAR();
    if (tid < 256) st_gran(ws, QA_OFF + (u32)((h * 4 + qt) * 256 + tb * 128 + tcol) * 8, asu(reduce8<4096>(sm, EXTRA + tr * 4)), tag);
    STAMP(14);
    if (XPOS1 == -1) { VMWAIT(4); LOAD_AT(-1) }
    dma4<true, true, false>(cb + 3 * 32768, co0, co0 ^ 64, co0 ^ 128, co0 ^ 192, cl + 3 * 32768);
    BAR();
    {
      const u32 qg = QG_OFF + (u32)bB * 65536 + (u32)tid * 16, ql = L_QB + (tid >> 7) * 1024 + ((tid >> 1) & 1) * 512 + ((tid >> 2) & 31) * 16 + (tid & 1) * 8;
      v4u q[8]; bool ok = true;
#pragma unroll
      for (int k = 0; k < 8; ++k) q[k] = ld2(rs, qg + ((k + jj) & 7) * 8192);
      __builtin_amdgcn_sched_barrier(0);
      __builtin_amdgcn_s_barrier();
      __builtin_amdgcn_sched_barrier(0);
      if (XPOS1 == 0) LOAD_V0(); if (XPOS2 == 0) LOAD_K1();
      __builtin_amdgcn_sched_barrier(0);
#pragma unroll
      for (int k = 0; k < 8; ++k) { ok &= (q[k].y == tag) & (q[k].w == tag); *(LAS v2u*)(sm + ql + ((k + jj) & 7) * 4096) = v2u{q[k].x, q[k].z}; }
      _Pragma("nounroll") for (u32 n = 0; !__all(ok) && !spinl(n);) {
        ok = true;
#pragma unroll 1
        for (int k = 0; k < 8; k += 4) {
          v4u x[4];
#pragma unroll
          for (int j = 0; j < 4; ++j) x[j] = ld2(rs, qg + (k + j) * 8192);
#pragma unroll
          for (int j = 0; j < 4; ++j) { ok &= (x[j].y == tag) & (x[j].w == tag); *(LAS v2u*)(sm + ql + (k + j) * 4096) = v2u{x[j].x, x[j].z}; }
        }
      }
    }
    STAMP(1);
    BAR();
    STAMP(8);
    LOAD_AT(1)
    f32x16 sc;
#pragma unroll
    for (int k = 0; k < 16; ++k) sc[k] = 0.f;
    float mw = 0.f;
    if (w < 4) {
      const u32 ca = (32 * w + hd) * 1024, cx = (h2 ^ (((hd & 3) << 2) | ((hd >> 2) & 3))) * 16;
      u32 qo = L_QB + lo16;
      asm volatile("" : "+v"(qo));
      v4u fa[3][2], fq[3][2];
#define QK_LOAD(B) _Pragma("unroll") for (int k = 0; k < 2; ++k) { fa[(B) % 3][k] = *(LAS v4u*)(sm + ca + ((B) >> 2) * 256 + (cx ^ ((2 * ((B) & 3) + k) * 32))); fq[(B) % 3][k] = *(LAS v4u*)(sm + qo + (2 * (B) + k) * 1024); }
      QK_LOAD(0) QK_LOAD(1)
#pragma unroll
      for (int bb = 0; bb < 16; ++bb) {
        if (bb < 14) { QK_LOAD(bb + 2) }
        __builtin_amdgcn_sched_barrier(0);
#pragma unroll
        for (int k = 0; k < 2; ++k) sc = mfma32(fa[bb % 3][k], fq[bb % 3][k], sc);
        __builtin_amdgcn_sched_barrier(0);
      }
#undef QK_LOAD
      mw = sc[0];
#pragma unroll
      for (int k = 1; k < 16; ++k) mw = fmaxf(mw, sc[k]);
      mw = x32max(mw);
    }
    STAMP(2);
    BAR();
    LOAD_AT(2)
    if (w < 4 && lane < 32) *(LAS float*)(sm + L_BM + hd * 16 + w * 4) = mw;
    BAR();
    if (w < 4) {
      const float M = C2 * max4(*(LAS f32x4*)(sm + L_BM + hd * 16));
      float ls = 0.f;
#pragma unroll
      for (int k = 0; k < 16; ++k) { sc[k] = ex2(fmaf(sc[k], C2, -M)); ls += sc[k]; }
      *(LAS v4u*)(sm + L_PF + w * 2048 + lo16) = v4u{cvtpk(sc[0], sc[1]), cvtpk(sc[2], sc[3]), cvtpk(sc[4], sc[5]), cvtpk(sc[6], sc[7])};
      *(LAS v4u*)(sm + L_PF + w * 2048 + 1024 + lo16) = v4u{cvtpk(sc[8], sc[9]), cvtpk(sc[10], sc[11]), cvtpk(sc[12], sc[13]), cvtpk(sc[14], sc[15])};
      ls = x32sum(ls);
      if (lane < 32) *(LAS float*)(sm + L_BL + hd * 16 + w * 4) = ls;
    }
    BAR();
    STAMP(11);
    LOAD_AT(3)
    f32x16 oa, ob;
#pragma unroll
    for (int k = 0; k < 16; ++k) { oa[k] = 0.f; ob[k] = 0.f; }
    {
      const int q = (lane >> 2) & 3, p = lane & 3, blk = (lane >> 4) & 1;
      const u32 base = (4 * h2 + q) * 1024 + (w >> 1) * 256 + 8 * (p & 1), lo4 = 8 * (w & 1) + 2 * blk + (p >> 1);
      const u32 a00 = base + 16 * (lo4 ^ ((q << 2) | h2)), a01 = base + 16 * ((lo4 + 4) ^ ((q << 2) | h2));
      const u32 a10 = base + 8192 + 16 * (lo4 ^ ((q << 2) | (2 + h2))), a11 = base + 8192 + 16 * ((lo4 + 4) ^ ((q << 2) | (2 + h2)));
      u32 po = L_PF + lo16;
      asm volatile("" : "+v"(po));
      v4u pb[2]; v2u tt[2][4];
#define TRR(A) __builtin_bit_cast(v2u, __builtin_amdgcn_ds_read_tr16_b64_v4i16((LAS s16x4*)(sm + (A))))
#define PV_LOAD(K, S) { pb[S] = *(LAS v4u*)(sm + po + (K) * 1024); tt[S][0] = TRR(a00 + (K) * 16384); tt[S][1] = TRR(a10 + (K) * 16384); tt[S][2] = TRR(a01 + (K) * 16384); tt[S][3] = TRR(a11 + (K) * 16384); }
      PV_LOAD(0, 0)
#pragma unroll
      for (int ks = 0; ks < 8; ++ks) {
        if (ks < 7) PV_LOAD(ks + 1, (ks + 1) & 1)
        __builtin_amdgcn_sched_barrier(0);
        oa = mfma32(v4u{tt[ks & 1][0].x, tt[ks & 1][0].y, tt[ks & 1][1].x, tt[ks & 1][1].y}, pb[ks & 1], oa);
        ob = mfma32(v4u{tt[ks & 1][2].x, tt[ks & 1][2].y, tt[ks & 1][3].x, tt[ks & 1][3].y}, pb[ks & 1], ob);
        __builtin_amdgcn_sched_barrier(0);
      }
#undef PV_LOAD
#undef TRR
    }
    STAMP(3);
    {
      const float Ls = sum4(*(LAS f32x4*)(sm + L_BL + hd * 16));
      const float inv = rcp(Ls);
      if (tid < 32) *(LAS float*)(sm + L_LSE + tid * 4) = fmaf(C2, max4(*(LAS f32x4*)(sm + L_BM + hd * 16)), __builtin_amdgcn_logf(Ls));
      const u32 so = hd * 1024 + (w >> 1) * 256 + 8 * h2, sx = (8 * (w & 1)) ^ (((hd & 3) << 2) | ((hd >> 2) & 3));
#pragma unroll
      for (int k = 0; k < 4; ++k) {
        *(LAS v2u*)(sm + so + (sx ^ k) * 16) = v2u{cvtpk(oa[4 * k] * inv, oa[4 * k + 1] * inv), cvtpk(oa[4 * k + 2] * inv, oa[4 * k + 3] * inv)};
        *(LAS v2u*)(sm + so + (sx ^ (4 + k)) * 16) = v2u{cvtpk(ob[4 * k] * inv, ob[4 * k + 1] * inv), cvtpk(ob[4 * k + 2] * inv, ob[4 * k + 3] * inv)};
      }
      asm volatile("s_waitcnt lgkmcnt(0)" ::: "memory");
#pragma unroll
      for (int i = 0; i < 4; ++i) {
        const int hh = 8 * i + (lane >> 3);
        const v4u v = *(LAS v4u*)(sm + hh * 1024 + (w >> 1) * 256 + ((8 * (w & 1) + (lane & 7)) ^ (((hh & 3) << 2) | ((hh >> 2) & 3))) * 16);
        __builtin_amdgcn_raw_buffer_store_b128(v, rs, PB_OFF + (u32)(((hh * 2 + (w >> 2)) * 2 + bB) * 64 + sB) * 512 + (w & 3) * 128 + (lane & 7) * 16, 0, 16);
      }
    }
    STAMP(13);
    BAR();
    if (isT) {
#pragma unroll
      for (int i = 0; i < 4; ++i) UW[4 + i] = __builtin_nontemporal_load((const GAS v4u*)(ub + (u32)(16 * 8192 + uo + us * i)));
      asm volatile("" ::: "memory");
    } else {
      dma4<false, true, false>(vb + 32768, lo16, lo16, lo16, lo16, ring + 3 * 4096);
#pragma unroll
      for (int i = 0; i < 4; ++i) *(LAS v4u*)(sm + ringo + 8192 + i * 1024 + lo16) = UW[i];
    }
#pragma unroll
    for (int i = 0; i < 8; ++i) *(LAS v4u*)(sm + ringo + i * 1024 + lo16) = KV[i];
    VMWAIT(4);
    lds_add(sm, F_DRAIN, lane);
  }
  if (!isB) {
    dma4<true, true>(vb, lo16, lo16, lo16, lo16, ring + 4096);
    dma4<true, true>(kb + 32768, ko0, ko1, ko2, ko3, ring + 2 * 4096);
    dma4<true, true>(vb + 32768, lo16, lo16, lo16, lo16, ring + 3 * 4096);
  }
  STAMP(4);
  lds_wait(sm, F_Q, 1);
  STAMP(5);
  float Mw;
  {
    v4u qf[4];
#pragma unroll
    for (int k = 0; k < 4; ++k) qf[k] = *(LAS v4u*)(sm + L_QIMG + (c & 3) * 256 + k * 64 + g * 16);
    const u32 pm0 = (c >> 2) == 0 ? 0x0000FFFFu : (c >> 2) == 1 ? 0xFFFF0000u : 0u, pm1 = (c >> 2) == 2 ? 0x0000FFFFu : (c >> 2) == 3 ? 0xFFFF0000u : 0u;
    float m = -INFINITY, l = 0.f;
    f32x4 o0 = ZERO4, o1 = ZERO4;
    const u32 kr = ringo + c * 256, vr = ringo + 4096 + g * 1024 + c * 16;
    v2u y[4];
#define QK_STEP(SO) { \
      const f32x4 sa = mfma16(*(LAS v4u*)(sm + kr + (SO) + ((8 + g) ^ c) * 16), qf[2], mfma16(*(LAS v4u*)(sm + kr + (SO) + ((0 + g) ^ c) * 16), qf[0], ZERO4)) \
                     + mfma16(*(LAS v4u*)(sm + kr + (SO) + ((12 + g) ^ c) * 16), qf[3], mfma16(*(LAS v4u*)(sm + kr + (SO) + ((4 + g) ^ c) * 16), qf[1], ZERO4)); \
      const float mn = fmaxf(m, x32max(x16max(max4(sa)))); \
      const float al = ex2((m - mn) * C2), mc = mn * C2; \
      m = mn; \
      const float p[4] = {ex2(fmaf(sa.x, C2, -mc)), ex2(fmaf(sa.y, C2, -mc)), ex2(fmaf(sa.z, C2, -mc)), ex2(fmaf(sa.w, C2, -mc))}; \
      l = fmaf(l, al, (p[0] + p[1]) + (p[2] + p[3])); \
      o0 *= al; o1 *= al; \
      _Pragma("unroll") for (int i = 0; i < 4; ++i) { const u32 pp = cvtpk(p[i], p[i]); y[i] = v2u{pp & pm0, pp & pm1}; } }
#define PV_STEP(SO) _Pragma("unroll") for (int i = 0; i < 4; ++i) { \
      const v4u V = *(LAS v4u*)(sm + vr + (SO) + i * 256); \
      o0 = mfma16h(v2u{V.x, V.y}, y[i], o0); o1 = mfma16h(v2u{V.z, V.w}, y[i], o1); }
#pragma unroll 1
    for (int t = -1; t < T - 1; ++t) {
      const u32 so = (t & 1) * 8192;
      if (t >= 0) {
        if (t + 2 < T) dma4<8, true>(kb + (u32)((t + 2) * 32768), ko0, ko1, ko2, ko3, ring + so);
        else VMWAIT(4);
        PV_STEP(so)
      }
      QK_STEP(so ^ 8192)
      if (t >= 0 && t + 2 < T) dma4<8, true>(vb + (u32)((t + 2) * 32768), lo16, lo16, lo16, lo16, ring + so + 4096);
    }
    if (lane < 4) *(LAS float*)(sm + L_MST + lane * 32 + w * 4) = m;
    lds_add(sm, F_M, lane);
    l = x32sum(x16sum(l));
    u32 mo = L_MRG + w * 2048 + (c & 3) * 512 + ((4 * g + (c >> 2)) ^ (2 * (c & 3))) * 16, lo = L_MST + 128 + lane * 32 + w * 4, so = ((T - 1) & 1) * 8192;
    asm volatile("" : "+v"(mo), "+v"(lo), "+v"(l));
    lds_wait(sm, F_M, 8);
    Mw = fmaxf(max4(*(LAS f32x4*)(sm + L_MST + (c & 3) * 32)), max4(*(LAS f32x4*)(sm + L_MST + (c & 3) * 32 + 16)));
    float f = ex2((m - Mw) * C2);
    asm volatile("" : "+v"(f), "+v"(Mw));
    const u32 f0 = g * 1024, fx = (c ^ (4 * g)) * 16;
    if (FPOS == 0 && !isB) { dma4<false, true, false>(fb, f0 + fx, f0 + 3072 + (fx ^ 16), f0 + 2 * 3072 + (fx ^ 32), f0 + 3 * 3072 + (fx ^ 48), ring); VMWAIT(4); }
    else if (FPOS == 1 && !isB) { VMWAIT(2); dma4<false, true, false>(fb, f0 + fx, f0 + 3072 + (fx ^ 16), f0 + 2 * 3072 + (fx ^ 32), f0 + 3 * 3072 + (fx ^ 48), ring); VMWAIT(4); }
    else if (!isT) VMWAIT(0);
    if (FPOS == 2 && !isB) dma4<false, true, false>(fb, f0 + fx, f0 + 3072 + (fx ^ 16), f0 + 2 * 3072 + (fx ^ 32), f0 + 3 * 3072 + (fx ^ 48), ring);
    PV_STEP(so)
    STAMP(6);
    *(LAS f32x4*)(sm + mo) = o0 * f;
    *(LAS f32x4*)(sm + mo + 256) = o1 * f;
    if (lane < 4) *(LAS float*)(sm + lo) = l * f;
#undef QK_STEP
#undef PV_STEP
  }
  {
    u32 ro = L_MRG + w * 512 + ((lane & 31) ^ (2 * w)) * 16, go = OA_OFF + (u32)((4 * xcd + w) * 32 + bA * 16 + sA) * 640 + lane * 16;
    const float Mc = C2 * asf(__builtin_amdgcn_readlane(asu(Mw), w & 3));
    asm volatile("" : "+v"(ro), "+v"(go));
    BAR();
    if (isT) {
#pragma unroll
      for (int i = 0; i < 8; ++i) X[i] = ld2(rs, PB_OFF + (u32)((h * 2 + half) * 128 + 16 * w + 4 * g + (i & 3)) * 512 + (i >> 2) * 256 + c * 16);
      asm volatile("" ::: "memory");
    }
    if (w < 4) {
      STAMP(18);
      f32x4 v[8];
#pragma unroll
      for (int ww = 0; ww < 8; ++ww) v[ww] = *(LAS f32x4*)(sm + ro + ww * 2048);
      const f32x4 la = *(LAS f32x4*)(sm + L_MST + 128 + w * 32), lb = *(LAS f32x4*)(sm + L_MST + 128 + w * 32 + 16);
      __builtin_amdgcn_sched_barrier(0);
      const float Ls = sum4(la + lb), inv = rcp(Ls);
      const f32x4 a = (((v[0] + v[1]) + (v[2] + v[3])) + ((v[4] + v[5]) + (v[6] + v[7]))) * inv;
      const u32 p0 = lane < 32 ? cvtpk(a.x, a.y) : asu(Mc + __builtin_amdgcn_logf(Ls));
      if (lane < 40) __builtin_amdgcn_raw_buffer_store_b128(v4u{p0, tag, cvtpk(a.z, a.w), tag}, rs, go, 0, 16);
      STAMP(7);
    }
  }
    if (isT) {
      v2u xw = *(LAS v2u*)(sm + L_LBV + w * 32 + g * 8);
      if ((c >> 3) != (w >> 2)) xw = v2u{0u, 0u};
      *(LAS f32x4*)(sm + L_PART + w * 1024 + lo16) = gemv4(X, xw, L, ZERO4);
      *(LAS f32x4*)(sm + L_PARTB + w * 1024 + lo16) = gemv4(X + 4, xw, L, ZERO4);
      asm volatile("" ::: "memory");
      v4u FW[8];
      const u32 po = (tid < 256 ? L_PART : L_PARTB) + tr * 4;
      STAMP(20);
      BAR();
      if (UPOS == 0) LOAD_FW()
      {
        const u32 pq = po + tb * 4096;
        const float p0 = *(LAS float*)(sm + pq), p1 = *(LAS float*)(sm + pq + 1024), p2 = *(LAS float*)(sm + pq + 2048), p3 = *(LAS float*)(sm + pq + 3072);
        *(LAS __bf16*)(sm + L_XBUF + (tb * 256 + (tid >> 8) * 128 + tcol) * 2) = (__bf16)((p0 + p1) + (p2 + p3));
      }
      BAR();
      if (UPOS == 1) LOAD_FW()
      const u32 xo = L_XBUF + (c >> 3) * 512 + w * 64 + g * 8;
      *(LAS f32x4*)(sm + L_PART + w * 1024 + lo16) = gemv4(UW + 4, *(LAS v2u*)(sm + xo + 32), L, gemv4(UW, *(LAS v2u*)(sm + xo), L, ZERO4));
      BAR();
      float own = 0.f;
      if (w < 4) {
        own = reduce8<1024>(sm, po);
        st_gran(ws, CX_OFF + (u32)((h * 2 + half) * 256 + tid) * 8, asu(own), tag);
        STAMP(19);
      } else asm volatile("s_sleep 3" ::: "memory");
      if (UPOS == 3) LOAD_FW()
      asm volatile("" ::: "memory");
      if (w < 4) {
        u64 x;
        if (CDELAY) asm volatile("s_sleep %0" :: "i"(CDELAY) : "memory");
        _Pragma("nounroll") for (u32 n = 0;;) { x = ld_gran(ws, CX_OFF + (u32)((h * 2 + (half ^ 1)) * 256 + tid) * 8); if (__all((u32)(x >> 32) == tag) || spin(n)) break; }
        *(LAS __bf16*)(sm + L_XBUF + (tb * 128 + tcol) * 2) = (__bf16)(half ? asf((u32)x) + own : own + asf((u32)x));
        STAMP(12);
      }
      BAR();
      const v2u xc = *(LAS v2u*)(sm + L_XBUF + (c >> 3) * 256 + w * 32 + g * 8);
      *(LAS f32x4*)(sm + L_PART + w * 1024 + lo16) = gemv4(FW, xc, L, ZERO4);
      *(LAS f32x4*)(sm + L_PARTB + w * 1024 + lo16) = gemv4(FW + 4, xc, L, ZERO4);
      BAR();
      st_gran(ws, YG_OFF + (u32)((((2 + tb) * 64 + 32 * half + 16 * (tid >> 8) + (tcol >> 3)) * 32 + h) * 8 + (tcol & 7)) * 8, asu(reduce8<1024>(sm, po)), tag);
      STAMP(9);
      final_reduce(sm, F_OUT + (w & 1) * 4, w >> 1, ws, pOut, 1, (xcd * 8 + (jj >> 1)) * 2 + (w & 1), lane, tag); STAMP(10);
      STAMP_OUT();
      return;
    }
  if (isB) { STAMP_OUT(); return; }
  if (w >= 2) { VMWAIT(0); lds_add(sm, F_FIN, lane); }
  if (w < 4) {
    const int ob = w & 1;
    const u32 ro = OA_OFF + (u32)(h * 32 + 16 * ob + g) * 640;
    v4u x[8]; u64 ls[4];
    if ((w >> 1) >= OPOLL) { STAMP_OUT(); return; }
    if (ODELAY) asm volatile("s_sleep %0" :: "i"(ODELAY) : "memory");
    if (OSENT) {
      const u32 so = OA_OFF + (u32)(h * 32 + 16 * ob + c) * 640 + 512;
      _Pragma("nounroll") for (u32 n = 0;;) {
        const u64 z = ld_gran(ws, so);
        if (__builtin_popcount((u32)__ballot((u32)(z >> 32) == tag) & 0xFFFFu) >= OTHR || ++n > SPIN_CAP) break;
        asm volatile("s_sleep %0" :: "i"(OSLEEP) : "memory");
      }
      STAMP(11);
    }
    if (w < 2) lds_add(sm, F_FIN, lane);
    else asm volatile("s_sleep %0" :: "i"(OSTAG) : "memory");
    _Pragma("nounroll") for (u32 n = 0;;) {
      bool ok = true;
#pragma unroll
      for (int k = 0; k < 4; ++k) { x[2 * k] = ld2(rs, ro + k * 2560 + c * 32); x[2 * k + 1] = ld2(rs, ro + k * 2560 + c * 32 + 16); ls[k] = ld_gran(ws, ro + k * 2560 + 512); }
#pragma unroll
      for (int k = 0; k < 4; ++k) ok &= (x[2 * k].y == tag) & (x[2 * k].w == tag) & (x[2 * k + 1].y == tag) & (x[2 * k + 1].w == tag) & ((u32)(ls[k] >> 32) == tag);
      if (__all(ok) || ++n > SPIN_CAP) break;
      if (__builtin_amdgcn_readfirstlane(__hip_atomic_load((LAS u32*)(sm + F_FIN + 8 + 4 * ob), RLX_WG))) { STAMP_OUT(); return; }
      asm volatile("s_sleep %0" :: "i"(OSLEEP) : "memory");
    }
    STAMP(8);
    const float M = x32max(x16max(fmaxf(fmaxf(asf((u32)ls[0]), asf((u32)ls[1])), fmaxf(asf((u32)ls[2]), asf((u32)ls[3])))));
    f32x4 oc = ZERO4; float lw = 0.f;
#pragma unroll
    for (int k = 0; k < 4; ++k) {
      const float e = ex2(asf((u32)ls[k]) - M);
      const u32 pp = cvtpk(e, e);
      lw += asf(pp << 16);
      oc = mfma16(v4u{x[2 * k].x, x[2 * k].z, x[2 * k + 1].x, x[2 * k + 1].z}, v4u{pp & L.m0, pp & L.m1, pp & L.m2, pp & L.m3}, oc);
    }
    const float inv = rcp(x32sum(x16sum(lw)));
    if (c < 8) {
      const u32 xo = L_XBUF + ob * 256 + (16 * ((c >> 1) & 1) + 8 * (c & 1) + 4 * (g >> 1) + (c >> 2)) * 2;
#pragma unroll
      for (int r = 0; r < 4; ++r) *(LAS __bf16*)(sm + xo + (32 * ((2 * (g & 1) + (r >> 1))) + 2 * (r & 1)) * 2) = (__bf16)(oc[r] * inv);
    }
    lds_add(sm, F_FIN + 8 + 4 * ob, lane);
    STAMP_OUT();
    return;
  }
  {
    const int q = (lane >> 2) & 3, p = lane & 3;
    lds_wait(sm, F_FIN, 8);
    v4u wt[4][2];
#pragma unroll
    for (int k = 0; k < 4; ++k) {
      const u32 ra = (2 * k + (g >> 1)) * 16384 + (8 * (g & 1) + q) * 256 + 8 * (p & 1);
#pragma unroll
      for (int e = 0; e < 2; ++e) {
        const u32 ch = 4 * (w - 4) + 2 * e + (p >> 1);
        const v2u t0 = __builtin_bit_cast(v2u, __builtin_amdgcn_ds_read_tr16_b64_v4i16((LAS s16x4*)(sm + ra + (ch ^ ((q << 2) | (2 * (g & 1)))) * 16)));
        const v2u t1 = __builtin_bit_cast(v2u, __builtin_amdgcn_ds_read_tr16_b64_v4i16((LAS s16x4*)(sm + ra + 1024 + (ch ^ ((q << 2) | (2 * (g & 1) + 1))) * 16)));
        wt[k][e] = v4u{t0.x, t0.y, t1.x, t1.y};
        asm volatile("" : "+v"(wt[k][e]));
      }
    }
    u32 xa = L_XBUF + (c & 1) * 256 + g * 16, yo = YG_OFF + (u32)((((g & 1) * 64 + 16 * qt + 4 * (w - 4) + 2 * (g >> 1) + (c >> 3)) * 32 + h) * 8 + (c & 7)) * 8;
    asm volatile("" : "+v"(xa), "+v"(yo));
    _Pragma("nounroll") for (u32 n = 0; n < (SPIN_CAP << 4); ++n) {
      const u64 f12 = __hip_atomic_load((LAS u64*)(sm + F_FIN + 8), RLX_WG);
      if (__builtin_amdgcn_readfirstlane(((u32)f12 > 0) & ((u32)(f12 >> 32) > 0))) break;
    }
    asm volatile("" ::: "memory");
    STAMP(19);
    v4u xo[4];
#pragma unroll
    for (int k = 0; k < 4; ++k) xo[k] = *(LAS v4u*)(sm + xa + k * 64);
    __builtin_amdgcn_sched_barrier(0);
    f32x4 y0 = ZERO4, y1 = ZERO4;
#pragma unroll
    for (int k = 0; k < 4; ++k) { y0 = mfma16(xo[k], wt[k][0], y0); y1 = mfma16(xo[k], wt[k][1], y1); }
    st_gran(ws, yo, asu(g == 0 ? y0.x : g == 1 ? y0.y : g == 2 ? y1.x : y1.y), tag);
  }
  STAMP(9);
  final_reduce(sm, F_OUT, w - 4, ws, pOut, 0, bid, lane, tag);
  if (bid == 0 && w == 4 && lane == 0) *(GAS u32*)ws = tag;
  STAMP(10);
  STAMP_OUT();
}

extern "C" void kernel_launch(void* const* d_in, const int* in_sizes, int n_in, void* d_out, int out_size, void* d_ws, size_t ws_size, hipStream_t stream) {
  fused_decode<<<256, 576, 0, stream>>>((const u16*)d_in[7], (const u16*)d_in[6], (char*)d_ws, (const u16*)d_in[5], (const u16*)d_in[0], (const u16*)d_in[3],
                                        (const u16*)d_in[8], (const u16*)d_in[1], (const u16*)d_in[2], (const u16*)d_in[4], (const u16*)d_in[9],
                                        (const u16*)d_in[10], (u16*)d_out);
}
```

```cpp
#include <hip/hip_runtime.h>
#include <stdint.h>

typedef unsigned short u16;
typedef unsigned u32;
typedef unsigned long long u64;
#define GAS __attribute__((address_space(1)))
#define LAS __attribute__((address_space(3)))
typedef u32 v4u __attribute__((ext_vector_type(4)));
typedef u32 v2u __attribute__((ext_vector_type(2)));
typedef float f32x2 __attribute__((ext_vector_type(2)));
typedef float f32x4 __attribute__((ext_vector_type(4)));
typedef float f32x16 __attribute__((ext_vector_type(16)));
typedef __bf16 bf16x8 __attribute__((ext_vector_type(8)));
typedef __bf16 bf16x2 __attribute__((ext_vector_type(2)));
typedef short s16x4 __attribute__((ext_vector_type(4)));
#define RLX_AGENT __ATOMIC_RELAXED, __HIP_MEMORY_SCOPE_AGENT
#define RLX_WG __ATOMIC_RELAXED, __HIP_MEMORY_SCOPE_WORKGROUP

constexpr int SK = 8192;
constexpr int NA = 46, NT = 8, NX = 128 - 2 * NA - NT;
static_assert(8 * NA + 4 * NX + 4 * NT == 512 && NX >= 16 && NA >= 16 && NT == 8, "tile split");
constexpr float C2 = 0.08838834764831845f * 1.4426950408889634f;
constexpr u32 SPIN_CAP = 1u << 17;

constexpr u32 QG_OFF = 4096;
constexpr u32 QA_OFF = QG_OFF + 131072;
constexpr u32 OA_OFF = QA_OFF + 262144;
constexpr u32 LB_OFF = OA_OFF + 32 * 32 * 640;
constexpr u32 CX_OFF = LB_OFF + 32768;
constexpr u32 YG_OFF = CX_OFF + 131072;
constexpr u32 PB_OFF = YG_OFF + 524288;
constexpr u32 WS_USED = PB_OFF + 4194304;
constexpr u32 EXTRA = 131072;
constexpr u32 L_PART = EXTRA, L_XBUF = EXTRA + 8192, L_FLAG = EXTRA + 15040, L_MST = EXTRA + 15104, L_QIMG = EXTRA + 15360, L_MRG = EXTRA + 16384, L_XS = EXTRA + 16384;
constexpr u32 L_QB = EXTRA, L_PF = EXTRA + 2048, L_BM = EXTRA, L_BL = EXTRA + 10240, L_LSE = EXTRA + 11264, L_PARTB = 0;
constexpr u32 F_Q = L_FLAG, F_DRAIN = L_FLAG + 8, F_M = L_FLAG + 12, F_FIN = L_FLAG + 32, L_LBV = EXTRA + 11392;
constexpr u32 QA_T = 11500, QS_T = 16000, CODE_KB = 21;
constexpr int LDELAY = 25, CDELAY = 12, YDELAY = 15, OTHR = 14;

__device__ __forceinline__ u32 cvtpk(float lo, float hi) { bf16x2 p = {(__bf16)lo, (__bf16)hi}; return __builtin_bit_cast(u32, p); }
__device__ __forceinline__ f32x4 mfma16(v4u a, v4u b, f32x4 c) { return __builtin_amdgcn_mfma_f32_16x16x32_bf16(__builtin_bit_cast(bf16x8, a), __builtin_bit_cast(bf16x8, b), c, 0, 0, 0); }
__device__ __forceinline__ f32x4 mfma16h(v2u a, v2u b, f32x4 c) { return __builtin_amdgcn_mfma_f32_16x16x16bf16_1k(__builtin_bit_cast(s16x4, a), __builtin_bit_cast(s16x4, b), c, 0, 0, 0); }
__device__ __forceinline__ f32x16 mfma32(v4u a, v4u b, f32x16 c) { return __builtin_amdgcn_mfma_f32_32x32x16_bf16(__builtin_bit_cast(bf16x8, a), __builtin_bit_cast(bf16x8, b), c, 0, 0, 0); }
__device__ __forceinline__ float ex2(float x) { return __builtin_amdgcn_exp2f(x); }
__device__ __forceinline__ float rcp(float x) { return __builtin_amdgcn_rcpf(x); }
__device__ __forceinline__ float asf(u32 x) { return __builtin_bit_cast(float, x); }
__device__ __forceinline__ u32 asu(float x) { return __builtin_bit_cast(u32, x); }
template <int CTRL> __device__ __forceinline__ float dpp(float x) { return asf((u32)__builtin_amdgcn_mov_dpp((int)asu(x), CTRL, 0xf, 0xf, true)); }
__device__ __forceinline__ float x16max(float x) { auto s = __builtin_amdgcn_permlane16_swap(asu(x), asu(x), false, false); return fmaxf(asf(s[0]), asf(s[1])); }
__device__ __forceinline__ float x32max(float x) { auto s = __builtin_amdgcn_permlane32_swap(asu(x), asu(x), false, false); return fmaxf(asf(s[0]), asf(s[1])); }
__device__ __forceinline__ float x16sum(float x) { auto s = __builtin_amdgcn_permlane16_swap(asu(x), asu(x), false, false); return asf(s[0]) + asf(s[1]); }
__device__ __forceinline__ float x32sum(float x) { auto s = __builtin_amdgcn_permlane32_swap(asu(x), asu(x), false, false); return asf(s[0]) + asf(s[1]); }
__device__ __forceinline__ float row16max(float x) { x = fmaxf(x, dpp<0x128>(x)); x = fmaxf(x, dpp<0x124>(x)); x = fmaxf(x, dpp<0x122>(x)); return fmaxf(x, dpp<0x121>(x)); }
__device__ __forceinline__ float row16sum(float x) { x += dpp<0x128>(x); x += dpp<0x124>(x); x += dpp<0x122>(x); return x + dpp<0x121>(x); }
__device__ __forceinline__ float max4(f32x4 a) { return fmaxf(fmaxf(a.x, a.y), fmaxf(a.z, a.w)); }
__device__ __forceinline__ float sum4(f32x4 a) { return (a.x + a.y) + (a.z + a.w); }
#define ZERO4 f32x4{0.f, 0.f, 0.f, 0.f}

#define BAR() asm volatile("s_waitcnt lgkmcnt(0)\n\ts_barrier" ::: "memory")
#define VMWAIT(n) asm volatile("s_waitcnt vmcnt(" #n ")" ::: "memory")

template <int PACED, bool NT, bool WAR = true>
__device__ __forceinline__ void dma4(u64 base, u32 o0, u32 o1, u32 o2, u32 o3, u32 lds) {
  if (WAR) asm volatile("s_waitcnt lgkmcnt(0)" ::: "memory");
#define DMA_BODY(W, P) \
  asm volatile("s_mov_b32 m0, %5\n\ts_nop 0\n\t" W "global_load_lds_dwordx4 %0, %4" P "\n\t" W "global_load_lds_dwordx4 %1, %4 offset:1024" P "\n\t" \
               W "global_load_lds_dwordx4 %2, %4 offset:2048" P "\n\t" W "global_load_lds_dwordx4 %3, %4 offset:3072" P \
               :: "v"(o0), "v"(o1), "v"(o2), "v"(o3), "s"(base), "s"(lds) : "memory")
  if (PACED == 8) DMA_BODY("s_waitcnt vmcnt(7)\n\t", " nt");
  else if (PACED && NT) DMA_BODY("s_waitcnt vmcnt(3)\n\t", " nt");
  else if (PACED) DMA_BODY("s_waitcnt vmcnt(3)\n\t", "");
  else if (NT) DMA_BODY("", " nt");
  else DMA_BODY("", "");
#undef DMA_BODY
}

struct Lane { u32 m0, m1, m2, m3; };
__device__ __forceinline__ Lane make_lane(int c) {
  const int j0 = c & 7; const u32 hm = (j0 & 1) ? 0xFFFF0000u : 0x0000FFFFu;
  return Lane{(j0 >> 1) == 0 ? hm : 0u, (j0 >> 1) == 1 ? hm : 0u, (j0 >> 1) == 2 ? hm : 0u, (j0 >> 1) == 3 ? hm : 0u};
}
__device__ __forceinline__ v4u diag(v2u xs, int i, const Lane& L) {
  const u32 src = i < 2 ? xs.x : xs.y;
  const u32 pp = __builtin_amdgcn_perm(src, src, (i & 1) ? 0x03020302u : 0x01000100u);
  return v4u{pp & L.m0, pp & L.m1, pp & L.m2, pp & L.m3};
}
__device__ __forceinline__ f32x4 gemv4(const v4u* W, v2u xs, const Lane& L, f32x4 acc) {
#pragma unroll
  for (int i = 0; i < 4; ++i) acc = mfma16(W[i], diag(xs, i, L), acc);
  return acc;
}
__device__ __forceinline__ void ldsW(v4u (&W)[4], LAS char* sm, u32 unit, int g, int c) {
#pragma unroll
  for (int i = 0; i < 4; ++i) W[i] = *(LAS v4u*)(sm + unit + (4 * g + i) * 256 + c * 16);
}
template <int STRIDE> __device__ __forceinline__ float reduce8(LAS char* sm, u32 off) {
  float v[8];
  asm volatile("" : "+v"(off));
#pragma unroll
  for (int w = 0; w < 8; ++w) v[w] = *(LAS float*)(sm + off + w * STRIDE);
  __builtin_amdgcn_sched_barrier(0);
  return ((v[0] + v[1]) + (v[2] + v[3])) + ((v[4] + v[5]) + (v[6] + v[7]));
}
__device__ __forceinline__ void st_gran(char* ws, u32 off, u32 val, u32 tag) { __hip_atomic_store((GAS u64*)(ws + off), ((u64)tag << 32) | val, RLX_AGENT); }
__device__ __forceinline__ u64 ld_gran(char* ws, u32 off) { return __hip_atomic_load((GAS u64*)(ws + off), RLX_AGENT); }
__device__ __forceinline__ v4u ld2(__amdgpu_buffer_rsrc_t rs, u32 off) { return __builtin_amdgcn_raw_buffer_load_b128(rs, off, 0, 16); }
__device__ __forceinline__ bool spin(u32& n) { asm volatile("s_sleep 1" ::: "memory"); return ++n > SPIN_CAP; }
__device__ __forceinline__ bool spinl(u32& n) { asm volatile("s_sleep 6" ::: "memory"); return ++n > SPIN_CAP; }
__device__ __forceinline__ void sleep_until(u64 t0, u32 cyc) {
  _Pragma("nounroll") for (u32 n = 0; n < 4096 && __builtin_amdgcn_s_memtime() - t0 < cyc; ++n) asm volatile("s_sleep 2" ::: "memory");
}
__device__ __forceinline__ void lds_add(LAS char* sm, u32 off, int lane) {
  asm volatile("s_waitcnt lgkmcnt(0)" ::: "memory");
  if (lane == 0) __hip_atomic_fetch_add((LAS u32*)(sm + off), 1u, RLX_WG);
}
__device__ __forceinline__ void lds_wait(LAS char* sm, u32 off, u32 want) {
  for (u32 n = 0; n < (SPIN_CAP << 4); ++n) {
    if ((u32)__builtin_amdgcn_readfirstlane(__hip_atomic_load((LAS u32*)(sm + off), RLX_WG)) >= want) break;
  }
  asm volatile("" ::: "memory");
}
__device__ __forceinline__ void qa_issue(v4u (&q)[16], __amdgpu_buffer_rsrc_t rs, int xcd, int b, int lane) {
#pragma unroll
  for (int k = 0; k < 16; ++k) q[k] = ld2(rs, QA_OFF + (u32)(((16 * xcd + k) * 256 + b * 128 + 2 * lane) * 8));
}
__device__ __forceinline__ v4u qa_finish(v4u (&q)[16], __amdgpu_buffer_rsrc_t rs, int xcd, int b, int lane, u32 tag) {
  _Pragma("nounroll") for (u32 n = 0;;) {
    bool ok = true;
#pragma unroll
    for (int k = 0; k < 16; ++k) ok &= (q[k].y == tag) & (q[k].w == tag);
    if (__all(ok) || spinl(n)) break;
    qa_issue(q, rs, xcd, b, lane);
  }
  v4u r;
#pragma unroll
  for (int hh = 0; hh < 4; ++hh)
    r[hh] = cvtpk((asf(q[4 * hh].x) + asf(q[4 * hh + 1].x)) + (asf(q[4 * hh + 2].x) + asf(q[4 * hh + 3].x)),
                  (asf(q[4 * hh].z) + asf(q[4 * hh + 1].z)) + (asf(q[4 * hh + 2].z) + asf(q[4 * hh + 3].z)));
  return r;
}
__device__ __forceinline__ void put_qa(LAS char* sm, v4u r, int lane) {
#pragma unroll
  for (int hh = 0; hh < 4; ++hh) *(LAS u32*)(sm + L_QIMG + hh * 256 + lane * 4) = r[hh];
  lds_add(sm, F_Q, lane);
}
__device__ __forceinline__ void final_reduce(char* ws, u64 pOut, int which, int idx, int lane, u32 tag) {
  const u32 yo = YG_OFF + (u32)((which * 128 + idx) * 256 + lane) * 8;
  const u32 oo = (u32)(((idx >> 6) * 1024 + which * 512 + (idx & 63) * 8 + (lane & 7)) * 2);
  asm volatile("s_sleep %0" :: "i"(YDELAY) : "memory");
  u64 y0, y1, y2, y3;
  _Pragma("nounroll") for (u32 n = 0;;) {
    y0 = ld_gran(ws, yo); y1 = ld_gran(ws, yo + 512); y2 = ld_gran(ws, yo + 1024); y3 = ld_gran(ws, yo + 1536);
    if (__all(((u32)(y0 >> 32) == tag) & ((u32)(y1 >> 32) == tag) & ((u32)(y2 >> 32) == tag) & ((u32)(y3 >> 32) == tag)) || spin(n)) break;
  }
  float x = (asf((u32)y0) + asf((u32)y1)) + (asf((u32)y2) + asf((u32)y3));
  x += dpp<0x128>(x);
  x = x32sum(x16sum(x));
  if (lane < 8) *(GAS __bf16*)(pOut + oo) = (__bf16)x;
}

__global__ void __launch_bounds__(576, 1)
fused_decode(const u16* __restrict__ Bwq, const u16* __restrict__ Bc, char* ws, const u16* __restrict__ Bx, const u16* __restrict__ Ax, const u16* __restrict__ Awq,
             const u16* __restrict__ Bwuk, const u16* __restrict__ Ak, const u16* __restrict__ Av, const u16* __restrict__ Awo, const u16* __restrict__ Bwuv,
             const u16* __restrict__ Bwo, u16* __restrict__ out) {
  __shared__ __attribute__((aligned(16))) char smem[163840];
  u64 pc0; asm volatile("s_getpc_b64 %0" : "=s"(pc0));
  const u64 ht0 = __builtin_amdgcn_s_memtime();
  LAS char* sm = (LAS char*)smem;
  const u32 sm0 = (u32)(size_t)sm;
  const int tid = threadIdx.x, lane = tid & 63, w = __builtin_amdgcn_readfirstlane(tid >> 6), g = lane >> 4, c = lane & 15;
  const int bid = blockIdx.x, xcd = bid & 7, isB = bid >> 7, jj = (bid >> 3) & 15, qt = jj & 3, h = 4 * xcd + (jj >> 2);
  u64 pAx = (u64)Ax, pAk = (u64)Ak, pAv = (u64)Av, pAwq = (u64)Awq, pAwo = (u64)Awo, pBx = (u64)Bx, pBc = (u64)Bc, pBwq = (u64)Bwq, pBwuk = (u64)Bwuk, pBwuv = (u64)Bwuv, pBwo = (u64)Bwo, pOut = (u64)out;
  const u32 ring = sm0 + w * 16384, ringo = w * 16384;
  const u32 wrow = (u32)(16 * w * 8192 + h * 256);
  const u32 wo0 = g * 8192 + c * 16, wo1 = wo0 + 31744, wo2 = wo0 + 2 * 31744, wo3 = wo0 + 3 * 31744, lo16 = lane * 16;
  const int bB = xcd >> 2, sB = (xcd & 3) * 16 + jj;
  const u64 cb = pBc + (u32)((bB * SK + sB * 128 + 4 * w) * 1024);
  const u32 co0 = lo16 ^ ((w & 3) * 16), cl = sm0 + w * 4096;
  if (w < 8) {
    asm volatile("s_nop 4" ::: "memory");
    dma4<false, false, false>(isB ? cb : pBwq + (wrow + (u32)qt * (128 * 8192)), isB ? co0 : wo0, isB ? co0 ^ 64 : wo1, isB ? co0 ^ 128 : wo2, isB ? co0 ^ 192 : wo3, isB ? cl : ring + 4096);
  }
  asm volatile("" : "+s"(pAx), "+s"(pAk), "+s"(pAv), "+s"(pAwo), "+s"(pBx), "+s"(pAwq), "+s"(pBwuk), "+s"(pBwuv), "+s"(pBwo), "+s"(pOut));
  u32 tag = *(const __attribute__((address_space(4))) u32*)(u64)ws;
  const __amdgpu_buffer_rsrc_t rs = __builtin_amdgcn_make_buffer_rsrc(ws, 0, (int)WS_USED, 0x00020000);
  const int isT = isB & ~jj & 1;
  const int bA = jj >> 3, sA = isB ? 8 + 4 * isT + ((jj >> 1) & 3) : (jj & 7);

  if (w == 8) {
    asm volatile("" : "+s"(tag)); tag += 1u;
    __builtin_amdgcn_s_setprio(3);
    u32 wa[6];
    {
      const u64 pc = pc0 & ~255ull;
      const u32 co = (u32)((jj & 3) * 6 * 1024 + lane * 16);
#pragma unroll
      for (int k = 0; k < 5; ++k) wa[k] = *(const GAS u32*)(pc + min(co + k * 1024 + (k == 4 ? (lane & 1) * 1024 : 0), (u32)(CODE_KB * 1024)));
      wa[5] = *(const GAS u32*)pOut;
      asm volatile("" ::: "memory");
    }
    if (!isB) {
      if (lane < 16) *(LAS u32*)(sm + L_FLAG + lane * 4) = 0u;
      BAR(); BAR();
      sleep_until(ht0, QA_T);
      v4u q[16];
      qa_issue(q, rs, xcd, bA, lane);
      put_qa(sm, qa_finish(q, rs, xcd, bA, lane, tag), lane);
    } else {
      BAR();
      v4u q[16];
      sleep_until(ht0, QA_T + 1000);
      qa_issue(q, rs, xcd, bA, lane);
      sleep_until(ht0, QS_T);
      BAR(); BAR(); BAR(); BAR(); BAR();
      if (lane < 16) *(LAS u32*)(sm + L_FLAG + lane * 4) = 0u;
      BAR(); BAR();
      put_qa(sm, qa_finish(q, rs, xcd, bA, lane, tag), lane);
      lds_wait(sm, F_DRAIN, 8);
      if (lane < 32) st_gran(ws, LB_OFF + (u32)((bB * 64 + sB) * 32 + lane) * 8, *(LAS u32*)(sm + L_LSE + lane * 4), tag);
      if (isT) {
        u64 l0, l1;
        asm volatile("s_sleep %0" :: "i"(LDELAY) : "memory");
        _Pragma("nounroll") for (u32 n = 0;;) {
          l0 = ld_gran(ws, LB_OFF + (u32)(lane * 32 + h) * 8); l1 = ld_gran(ws, LB_OFF + (u32)((64 + lane) * 32 + h) * 8);
          if (__all(((u32)(l0 >> 32) == tag) & ((u32)(l1 >> 32) == tag)) || spin(n)) break;
        }
        const float f0 = asf((u32)l0), f1 = asf((u32)l1);
        const float e0 = ex2(f0 - x32max(x16max(row16max(f0)))), e1 = ex2(f1 - x32max(x16max(row16max(f1))));
        *(LAS __bf16*)(sm + L_LBV + lane * 2) = (__bf16)(e0 * rcp(x32sum(x16sum(row16sum(e0)))));
        *(LAS __bf16*)(sm + L_LBV + 128 + lane * 2) = (__bf16)(e1 * rcp(x32sum(x16sum(row16sum(e1)))));
        asm volatile("s_waitcnt lgkmcnt(0)" ::: "memory");
      }
    }
    asm volatile("" :: "v"(wa[0]), "v"(wa[1]), "v"(wa[2]), "v"(wa[3]), "v"(wa[4]), "v"(wa[5]));
    return;
  }

  const u32 sw = (c ^ g) * 16;
  const u32 ko0 = g * 256 + sw, ko1 = g * 256 + (sw ^ 64), ko2 = g * 256 + (sw ^ 128), ko3 = g * 256 + (sw ^ 192);
  const int nt = isT ? NT : isB ? NX : NA, T = (nt >> 3) + (w < (nt & 7));
  const u32 kv_off = (u32)((bA * 8 + xcd) * SK + 16 * ((isT ? 8 * NA + 4 * NX + NT * (sA - 12) : isB ? 8 * NA + NX * (sA - 8) : NA * sA) + w)) * 256;
  u64 kb = pAk + kv_off, vb = pAv + kv_off, fb = pAwo + (u32)((128 * h + 16 * w) * 1024 + qt * 256);
  asm volatile("" : "+s"(kb), "+s"(vb), "+s"(fb));
  const int tb = (tid >> 5) & 1, tcol = 32 * ((tid >> 6) & 3) + (tid & 31), tr = (tid & 0xE0) | ((tid & 7) << 2) | ((tid >> 3) & 3);
  const Lane L = make_lane(c);

  const int half = (jj >> 1) & 1;
  v4u UW[8], X[8];
  if (!isB) {
    {
      const u64 src = pBx + (u32)(((lane >> 1) & 1) * 512 + 128 * ((lane >> 2) & 3) + 16 * w + 8 * (lane & 1)) * 2;
      asm volatile("s_mov_b32 m0, %1\n\ts_nop 4\n\tglobal_load_lds_dwordx4 %0, off" :: "v"(src), "s"(sm0 + L_XS + w * 1024) : "memory");
    }
    const u64 wq_b = pBwq + wrow;
    const u32 xso = L_XS + w * 1024 + (c >> 3) * 32 + g * 8;
    f32x4 acc = ZERO4;
    v4u W[4];
    asm volatile("" : "+s"(tag)); tag += 1u;
    dma4<false, true, false>(pBwuk + (wrow + (u32)qt * (128 * 8192)), g * 8192 + sw, g * 8192 + (sw ^ 64) + 31744, g * 8192 + (sw ^ 128) + 2 * 31744, g * 8192 + (sw ^ 192) + 3 * 31744, ring + 3 * 4096);
    VMWAIT(4);
    dma4<false, false, false>(wq_b + (u32)(((qt + 1) & 3) * (128 * 8192)), wo0, wo1, wo2, wo3, ring + 2 * 4096);
    dma4<false, false, false>(wq_b + (u32)(((qt + 2) & 3) * (128 * 8192)), wo0, wo1, wo2, wo3, ring);
    ldsW(W, sm, ringo + 4096, g, c); acc = gemv4(W, *(LAS v2u*)(sm + xso + qt * 64), L, acc);
    dma4<false, false>(wq_b + (u32)(((qt + 3) & 3) * (128 * 8192)), wo0, wo1, wo2, wo3, ring + 4096);
    VMWAIT(8);
    ldsW(W, sm, ringo + 2 * 4096, g, c); acc = gemv4(W, *(LAS v2u*)(sm + xso + ((qt + 1) & 3) * 64), L, acc);
    VMWAIT(4);
    ldsW(W, sm, ringo, g, c); acc = gemv4(W, *(LAS v2u*)(sm + xso + ((qt + 2) & 3) * 64), L, acc);
    dma4<true, true>(kb, ko0, ko1, ko2, ko3, ring);
    ldsW(W, sm, ringo + 4096, g, c); acc = gemv4(W, *(LAS v2u*)(sm + xso + ((qt + 3) & 3) * 64), L, acc);
    *(LAS f32x4*)(sm + L_PART + w * 1024 + lo16) = acc;
    BAR();
    if (tid < 256) *(LAS __bf16*)(sm + L_XBUF + (tb * 128 + tcol) * 2) = (__bf16)reduce8<1024>(sm, L_PART + tr * 4);
    BAR();
    {
      f32x4 qa = ZERO4;
#pragma unroll
      for (int k = 0; k < 4; ++k)
        qa = mfma16(*(LAS v4u*)(sm + ringo + 3 * 4096 + c * 256 + ((4 * k + g) ^ c) * 16), *(LAS v4u*)(sm + L_XBUF + (c & 1) * 256 + k * 64 + g * 16), qa);
      if (c < 2) {
        __builtin_amdgcn_raw_buffer_store_b128(v4u{cvtpk(qa.x, qa.y), tag, cvtpk(qa.z, qa.w), tag}, rs, QG_OFF + (u32)c * 65536 + (u32)(((8 * qt + w) * 32 + h) * 4 + g) * 16, 0, 16);
      }
    }
  } else {
    const int h2 = lane >> 5, hd = lane & 31;
    v2u xs;
    asm volatile("global_load_dwordx2 %0, %1, %2" : "=v"(xs) : "v"((u32)(((c >> 3) * 512 + 128 * qt + 16 * w + 4 * g) * 2)), "s"(pAx) : "memory");
    dma4<true, true, false>(pAwq + (wrow + (u32)qt * (128 * 8192)), wo0, wo1, wo2, wo3, sm0 + EXTRA + w * 4096);
    asm volatile("" : "+v"(xs));
    asm volatile("" : "+s"(tag)); tag += 1u;
    dma4<true, true, false>(cb + 32768, co0, co0 ^ 64, co0 ^ 128, co0 ^ 192, cl + 32768);
    v4u KV[8];
#pragma unroll
    for (int i = 0; i < 4; ++i) KV[i] = __builtin_nontemporal_load((const GAS v4u*)(kb + (u32)((ko0 ^ (64 * i)) + 1024 * i)));
    const u64 ub = isT ? pBwuv + (u32)((256 * half + 32 * w) * 8192 + h * 256) : kb + 32768;
    const u32 uo = isT ? g * 32768 + c * 16 : ko0, us = isT ? 8192 : 1024, ux = isT ? 0 : 64;
    {
      v4u W[4];
      ldsW(W, sm, EXTRA + w * 4096, g, c);
      const f32x4 acc = gemv4(W, xs, L, ZERO4);
      *(LAS f32x4*)(sm + EXTRA + w * 4096 + lo16) = acc;
    }
    BAR();
    if (tid < 256) st_gran(ws, QA_OFF + (u32)((h * 4 + qt) * 256 + tb * 128 + tcol) * 8, asu(reduce8<4096>(sm, EXTRA + tr * 4)), tag);
    dma4<true, true, false>(cb + 2 * 32768, co0, co0 ^ 64, co0 ^ 128, co0 ^ 192, cl + 2 * 32768);
    dma4<true, true, false>(cb + 3 * 32768, co0, co0 ^ 64, co0 ^ 128, co0 ^ 192, cl + 3 * 32768);
    BAR();
    {
      const u32 qg = QG_OFF + (u32)bB * 65536 + (u32)tid * 16, ql = L_QB + (tid >> 7) * 1024 + ((tid >> 1) & 1) * 512 + ((tid >> 2) & 31) * 16 + (tid & 1) * 8;
      v4u q[8]; bool ok = true;
#pragma unroll
      for (int k = 0; k < 8; ++k) q[k] = ld2(rs, qg + ((k + jj) & 7) * 8192);
      __builtin_amdgcn_sched_barrier(0);
      __builtin_amdgcn_s_barrier();
      __builtin_amdgcn_sched_barrier(0);
#pragma unroll
      for (int i = 0; i < 4; ++i) KV[4 + i] = __builtin_nontemporal_load((const GAS v4u*)(vb + (u32)(lo16 + 1024 * i)));
      __builtin_amdgcn_sched_barrier(0);
#pragma unroll
      for (int k = 0; k < 8; ++k) { ok &= (q[k].y == tag) & (q[k].w == tag); *(LAS v2u*)(sm + ql + ((k + jj) & 7) * 4096) = v2u{q[k].x, q[k].z}; }
      _Pragma("nounroll") for (u32 n = 0; !__all(ok) && !spinl(n);) {
        ok = true;
#pragma unroll 1
        for (int k = 0; k < 8; k += 4) {
          v4u x[4];
#pragma unroll
          for (int j = 0; j < 4; ++j) x[j] = ld2(rs, qg + (k + j) * 8192);
#pragma unroll
          for (int j = 0; j < 4; ++j) { ok &= (x[j].y == tag) & (x[j].w == tag); *(LAS v2u*)(sm + ql + (k + j) * 4096) = v2u{x[j].x, x[j].z}; }
        }
      }
    }
    BAR();
    f32x16 sc;
#pragma unroll
    for (int k = 0; k < 16; ++k) sc[k] = 0.f;
    float mw = 0.f;
    if (w < 4) {
      const u32 ca = (32 * w + hd) * 1024, cx = (h2 ^ (((hd & 3) << 2) | ((hd >> 2) & 3))) * 16;
      u32 qo = L_QB + lo16;
      asm volatile("" : "+v"(qo));
      v4u fa[3][2], fq[3][2];
#define QK_LOAD(B) _Pragma("unroll") for (int k = 0; k < 2; ++k) { fa[(B) % 3][k] = *(LAS v4u*)(sm + ca + ((B) >> 2) * 256 + (cx ^ ((2 * ((B) & 3) + k) * 32))); fq[(B) % 3][k] = *(LAS v4u*)(sm + qo + (2 * (B) + k) * 1024); }
      QK_LOAD(0) QK_LOAD(1)
      BAR();
#pragma unroll
      for (int bb = 0; bb < 16; ++bb) {
        if (bb < 14) { QK_LOAD(bb + 2) }
        __builtin_amdgcn_sched_barrier(0);
#pragma unroll
        for (int k = 0; k < 2; ++k) sc = mfma32(fa[bb % 3][k], fq[bb % 3][k], sc);
        __builtin_amdgcn_sched_barrier(0);
      }
#undef QK_LOAD
      mw = sc[0];
#pragma unroll
      for (int k = 1; k < 16; ++k) mw = fmaxf(mw, sc[k]);
      mw = x32max(mw);
    } else BAR();
#pragma unroll
    for (int i = 0; i < 4; ++i) UW[i] = __builtin_nontemporal_load((const GAS v4u*)(ub + (u32)((uo ^ (ux * i)) + us * i)));
    asm volatile("" ::: "memory");
    if (w < 4 && lane < 32) *(LAS float*)(sm + L_BM + hd * 16 + w * 4) = mw;
    BAR();
    if (w < 4) {
      const float M = C2 * max4(*(LAS f32x4*)(sm + L_BM + hd * 16));
      float ls = 0.f;
#pragma unroll
      for (int k = 0; k < 16; ++k) { sc[k] = ex2(fmaf(sc[k], C2, -M)); ls += sc[k]; }
      *(LAS v4u*)(sm + L_PF + w * 2048 + lo16) = v4u{cvtpk(sc[0], sc[1]), cvtpk(sc[2], sc[3]), cvtpk(sc[4], sc[5]), cvtpk(sc[6], sc[7])};
      *(LAS v4u*)(sm + L_PF + w * 2048 + 1024 + lo16) = v4u{cvtpk(sc[8], sc[9]), cvtpk(sc[10], sc[11]), cvtpk(sc[12], sc[13]), cvtpk(sc[14], sc[15])};
      ls = x32sum(ls);
      if (lane < 32) *(LAS float*)(sm + L_BL + hd * 16 + w * 4) = ls;
    }
    BAR();
    {
      const int q = (lane >> 2) & 3, p = lane & 3, blk = g & 1;
      const u32 base = (4 * h2 + q) * 1024 + (w >> 1) * 256 + 8 * (p & 1), lo4 = 8 * (w & 1) + 2 * blk + (p >> 1);
      u32 po = L_PF + lo16;
      asm volatile("" : "+v"(po));
      const float Ls = sum4(*(LAS f32x4*)(sm + L_BL + hd * 16));
      const float inv = rcp(Ls);
      if (tid < 32) *(LAS float*)(sm + L_LSE + tid * 4) = fmaf(C2, max4(*(LAS f32x4*)(sm + L_BM + hd * 16)), __builtin_amdgcn_logf(Ls));
      const u32 so = hd * 1024 + (w >> 1) * 256 + 8 * h2, sx = (8 * (w & 1)) ^ (((hd & 3) << 2) | ((hd >> 2) & 3));
      const int hr = lane >> 2;
      const u32 ro = hr * 1024 + (w >> 1) * 256, rx = (8 * (w & 1) + p) ^ (((hr & 3) << 2) | (hr >> 2));
      const u32 go = PB_OFF + (u32)(((hr * 2 + (w >> 2)) * 2 + bB) * 64 + sB) * 512 + (w & 3) * 128 + p * 16;
#define TRR(A) __builtin_bit_cast(v2u, __builtin_amdgcn_ds_read_tr16_b64_v4i16((LAS s16x4*)(sm + (A))))
#pragma unroll
      for (int e = 0; e < 2; ++e) {
        const u32 a0 = base + 16 * ((lo4 + 4 * e) ^ ((q << 2) | h2)), a1 = base + 8192 + 16 * ((lo4 + 4 * e) ^ ((q << 2) | (2 + h2)));
        f32x16 o;
#pragma unroll
        for (int k = 0; k < 16; ++k) o[k] = 0.f;
        v4u pb[3]; v2u t0[3], t1[3];
#define PV_LOAD(K) { pb[(K) % 3] = *(LAS v4u*)(sm + po + (K) * 1024); t0[(K) % 3] = TRR(a0 + (K) * 16384); t1[(K) % 3] = TRR(a1 + (K) * 16384); }
        PV_LOAD(0) PV_LOAD(1)
#pragma unroll
        for (int ks = 0; ks < 8; ++ks) {
          if (ks < 6) PV_LOAD(ks + 2)
          __builtin_amdgcn_sched_barrier(0);
          o = mfma32(v4u{t0[ks % 3].x, t0[ks % 3].y, t1[ks % 3].x, t1[ks % 3].y}, pb[ks % 3], o);
          __builtin_amdgcn_sched_barrier(0);
        }
#undef PV_LOAD
#pragma unroll
        for (int k = 0; k < 4; ++k)
          *(LAS v2u*)(sm + so + (sx ^ (4 * e + k)) * 16) = v2u{cvtpk(o[4 * k] * inv, o[4 * k + 1] * inv), cvtpk(o[4 * k + 2] * inv, o[4 * k + 3] * inv)};
        asm volatile("s_waitcnt lgkmcnt(0)" ::: "memory");
        const v4u v0 = *(LAS v4u*)(sm + ro + (rx ^ (4 * e)) * 16), v1 = *(LAS v4u*)(sm + ro + 16384 + (rx ^ (4 * e)) * 16);
        __builtin_amdgcn_raw_buffer_store_b128(v0, rs, go + e * 64, 0, 16);
        __builtin_amdgcn_raw_buffer_store_b128(v1, rs, go + e * 64 + 16 * 2 * 2 * 64 * 512, 0, 16);

      }
#undef TRR
    }
    BAR();
    if (!isT) {
      dma4<false, true, false>(vb + 32768, lo16, lo16, lo16, lo16, ring + 3 * 4096);
#pragma unroll
      for (int i = 0; i < 4; ++i) *(LAS v4u*)(sm + ringo + 8192 + i * 1024 + lo16) = UW[i];
    }
#pragma unroll
    for (int i = 0; i < 8; ++i) *(LAS v4u*)(sm + ringo + i * 1024 + lo16) = KV[i];
    if (isT) VMWAIT(0); else VMWAIT(4);
    lds_add(sm, F_DRAIN, lane);
  }
  if (!isB) {
    dma4<true, true>(vb, lo16, lo16, lo16, lo16, ring + 4096);
    dma4<true, true>(kb + 32768, ko0, ko1, ko2, ko3, ring + 2 * 4096);
    dma4<true, true>(vb + 32768, lo16, lo16, lo16, lo16, ring + 3 * 4096);
  }
  lds_wait(sm, F_Q, 1);
  float Mw;
  {
    v4u qf[4];
#pragma unroll
    for (int k = 0; k < 4; ++k) qf[k] = *(LAS v4u*)(sm + L_QIMG + (c & 3) * 256 + k * 64 + g * 16);
    const u32 pm0 = (c >> 2) == 0 ? 0x0000FFFFu : (c >> 2) == 1 ? 0xFFFF0000u : 0u, pm1 = (c >> 2) == 2 ? 0x0000FFFFu : (c >> 2) == 3 ? 0xFFFF0000u : 0u;
    float m = -INFINITY, l = 0.f;
    f32x4 o0 = ZERO4, o1 = ZERO4;
    const u32 kr = ringo + c * 256, vr = ringo + 4096 + g * 1024 + c * 16;
    v2u y[4];
#define QK_STEP(SO) { \
      const f32x4 sa = mfma16(*(LAS v4u*)(sm + kr + (SO) + ((8 + g) ^ c) * 16), qf[2], mfma16(*(LAS v4u*)(sm + kr + (SO) + ((0 + g) ^ c) * 16), qf[0], ZERO4)) \
                     + mfma16(*(LAS v4u*)(sm + kr + (SO) + ((12 + g) ^ c) * 16), qf[3], mfma16(*(LAS v4u*)(sm + kr + (SO) + ((4 + g) ^ c) * 16), qf[1], ZERO4)); \
      const float mn = fmaxf(m, x32max(x16max(max4(sa)))); \
      const float al = ex2((m - mn) * C2), mc = mn * C2; \
      m = mn; \
      const float p[4] = {ex2(fmaf(sa.x, C2, -mc)), ex2(fmaf(sa.y, C2, -mc)), ex2(fmaf(sa.z, C2, -mc)), ex2(fmaf(sa.w, C2, -mc))}; \
      l = fmaf(l, al, (p[0] + p[1]) + (p[2] + p[3])); \
      o0 *= al; o1 *= al; \
      _Pragma("unroll") for (int i = 0; i < 4; ++i) { const u32 pp = cvtpk(p[i], p[i]); y[i] = v2u{pp & pm0, pp & pm1}; } }
#define PV_STEP(SO) _Pragma("unroll") for (int i = 0; i < 4; ++i) { \
      const v4u V = *(LAS v4u*)(sm + vr + (SO) + i * 256); \
      o0 = mfma16h(v2u{V.x, V.y}, y[i], o0); o1 = mfma16h(v2u{V.z, V.w}, y[i], o1); }
#pragma unroll 1
    for (int t = -1; t < T - 1; ++t) {
      const u32 so = (t & 1) * 8192;
      if (t >= 0) {
        if (t + 2 < T) dma4<8, true>(kb + (u32)((t + 2) * 32768), ko0, ko1, ko2, ko3, ring + so);
        else VMWAIT(4);
        PV_STEP(so)
      }
      QK_STEP(so ^ 8192)
      if (t >= 0 && t + 2 < T) dma4<8, true>(vb + (u32)((t + 2) * 32768), lo16, lo16, lo16, lo16, ring + so + 4096);
    }
    if (lane < 4) *(LAS float*)(sm + L_MST + lane * 32 + w * 4) = m;
    lds_add(sm, F_M, lane);
    l = x32sum(x16sum(l));
    u32 mo = L_MRG + w * 2048 + (c & 3) * 512 + ((4 * g + (c >> 2)) ^ (2 * (c & 3))) * 16, lo = L_MST + 128 + lane * 32 + w * 4, so = ((T - 1) & 1) * 8192;
    asm volatile("" : "+v"(mo), "+v"(lo), "+v"(l));
    lds_wait(sm, F_M, 8);
    Mw = fmaxf(max4(*(LAS f32x4*)(sm + L_MST + (c & 3) * 32)), max4(*(LAS f32x4*)(sm + L_MST + (c & 3) * 32 + 16)));
    float f = ex2((m - Mw) * C2);
    asm volatile("" : "+v"(f), "+v"(Mw));
    if (!isB) {
      const u32 f0 = g * 1024, fx = (c ^ (4 * g)) * 16;
      dma4<false, true, false>(fb, f0 + fx, f0 + 3072 + (fx ^ 16), f0 + 2 * 3072 + (fx ^ 32), f0 + 3 * 3072 + (fx ^ 48), ring);
      VMWAIT(4);
    } else if (!isT) VMWAIT(0);
    {
      v4u V[4];
#pragma unroll
      for (int i = 0; i < 4; ++i) V[i] = *(LAS v4u*)(sm + vr + so + i * 256);
      __builtin_amdgcn_sched_barrier(0);
#pragma unroll
      for (int i = 0; i < 4; ++i) { o0 = mfma16h(v2u{V[i].x, V[i].y}, y[i], o0); o1 = mfma16h(v2u{V[i].z, V[i].w}, y[i], o1); }
    }
    *(LAS f32x4*)(sm + mo) = o0 * f;
    *(LAS f32x4*)(sm + mo + 256) = o1 * f;
    if (lane < 4) *(LAS float*)(sm + lo) = l * f;
#undef QK_STEP
#undef PV_STEP
  }
  {
    u32 ro = L_MRG + w * 512 + ((lane & 31) ^ (2 * w)) * 16, go = OA_OFF + (u32)((4 * xcd + w) * 32 + bA * 16 + sA) * 640 + lane * 16;
    const float Mc = C2 * asf(__builtin_amdgcn_readlane(asu(Mw), w & 3));
    asm volatile("" : "+v"(ro), "+v"(go));
    BAR();
    if (isT) {
#pragma unroll
      for (int i = 0; i < 8; ++i) X[i] = ld2(rs, PB_OFF + (u32)((h * 2 + half) * 128 + 16 * w + 4 * g + (i & 3)) * 512 + (i >> 2) * 256 + c * 16);
      asm volatile("s_sleep 2" ::: "memory");
#pragma unroll
      for (int i = 0; i < 4; ++i) UW[4 + i] = __builtin_nontemporal_load((const GAS v4u*)(pBwuv + (u32)((256 * half + 32 * w + 16 + 4 * g + i) * 8192 + h * 256 + c * 16)));
      asm volatile("" ::: "memory");
    }
    if (w < 4) {
      f32x4 v[8];
#pragma unroll
      for (int ww = 0; ww < 8; ++ww) v[ww] = *(LAS f32x4*)(sm + ro + ww * 2048);
      const f32x4 la = *(LAS f32x4*)(sm + L_MST + 128 + w * 32), lb = *(LAS f32x4*)(sm + L_MST + 128 + w * 32 + 16);
      __builtin_amdgcn_sched_barrier(0);
      const float Ls = sum4(la + lb), inv = rcp(Ls);
      const f32x4 a = (((v[0] + v[1]) + (v[2] + v[3])) + ((v[4] + v[5]) + (v[6] + v[7]))) * inv;
      u32 lse = asu(Mc + __builtin_amdgcn_logf(Ls));
      asm volatile("" : "+v"(lse));
      const u32 p0 = lane < 32 ? cvtpk(a.x, a.y) : lse;
      if (lane < 40) __builtin_amdgcn_raw_buffer_store_b128(v4u{p0, tag, cvtpk(a.z, a.w), tag}, rs, go, 0, 16);
    }
  }
    if (isT) {
      v2u xw = *(LAS v2u*)(sm + L_LBV + w * 32 + g * 8);
      if ((c >> 3) != (w >> 2)) xw = v2u{0u, 0u};
      *(LAS f32x4*)(sm + L_PART + w * 1024 + lo16) = gemv4(X, xw, L, ZERO4);
      *(LAS f32x4*)(sm + L_PARTB + w * 1024 + lo16) = gemv4(X + 4, xw, L, ZERO4);
      asm volatile("" ::: "memory");
      v4u FW[8];
      const u32 po = (tid < 256 ? L_PART : L_PARTB) + tr * 4;
      BAR();
      {
        const u32 pq = po + tb * 4096;
        const float p0 = *(LAS float*)(sm + pq), p1 = *(LAS float*)(sm + pq + 1024), p2 = *(LAS float*)(sm + pq + 2048), p3 = *(LAS float*)(sm + pq + 3072);
        *(LAS __bf16*)(sm + L_XBUF + (tb * 256 + (tid >> 8) * 128 + tcol) * 2) = (__bf16)((p0 + p1) + (p2 + p3));
      }
      BAR();
      const u32 xo = L_XBUF + (c >> 3) * 512 + w * 64 + g * 8;
      *(LAS f32x4*)(sm + L_PART + w * 1024 + lo16) = gemv4(UW + 4, *(LAS v2u*)(sm + xo + 32), L, gemv4(UW, *(LAS v2u*)(sm + xo), L, ZERO4));
      BAR();
      float own = 0.f;
      if (w < 4) {
        own = reduce8<1024>(sm, po);
        st_gran(ws, CX_OFF + (u32)((h * 2 + half) * 256 + tid) * 8, asu(own), tag);
      } else asm volatile("s_sleep 3" ::: "memory");
#pragma unroll
      for (int i = 0; i < 8; ++i) FW[i] = __builtin_nontemporal_load((const GAS v4u*)(pBwo + (u32)((128 * h + 16 * w + 4 * g + (i & 3)) * 1024 + half * 512 + (i >> 2) * 256 + c * 16)));
      asm volatile("" ::: "memory");
      if (w < 4) {
        u64 x;
        asm volatile("s_sleep %0" :: "i"(CDELAY) : "memory");
        _Pragma("nounroll") for (u32 n = 0;;) { x = ld_gran(ws, CX_OFF + (u32)((h * 2 + (half ^ 1)) * 256 + tid) * 8); if (__all((u32)(x >> 32) == tag) || spin(n)) break; }
        *(LAS __bf16*)(sm + L_XBUF + (tb * 128 + tcol) * 2) = (__bf16)(half ? asf((u32)x) + own : own + asf((u32)x));
      }
      BAR();
      const v2u xc = *(LAS v2u*)(sm + L_XBUF + (c >> 3) * 256 + w * 32 + g * 8);
      *(LAS f32x4*)(sm + L_PART + w * 1024 + lo16) = gemv4(FW, xc, L, ZERO4);
      *(LAS f32x4*)(sm + L_PARTB + w * 1024 + lo16) = gemv4(FW + 4, xc, L, ZERO4);
      BAR();
      st_gran(ws, YG_OFF + (u32)((((2 + tb) * 64 + 32 * half + 16 * (tid >> 8) + (tcol >> 3)) * 32 + h) * 8 + (tcol & 7)) * 8, asu(reduce8<1024>(sm, po)), tag);
      if (w < 2) final_reduce(ws, pOut, 1, (xcd * 8 + (jj >> 1)) * 2 + w, lane, tag);
      return;
    }
  if (isB) { return; }
  if (w >= 2) { VMWAIT(0); lds_add(sm, F_FIN, lane); }
  if (w < 4) {
    if (w >= 2) return;
    const int ob = w;
    const u32 ro = OA_OFF + (u32)(h * 32 + 16 * ob + g) * 640;
    v4u x[8]; u64 ls[4];
    {
      const u32 so = OA_OFF + (u32)(h * 32 + 16 * ob + c) * 640 + 512;
      _Pragma("nounroll") for (u32 n = 0;;) {
        const u64 z = ld_gran(ws, so);
        if (__builtin_popcount((u32)__ballot((u32)(z >> 32) == tag) & 0xFFFFu) >= OTHR || ++n > SPIN_CAP) break;
        asm volatile("s_sleep 1" ::: "memory");
      }
    }
    lds_add(sm, F_FIN, lane);
    _Pragma("nounroll") for (u32 n = 0;;) {
      bool ok = true;
#pragma unroll
      for (int k = 0; k < 4; ++k) { x[2 * k] = ld2(rs, ro + k * 2560 + c * 32); x[2 * k + 1] = ld2(rs, ro + k * 2560 + c * 32 + 16); ls[k] = ld_gran(ws, ro + k * 2560 + 512); }
#pragma unroll
      for (int k = 0; k < 4; ++k) ok &= (x[2 * k].y == tag) & (x[2 * k].w == tag) & (x[2 * k + 1].y == tag) & (x[2 * k + 1].w == tag) & ((u32)(ls[k] >> 32) == tag);
      if (__all(ok) || ++n > SPIN_CAP) break;
      asm volatile("s_sleep 1" ::: "memory");
    }
    const float M = x32max(x16max(fmaxf(fmaxf(asf((u32)ls[0]), asf((u32)ls[1])), fmaxf(asf((u32)ls[2]), asf((u32)ls[3])))));
    f32x4 oc = ZERO4; float lw = 0.f;
#pragma unroll
    for (int k = 0; k < 4; ++k) {
      const float e = ex2(asf((u32)ls[k]) - M);
      const u32 pp = cvtpk(e, e);
      lw += asf(pp << 16);
      oc = mfma16(v4u{x[2 * k].x, x[2 * k].z, x[2 * k + 1].x, x[2 * k + 1].z}, v4u{pp & L.m0, pp & L.m1, pp & L.m2, pp & L.m3}, oc);
    }
    const float inv = rcp(x32sum(x16sum(lw)));
    if (c < 8) {
      const u32 xo = L_XBUF + ob * 256 + (16 * ((c >> 1) & 1) + 8 * (c & 1) + 4 * (g >> 1) + (c >> 2)) * 2;
#pragma unroll
      for (int r = 0; r < 4; ++r) *(LAS __bf16*)(sm + xo + (32 * ((2 * (g & 1) + (r >> 1))) + 2 * (r & 1)) * 2) = (__bf16)(oc[r] * inv);
    }
    lds_add(sm, F_FIN + 8 + 4 * ob, lane);
    return;
  }
  {
    const int q = (lane >> 2) & 3, p = lane & 3;
    lds_wait(sm, F_FIN, 8);
    v4u wt[4][2];
#pragma unroll
    for (int k = 0; k < 4; ++k) {
      const u32 ra = (2 * k + (g >> 1)) * 16384 + (8 * (g & 1) + q) * 256 + 8 * (p & 1);
#pragma unroll
      for (int e = 0; e < 2; ++e) {
        const u32 ch = 4 * (w - 4) + 2 * e + (p >> 1);
        const v2u t0 = __builtin_bit_cast(v2u, __builtin_amdgcn_ds_read_tr16_b64_v4i16((LAS s16x4*)(sm + ra + (ch ^ ((q << 2) | (2 * (g & 1)))) * 16)));
        const v2u t1 = __builtin_bit_cast(v2u, __builtin_amdgcn_ds_read_tr16_b64_v4i16((LAS s16x4*)(sm + ra + 1024 + (ch ^ ((q << 2) | (2 * (g & 1) + 1))) * 16)));
        wt[k][e] = v4u{t0.x, t0.y, t1.x, t1.y};
        asm volatile("" : "+v"(wt[k][e]));
      }
    }
    u32 xa = L_XBUF + (c & 1) * 256 + g * 16, yo = YG_OFF + (u32)((((g & 1) * 64 + 16 * qt + 4 * (w - 4) + 2 * (g >> 1) + (c >> 3)) * 32 + h) * 8 + (c & 7)) * 8;
    asm volatile("" : "+v"(xa), "+v"(yo));
    _Pragma("nounroll") for (u32 n = 0; n < (SPIN_CAP << 4); ++n) {
      const u64 f12 = __hip_atomic_load((LAS u64*)(sm + F_FIN + 8), RLX_WG);
      if (__builtin_amdgcn_readfirstlane(((u32)f12 > 0) & ((u32)(f12 >> 32) > 0))) break;
    }
    asm volatile("" ::: "memory");
    v4u xo[4];
#pragma unroll
    for (int k = 0; k < 4; ++k) xo[k] = *(LAS v4u*)(sm + xa + k * 64);
    __builtin_amdgcn_sched_barrier(0);
    f32x4 y0 = ZERO4, y1 = ZERO4;
#pragma unroll
    for (int k = 0; k < 4; ++k) { y0 = mfma16(xo[k], wt[k][0], y0); y1 = mfma16(xo[k], wt[k][1], y1); }
    st_gran(ws, yo, asu(g == 0 ? y0.x : g == 1 ? y0.y : g == 2 ? y1.x : y1.y), tag);
  }
  if (w != 4) return;
  final_reduce(ws, pOut, 0, bid, lane, tag);
  if (bid == 0 && lane == 0) *(GAS u32*)ws = tag;
}

extern "C" void kernel_launch(void* const* d_in, const int* in_sizes, int n_in, void* d_out, int out_size, void* d_ws, size_t ws_size, hipStream_t stream) {
  fused_decode<<<256, 576, 0, stream>>>((const u16*)d_in[7], (const u16*)d_in[6], (char*)d_ws, (const u16*)d_in[5], (const u16*)d_in[0], (const u16*)d_in[3],
                                        (const u16*)d_in[8], (const u16*)d_in[1], (const u16*)d_in[2], (const u16*)d_in[4], (const u16*)d_in[9],
                                        (const u16*)d_in[10], (u16*)d_out);
}
```

```cpp
#include <hip/hip_runtime.h>
#include <stdint.h>

typedef unsigned short u16;
typedef unsigned u32;
typedef unsigned long long u64;
#define GAS __attribute__((address_space(1)))
#define LAS __attribute__((address_space(3)))
typedef u32 v4u __attribute__((ext_vector_type(4)));
typedef u32 v2u __attribute__((ext_vector_type(2)));
typedef float f32x2 __attribute__((ext_vector_type(2)));
typedef float f32x4 __attribute__((ext_vector_type(4)));
typedef float f32x16 __attribute__((ext_vector_type(16)));
typedef __bf16 bf16x8 __attribute__((ext_vector_type(8)));
typedef __bf16 bf16x2 __attribute__((ext_vector_type(2)));
typedef short s16x4 __attribute__((ext_vector_type(4)));
#define RLX_AGENT __ATOMIC_RELAXED, __HIP_MEMORY_SCOPE_AGENT
#define RLX_WG __ATOMIC_RELAXED, __HIP_MEMORY_SCOPE_WORKGROUP

constexpr int SK = 8192;
constexpr int NA = 46, NT = 8, NX = 128 - 2 * NA - NT;
static_assert(8 * NA + 4 * NX + 4 * NT == 512 && NX >= 16 && NA >= 16 && NT == 8, "tile split");
constexpr float C2 = 0.08838834764831845f * 1.4426950408889634f;
constexpr u32 SPIN_CAP = 1u << 17;

constexpr u32 QG_OFF = 4096;
constexpr u32 QA_OFF = QG_OFF + 131072;
constexpr u32 OA_OFF = QA_OFF + 262144;
constexpr u32 LB_OFF = OA_OFF + 32 * 32 * 640;
constexpr u32 CX_OFF = LB_OFF + 32768;
constexpr u32 YG_OFF = CX_OFF + 131072;
constexpr u32 PB_OFF = YG_OFF + 524288;
constexpr u32 WS_USED = PB_OFF + 4194304;
constexpr u32 EXTRA = 131072;
constexpr u32 L_PART = EXTRA, L_XBUF = EXTRA + 8192, L_FLAG = EXTRA + 15040, L_MST = EXTRA + 15104, L_QIMG = EXTRA + 15360, L_MRG = EXTRA + 16384, L_XS = EXTRA + 16384;
constexpr u32 L_QB = EXTRA, L_PF = EXTRA + 2048, L_BM = EXTRA, L_BL = EXTRA + 10240, L_LSE = EXTRA + 11264, L_PARTB = 0;
constexpr u32 F_Q = L_FLAG, F_DRAIN = L_FLAG + 8, F_M = L_FLAG + 12, F_FIN = L_FLAG + 32, L_LBV = EXTRA + 11392;
constexpr u32 QA_T = 500, QS_T = 600, CODE_KB = 21;
constexpr int LDELAY = 25, CDELAY = 12, YDELAY = 15, OTHR = 14;

__device__ __forceinline__ u32 cvtpk(float lo, float hi) { bf16x2 p = {(__bf16)lo, (__bf16)hi}; return __builtin_bit_cast(u32, p); }
__device__ __forceinline__ f32x4 mfma16(v4u a, v4u b, f32x4 c) { return __builtin_amdgcn_mfma_f32_16x16x32_bf16(__builtin_bit_cast(bf16x8, a), __builtin_bit_cast(bf16x8, b), c, 0, 0, 0); }
__device__ __forceinline__ f32x4 mfma16h(v2u a, v2u b, f32x4 c) { return __builtin_amdgcn_mfma_f32_16x16x16bf16_1k(__builtin_bit_cast(s16x4, a), __builtin_bit_cast(s16x4, b), c, 0, 0, 0); }
__device__ __forceinline__ f32x16 mfma32(v4u a, v4u b, f32x16 c) { return __builtin_amdgcn_mfma_f32_32x32x16_bf16(__builtin_bit_cast(bf16x8, a), __builtin_bit_cast(bf16x8, b), c, 0, 0, 0); }
__device__ __forceinline__ float ex2(float x) { return __builtin_amdgcn_exp2f(x); }
__device__ __forceinline__ float rcp(float x) { return __builtin_amdgcn_rcpf(x); }
__device__ __forceinline__ float asf(u32 x) { return __builtin_bit_cast(float, x); }
__device__ __forceinline__ u32 asu(float x) { return __builtin_bit_cast(u32, x); }
template <int CTRL> __device__ __forceinline__ float dpp(float x) { return asf((u32)__builtin_amdgcn_mov_dpp((int)asu(x), CTRL, 0xf, 0xf, true)); }
__device__ __forceinline__ float x16max(float x) { auto s = __builtin_amdgcn_permlane16_swap(asu(x), asu(x), false, false); return fmaxf(asf(s[0]), asf(s[1])); }
__device__ __forceinline__ float x32max(float x) { auto s = __builtin_amdgcn_permlane32_swap(asu(x), asu(x), false, false); return fmaxf(asf(s[0]), asf(s[1])); }
__device__ __forceinline__ float x16sum(float x) { auto s = __builtin_amdgcn_permlane16_swap(asu(x), asu(x), false, false); return asf(s[0]) + asf(s[1]); }
__device__ __forceinline__ float x32sum(float x) { auto s = __builtin_amdgcn_permlane32_swap(asu(x), asu(x), false, false); return asf(s[0]) + asf(s[1]); }
__device__ __forceinline__ float row16max(float x) { x = fmaxf(x, dpp<0x128>(x)); x = fmaxf(x, dpp<0x124>(x)); x = fmaxf(x, dpp<0x122>(x)); return fmaxf(x, dpp<0x121>(x)); }
__device__ __forceinline__ float row16sum(float x) { x += dpp<0x128>(x); x += dpp<0x124>(x); x += dpp<0x122>(x); return x + dpp<0x121>(x); }
__device__ __forceinline__ float max4(f32x4 a) { return fmaxf(fmaxf(a.x, a.y), fmaxf(a.z, a.w)); }
__device__ __forceinline__ float sum4(f32x4 a) { return (a.x + a.y) + (a.z + a.w); }
#define ZERO4 f32x4{0.f, 0.f, 0.f, 0.f}

#define BAR() asm volatile("s_waitcnt lgkmcnt(0)\n\ts_barrier" ::: "memory")
#define VMWAIT(n) asm volatile("s_waitcnt vmcnt(" #n ")" ::: "memory")

template <int PACED, bool NT, bool WAR = true>
__device__ __forceinline__ void dma4(u64 base, u32 o0, u32 o1, u32 o2, u32 o3, u32 lds) {
  if (WAR) asm volatile("s_waitcnt lgkmcnt(0)" ::: "memory");
#define DMA_BODY(W, P) \
  asm volatile("s_mov_b32 m0, %5\n\ts_nop 0\n\t" W "global_load_lds_dwordx4 %0, %4" P "\n\t" W "global_load_lds_dwordx4 %1, %4 offset:1024" P "\n\t" \
               W "global_load_lds_dwordx4 %2, %4 offset:2048" P "\n\t" W "global_load_lds_dwordx4 %3, %4 offset:3072" P \
               :: "v"(o0), "v"(o1), "v"(o2), "v"(o3), "s"(base), "s"(lds) : "memory")
  if (PACED == 8) DMA_BODY("s_waitcnt vmcnt(7)\n\t", " nt");
  else if (PACED && NT) DMA_BODY("s_waitcnt vmcnt(3)\n\t", " nt");
  else if (PACED) DMA_BODY("s_waitcnt vmcnt(3)\n\t", "");
  else if (NT) DMA_BODY("", " nt");
  else DMA_BODY("", "");
#undef DMA_BODY
}

struct Lane { u32 m0, m1, m2, m3; };
__device__ __forceinline__ Lane make_lane(int c) {
  const int j0 = c & 7; const u32 hm = (j0 & 1) ? 0xFFFF0000u : 0x0000FFFFu;
  return Lane{(j0 >> 1) == 0 ? hm : 0u, (j0 >> 1) == 1 ? hm : 0u, (j0 >> 1) == 2 ? hm : 0u, (j0 >> 1) == 3 ? hm : 0u};
}
__device__ __forceinline__ v4u diag(v2u xs, int i, const Lane& L) {
  const u32 src = i < 2 ? xs.x : xs.y;
  const u32 pp = __builtin_amdgcn_perm(src, src, (i & 1) ? 0x03020302u : 0x01000100u);
  return v4u{pp & L.m0, pp & L.m1, pp & L.m2, pp & L.m3};
}
__device__ __forceinline__ f32x4 gemv4(const v4u* W, v2u xs, const Lane& L, f32x4 acc) {
#pragma unroll
  for (int i = 0; i < 4; ++i) acc = mfma16(W[i], diag(xs, i, L), acc);
  return acc;
}
__device__ __forceinline__ void ldsW(v4u (&W)[4], LAS char* sm, u32 unit, int g, int c) {
#pragma unroll
  for (int i = 0; i < 4; ++i) W[i] = *(LAS v4u*)(sm + unit + (4 * g + i) * 256 + c * 16);
}
template <int STRIDE> __device__ __forceinline__ float reduce8(LAS char* sm, u32 off) {
  float v[8];
  asm volatile("" : "+v"(off));
#pragma unroll
  for (int w = 0; w < 8; ++w) v[w] = *(LAS float*)(sm + off + w * STRIDE);
  __builtin_amdgcn_sched_barrier(0);
  return ((v[0] + v[1]) + (v[2] + v[3])) + ((v[4] + v[5]) + (v[6] + v[7]));
}
__device__ __forceinline__ void st_gran(char* ws, u32 off, u32 val, u32 tag) { __hip_atomic_store((GAS u64*)(ws + off), ((u64)tag << 32) | val, RLX_AGENT); }
__device__ __forceinline__ u64 ld_gran(char* ws, u32 off) { return __hip_atomic_load((GAS u64*)(ws + off), RLX_AGENT); }
__device__ __forceinline__ v4u ld2(__amdgpu_buffer_rsrc_t rs, u32 off) { return __builtin_amdgcn_raw_buffer_load_b128(rs, off, 0, 16); }
__device__ __forceinline__ bool spin(u32& n) { asm volatile("s_sleep 1" ::: "memory"); return ++n > SPIN_CAP; }
__device__ __forceinline__ bool spinl(u32& n) { asm volatile("s_sleep 6" ::: "memory"); return ++n > SPIN_CAP; }
__device__ __forceinline__ void sleep_until(u64 t0, u32 ticks) {
  _Pragma("nounroll") for (u32 n = 0; n < 4096 && __builtin_amdgcn_s_memrealtime() - t0 < ticks; ++n) asm volatile("s_sleep 1" ::: "memory");
}
__device__ __forceinline__ void lds_add(LAS char* sm, u32 off, int lane) {
  asm volatile("s_waitcnt lgkmcnt(0)" ::: "memory");
  if (lane == 0) __hip_atomic_fetch_add((LAS u32*)(sm + off), 1u, RLX_WG);
}
__device__ __forceinline__ void lds_wait(LAS char* sm, u32 off, u32 want) {
  for (u32 n = 0; n < (SPIN_CAP << 4); ++n) {
    if ((u32)__builtin_amdgcn_readfirstlane(__hip_atomic_load((LAS u32*)(sm + off), RLX_WG)) >= want) break;
  }
  asm volatile("" ::: "memory");
}
__device__ __forceinline__ void qa_issue(v4u (&q)[16], __amdgpu_buffer_rsrc_t rs, int xcd, int b, int lane) {
#pragma unroll
  for (int k = 0; k < 16; ++k) q[k] = ld2(rs, QA_OFF + (u32)(((16 * xcd + k) * 256 + b * 128 + 2 * lane) * 8));
}
__device__ __forceinline__ v4u qa_finish(v4u (&q)[16], __amdgpu_buffer_rsrc_t rs, char* ws, int xcd, int b, int lane, u32 tag) {
  _Pragma("nounroll") for (u32 n = 0;;) {
    bool ok = true;
#pragma unroll
    for (int k = 0; k < 16; ++k) ok &= (q[k].y == tag) & (q[k].w == tag);
    if (__all(ok) || spinl(n)) break;
    _Pragma("nounroll") while (!__all((u32)(ld_gran(ws, QA_OFF + (u32)((16 * xcd + (lane & 15)) * 256 + b * 128) * 8) >> 32) == tag) && !spinl(n)) {}
    qa_issue(q, rs, xcd, b, lane);
  }
  v4u r;
#pragma unroll
  for (int hh = 0; hh < 4; ++hh)
    r[hh] = cvtpk((asf(q[4 * hh].x) + asf(q[4 * hh + 1].x)) + (asf(q[4 * hh + 2].x) + asf(q[4 * hh + 3].x)),
                  (asf(q[4 * hh].z) + asf(q[4 * hh + 1].z)) + (asf(q[4 * hh + 2].z) + asf(q[4 * hh + 3].z)));
  return r;
}
__device__ __forceinline__ void put_qa(LAS char* sm, v4u r, int lane) {
#pragma unroll
  for (int hh = 0; hh < 4; ++hh) *(LAS u32*)(sm + L_QIMG + hh * 256 + lane * 4) = r[hh];
  lds_add(sm, F_Q, lane);
}
__device__ __forceinline__ void final_reduce(char* ws, u64 pOut, int which, int idx, int lane, u32 tag) {
  const u32 yo = YG_OFF + (u32)((which * 128 + idx) * 256 + lane) * 8;
  const u32 oo = (u32)(((idx >> 6) * 1024 + which * 512 + (idx & 63) * 8 + (lane & 7)) * 2);
  asm volatile("s_sleep %0" :: "i"(YDELAY) : "memory");
  u64 y0, y1, y2, y3;
  _Pragma("nounroll") for (u32 n = 0;;) {
    y0 = ld_gran(ws, yo); y1 = ld_gran(ws, yo + 512); y2 = ld_gran(ws, yo + 1024); y3 = ld_gran(ws, yo + 1536);
    if (__all(((u32)(y0 >> 32) == tag) & ((u32)(y1 >> 32) == tag) & ((u32)(y2 >> 32) == tag) & ((u32)(y3 >> 32) == tag)) || spin(n)) break;
  }
  float x = (asf((u32)y0) + asf((u32)y1)) + (asf((u32)y2) + asf((u32)y3));
  x += dpp<0x128>(x);
  x = x32sum(x16sum(x));
  if (lane < 8) *(GAS __bf16*)(pOut + oo) = (__bf16)x;
}

__global__ void __launch_bounds__(576, 1)
fused_decode(const u16* __restrict__ Bwq, const u16* __restrict__ Bc, char* ws, const u16* __restrict__ Bx, const u16* __restrict__ Ax, const u16* __restrict__ Awq,
             const u16* __restrict__ Bwuk, const u16* __restrict__ Ak, const u16* __restrict__ Av, const u16* __restrict__ Awo, const u16* __restrict__ Bwuv,
             const u16* __restrict__ Bwo, u16* __restrict__ out) {
  __shared__ __attribute__((aligned(16))) char smem[163840];
  u64 pc0; asm volatile("s_getpc_b64 %0" : "=s"(pc0));
  LAS char* sm = (LAS char*)smem;
  const u32 sm0 = (u32)(size_t)sm;
  const int tid = threadIdx.x, lane = tid & 63, w = __builtin_amdgcn_readfirstlane(tid >> 6), g = lane >> 4, c = lane & 15;
  const int bid = blockIdx.x, xcd = bid & 7, isB = bid >> 7, jj = (bid >> 3) & 15, qt = jj & 3, h = 4 * xcd + (jj >> 2);
  u64 pAx = (u64)Ax, pAk = (u64)Ak, pAv = (u64)Av, pAwq = (u64)Awq, pAwo = (u64)Awo, pBx = (u64)Bx, pBc = (u64)Bc, pBwq = (u64)Bwq, pBwuk = (u64)Bwuk, pBwuv = (u64)Bwuv, pBwo = (u64)Bwo, pOut = (u64)out;
  const u32 ring = sm0 + w * 16384, ringo = w * 16384;
  const u32 wrow = (u32)(16 * w * 8192 + h * 256);
  const u32 wo0 = g * 8192 + c * 16, wo1 = wo0 + 31744, wo2 = wo0 + 2 * 31744, wo3 = wo0 + 3 * 31744, lo16 = lane * 16;
  const int bB = xcd >> 2, sB = (xcd & 3) * 16 + jj;
  const u64 cb = pBc + (u32)((bB * SK + sB * 128 + 4 * w) * 1024);
  const u32 co0 = lo16 ^ ((w & 3) * 16), cl = sm0 + w * 4096;
  u64 ht0 = 0;
  if (w == 8) asm volatile("s_memrealtime %0\n\ts_waitcnt lgkmcnt(0)" : "=s"(ht0) :: "memory");
  if (w < 8) {
    asm volatile("s_nop 4" ::: "memory");
    if (isB) dma4<false, true, false>(cb, co0, co0 ^ 64, co0 ^ 128, co0 ^ 192, cl);
    else dma4<false, false, false>(pBwq + (wrow + (u32)qt * (128 * 8192)), wo0, wo1, wo2, wo3, ring + 4096);
  }
  asm volatile("" : "+s"(pAx), "+s"(pAk), "+s"(pAv), "+s"(pAwo), "+s"(pBx), "+s"(pAwq), "+s"(pBwuk), "+s"(pBwuv), "+s"(pBwo), "+s"(pOut));
  u32 tag = *(const __attribute__((address_space(4))) u32*)(u64)ws;
  const __amdgpu_buffer_rsrc_t rs = __builtin_amdgcn_make_buffer_rsrc(ws, 0, (int)WS_USED, 0x00020000);
  const int isT = isB & ~jj & 1;
  const int bA = jj >> 3, sA = isB ? 8 + 4 * isT + ((jj >> 1) & 3) : (jj & 7);

  if (w == 8) {
    asm volatile("" : "+s"(tag)); tag += 1u;
    __builtin_amdgcn_s_setprio(3);
    u32 wa[6];
    {
      const u64 pc = pc0 & ~255ull;
      const u32 co = (u32)((jj & 3) * 6 * 1024 + lane * 16);
#pragma unroll
      for (int k = 0; k < 5; ++k) wa[k] = *(const GAS u32*)(pc + min(co + k * 1024 + (k == 4 ? (lane & 1) * 1024 : 0), (u32)(CODE_KB * 1024)));
      wa[5] = *(const GAS u32*)pOut;
      asm volatile("" ::: "memory");
    }
    if (!isB) {
      if (lane < 16) *(LAS u32*)(sm + L_FLAG + lane * 4) = 0u;
      BAR(); BAR();
      sleep_until(ht0, QA_T);
      v4u q[16];
      qa_issue(q, rs, xcd, bA, lane);
      put_qa(sm, qa_finish(q, rs, ws, xcd, bA, lane, tag), lane);
    } else {
      BAR();
      v4u q[16];
      sleep_until(ht0, QA_T + 45);
      qa_issue(q, rs, xcd, bA, lane);
      sleep_until(ht0, QS_T);
      {
        const u32 so = QG_OFF + (u32)bB * 65536 + (u32)((8 * (lane >> 5) + 7) * 32 + (lane & 31)) * 64;
        _Pragma("nounroll") for (u32 n = 0;;) { const u64 x = ld_gran(ws, so), y = ld_gran(ws, so + 8 * 4096); if (__all(((u32)(x >> 32) == tag) & ((u32)(y >> 32) == tag)) || spin(n)) break; }
      }
      BAR(); BAR(); BAR(); BAR(); BAR();
      if (lane < 16) *(LAS u32*)(sm + L_FLAG + lane * 4) = 0u;
      BAR(); BAR();
      put_qa(sm, qa_finish(q, rs, ws, xcd, bA, lane, tag), lane);
      lds_wait(sm, F_DRAIN, 8);
      if (lane < 32) st_gran(ws, LB_OFF + (u32)((bB * 64 + sB) * 32 + lane) * 8, *(LAS u32*)(sm + L_LSE + lane * 4), tag);
      if (isT) {
        u64 l0, l1;
        asm volatile("s_sleep %0" :: "i"(LDELAY) : "memory");
        _Pragma("nounroll") for (u32 n = 0;;) {
          l0 = ld_gran(ws, LB_OFF + (u32)(lane * 32 + h) * 8); l1 = ld_gran(ws, LB_OFF + (u32)((64 + lane) * 32 + h) * 8);
          if (__all(((u32)(l0 >> 32) == tag) & ((u32)(l1 >> 32) == tag)) || spin(n)) break;
        }
        const float f0 = asf((u32)l0), f1 = asf((u32)l1);
        const float e0 = ex2(f0 - x32max(x16max(row16max(f0)))), e1 = ex2(f1 - x32max(x16max(row16max(f1))));
        *(LAS __bf16*)(sm + L_LBV + lane * 2) = (__bf16)(e0 * rcp(x32sum(x16sum(row16sum(e0)))));
        *(LAS __bf16*)(sm + L_LBV + 128 + lane * 2) = (__bf16)(e1 * rcp(x32sum(x16sum(row16sum(e1)))));
        asm volatile("s_waitcnt lgkmcnt(0)" ::: "memory");
      }
    }
    asm volatile("" :: "v"(wa[0]), "v"(wa[1]), "v"(wa[2]), "v"(wa[3]), "v"(wa[4]), "v"(wa[5]));
    return;
  }

  const u32 sw = (c ^ g) * 16;
  const u32 ko0 = g * 256 + sw, ko1 = g * 256 + (sw ^ 64), ko2 = g * 256 + (sw ^ 128), ko3 = g * 256 + (sw ^ 192);
  const int nt = isT ? NT : isB ? NX : NA, T = (nt >> 3) + (w < (nt & 7));
  const u32 kv_off = (u32)((bA * 8 + xcd) * SK + 16 * ((isT ? 8 * NA + 4 * NX + NT * (sA - 12) : isB ? 8 * NA + NX * (sA - 8) : NA * sA) + w)) * 256;
  u64 kb = pAk + kv_off, vb = pAv + kv_off, fb = pAwo + (u32)((128 * h + 16 * w) * 1024 + qt * 256);
  asm volatile("" : "+s"(kb), "+s"(vb), "+s"(fb));
  const int tb = (tid >> 5) & 1, tcol = 32 * ((tid >> 6) & 3) + (tid & 31), tr = (tid & 0xE0) | ((tid & 7) << 2) | ((tid >> 3) & 3);
  const Lane L = make_lane(c);

  const int half = (jj >> 1) & 1;
  v4u UW[8], X[8];
  if (!isB) {
    {
      const u64 src = pBx + (u32)(((lane >> 1) & 1) * 512 + 128 * ((lane >> 2) & 3) + 16 * w + 8 * (lane & 1)) * 2;
      asm volatile("s_mov_b32 m0, %1\n\ts_nop 4\n\tglobal_load_lds_dwordx4 %0, off" :: "v"(src), "s"(sm0 + L_XS + w * 1024) : "memory");
    }
    const u64 wq_b = pBwq + wrow;
    const u32 xso = L_XS + w * 1024 + (c >> 3) * 32 + g * 8;
    f32x4 acc = ZERO4;
    v4u W[4];
    asm volatile("" : "+s"(tag)); tag += 1u;
    dma4<false, true, false>(pBwuk + (wrow + (u32)qt * (128 * 8192)), g * 8192 + sw, g * 8192 + (sw ^ 64) + 31744, g * 8192 + (sw ^ 128) + 2 * 31744, g * 8192 + (sw ^ 192) + 3 * 31744, ring + 3 * 4096);
    VMWAIT(4);
    dma4<false, false, false>(wq_b + (u32)(((qt + 1) & 3) * (128 * 8192)), wo0, wo1, wo2, wo3, ring + 2 * 4096);
    dma4<false, false, false>(wq_b + (u32)(((qt + 2) & 3) * (128 * 8192)), wo0, wo1, wo2, wo3, ring);
    ldsW(W, sm, ringo + 4096, g, c); acc = gemv4(W, *(LAS v2u*)(sm + xso + qt * 64), L, acc);
    dma4<false, false>(wq_b + (u32)(((qt + 3) & 3) * (128 * 8192)), wo0, wo1, wo2, wo3, ring + 4096);
    VMWAIT(8);
    ldsW(W, sm, ringo + 2 * 4096, g, c); acc = gemv4(W, *(LAS v2u*)(sm + xso + ((qt + 1) & 3) * 64), L, acc);
    VMWAIT(4);
    ldsW(W, sm, ringo, g, c); acc = gemv4(W, *(LAS v2u*)(sm + xso + ((qt + 2) & 3) * 64), L, acc);
    dma4<true, true>(kb, ko0, ko1, ko2, ko3, ring);
    ldsW(W, sm, ringo + 4096, g, c); acc = gemv4(W, *(LAS v2u*)(sm + xso + ((qt + 3) & 3) * 64), L, acc);
    *(LAS f32x4*)(sm + L_PART + w * 1024 + lo16) = acc;
    BAR();
    if (tid < 256) *(LAS __bf16*)(sm + L_XBUF + (tb * 128 + tcol) * 2) = (__bf16)reduce8<1024>(sm, L_PART + tr * 4);
    BAR();
    {
      f32x4 qa = ZERO4;
#pragma unroll
      for (int k = 0; k < 4; ++k)
        qa = mfma16(*(LAS v4u*)(sm + ringo + 3 * 4096 + c * 256 + ((4 * k + g) ^ c) * 16), *(LAS v4u*)(sm + L_XBUF + (c & 1) * 256 + k * 64 + g * 16), qa);
      if (c < 2) {
        __builtin_amdgcn_raw_buffer_store_b128(v4u{cvtpk(qa.x, qa.y), tag, cvtpk(qa.z, qa.w), tag}, rs, QG_OFF + (u32)c * 65536 + (u32)(((8 * qt + w) * 32 + h) * 4 + g) * 16, 0, 16);
      }
    }
  } else {
    const int h2 = lane >> 5, hd = lane & 31;
    v2u xs;
    asm volatile("global_load_dwordx2 %0, %1, %2" : "=v"(xs) : "v"((u32)(((c >> 3) * 512 + 128 * qt + 16 * w + 4 * g) * 2)), "s"(pAx) : "memory");
    dma4<true, true, false>(pAwq + (wrow + (u32)qt * (128 * 8192)), wo0, wo1, wo2, wo3, sm0 + EXTRA + w * 4096);
    asm volatile("" : "+v"(xs));
    asm volatile("" : "+s"(tag)); tag += 1u;
    dma4<true, true, false>(cb + 32768, co0, co0 ^ 64, co0 ^ 128, co0 ^ 192, cl + 32768);
    v4u KV[8];
#pragma unroll
    for (int i = 0; i < 4; ++i) KV[i] = __builtin_nontemporal_load((const GAS v4u*)(kb + (u32)((ko0 ^ (64 * i)) + 1024 * i)));
    const u64 ub = isT ? pBwuv + (u32)((256 * half + 32 * w) * 8192 + h * 256) : kb + 32768;
    const u32 uo = isT ? g * 32768 + c * 16 : ko0, us = isT ? 8192 : 1024, ux = isT ? 0 : 64;
    {
      v4u W[4];
      ldsW(W, sm, EXTRA + w * 4096, g, c);
      const f32x4 acc = gemv4(W, xs, L, ZERO4);
      *(LAS f32x4*)(sm + EXTRA + w * 4096 + lo16) = acc;
    }
    BAR();
    if (tid < 256) st_gran(ws, QA_OFF + (u32)((h * 4 + qt) * 256 + tb * 128 + tcol) * 8, asu(reduce8<4096>(sm, EXTRA + tr * 4)), tag);
    dma4<true, true, false>(cb + 2 * 32768, co0, co0 ^ 64, co0 ^ 128, co0 ^ 192, cl + 2 * 32768);
    dma4<true, true, false>(cb + 3 * 32768, co0, co0 ^ 64, co0 ^ 128, co0 ^ 192, cl + 3 * 32768);
    BAR();
    {
      const u32 qg = QG_OFF + (u32)bB * 65536 + (u32)tid * 16, ql = L_QB + (tid >> 7) * 1024 + ((tid >> 1) & 1) * 512 + ((tid >> 2) & 31) * 16 + (tid & 1) * 8;
      v4u q[8]; bool ok = true;
#pragma unroll
      for (int k = 0; k < 8; ++k) q[k] = ld2(rs, qg + ((k + jj) & 7) * 8192);
      __builtin_amdgcn_sched_barrier(0);
      __builtin_amdgcn_s_barrier();
      __builtin_amdgcn_sched_barrier(0);
#pragma unroll
      for (int i = 0; i < 4; ++i) KV[4 + i] = __builtin_nontemporal_load((const GAS v4u*)(vb + (u32)(lo16 + 1024 * i)));
      __builtin_amdgcn_sched_barrier(0);
#pragma unroll
      for (int k = 0; k < 8; ++k) { ok &= (q[k].y == tag) & (q[k].w == tag); *(LAS v2u*)(sm + ql + ((k + jj) & 7) * 4096) = v2u{q[k].x, q[k].z}; }
      _Pragma("nounroll") for (u32 n = 0; !__all(ok) && !spinl(n);) {
        ok = true;
#pragma unroll 1
        for (int k = 0; k < 8; k += 4) {
          v4u x[4];
#pragma unroll
          for (int j = 0; j < 4; ++j) x[j] = ld2(rs, qg + (k + j) * 8192);
#pragma unroll
          for (int j = 0; j < 4; ++j) { ok &= (x[j].y == tag) & (x[j].w == tag); *(LAS v2u*)(sm + ql + (k + j) * 4096) = v2u{x[j].x, x[j].z}; }
        }
      }
    }
    BAR();
    f32x16 sc;
#pragma unroll
    for (int k = 0; k < 16; ++k) sc[k] = 0.f;
    float mw = 0.f;
    if (w < 4) {
      const u32 ca = (32 * w + hd) * 1024, cx = (h2 ^ (((hd & 3) << 2) | ((hd >> 2) & 3))) * 16;
      u32 qo = L_QB + lo16;
      asm volatile("" : "+v"(qo));
      v4u fa[3][2], fq[3][2];
#define QK_LOAD(B) _Pragma("unroll") for (int k = 0; k < 2; ++k) { fa[(B) % 3][k] = *(LAS v4u*)(sm + ca + ((B) >> 2) * 256 + (cx ^ ((2 * ((B) & 3) + k) * 32))); fq[(B) % 3][k] = *(LAS v4u*)(sm + qo + (2 * (B) + k) * 1024); }
      QK_LOAD(0) QK_LOAD(1)
      BAR();
#pragma unroll
      for (int bb = 0; bb < 16; ++bb) {
        if (bb < 14) { QK_LOAD(bb + 2) }
        __builtin_amdgcn_sched_barrier(0);
#pragma unroll
        for (int k = 0; k < 2; ++k) sc = mfma32(fa[bb % 3][k], fq[bb % 3][k], sc);
        __builtin_amdgcn_sched_barrier(0);
      }
#undef QK_LOAD
      mw = sc[0];
#pragma unroll
      for (int k = 1; k < 16; ++k) mw = fmaxf(mw, sc[k]);
      mw = x32max(mw);
    } else BAR();
#pragma unroll
    for (int i = 0; i < 4; ++i) UW[i] = __builtin_nontemporal_load((const GAS v4u*)(ub + (u32)((uo ^ (ux * i)) + us * i)));
    asm volatile("" ::: "memory");
    if (w < 4 && lane < 32) *(LAS float*)(sm + L_BM + hd * 16 + w * 4) = mw;
    BAR();
    if (w < 4) {
      const float M = C2 * max4(*(LAS f32x4*)(sm + L_BM + hd * 16));
      float ls = 0.f;
#pragma unroll
      for (int k = 0; k < 16; ++k) { sc[k] = ex2(fmaf(sc[k], C2, -M)); ls += sc[k]; }
      *(LAS v4u*)(sm + L_PF + w * 2048 + lo16) = v4u{cvtpk(sc[0], sc[1]), cvtpk(sc[2], sc[3]), cvtpk(sc[4], sc[5]), cvtpk(sc[6], sc[7])};
      *(LAS v4u*)(sm + L_PF + w * 2048 + 1024 + lo16) = v4u{cvtpk(sc[8], sc[9]), cvtpk(sc[10], sc[11]), cvtpk(sc[12], sc[13]), cvtpk(sc[14], sc[15])};
      ls = x32sum(ls);
      if (lane < 32) *(LAS float*)(sm + L_BL + hd * 16 + w * 4) = ls;
    }
    BAR();
    {
      const int q = (lane >> 2) & 3, p = lane & 3, blk = g & 1;
      const u32 base = (4 * h2 + q) * 1024 + (w >> 1) * 256 + 8 * (p & 1), lo4 = 8 * (w & 1) + 2 * blk + (p >> 1);
      u32 po = L_PF + lo16;
      asm volatile("" : "+v"(po));
      const float Ls = sum4(*(LAS f32x4*)(sm + L_BL + hd * 16));
      const float inv = rcp(Ls);
      if (tid < 32) *(LAS float*)(sm + L_LSE + tid * 4) = fmaf(C2, max4(*(LAS f32x4*)(sm + L_BM + hd * 16)), __builtin_amdgcn_logf(Ls));
      const u32 so = hd * 1024 + (w >> 1) * 256 + 8 * h2, sx = (8 * (w & 1)) ^ (((hd & 3) << 2) | ((hd >> 2) & 3));
      const int hr = lane >> 2;
      const u32 ro = hr * 1024 + (w >> 1) * 256, rx = (8 * (w & 1) + p) ^ (((hr & 3) << 2) | (hr >> 2));
      const u32 go = PB_OFF + (u32)(((hr * 2 + (w >> 2)) * 2 + bB) * 64 + sB) * 512 + (w & 3) * 128 + p * 16;
#define TRR(A) __builtin_bit_cast(v2u, __builtin_amdgcn_ds_read_tr16_b64_v4i16((LAS s16x4*)(sm + (A))))
#pragma unroll
      for (int e = 0; e < 2; ++e) {
        const u32 a0 = base + 16 * ((lo4 + 4 * e) ^ ((q << 2) | h2)), a1 = base + 8192 + 16 * ((lo4 + 4 * e) ^ ((q << 2) | (2 + h2)));
        f32x16 o;
#pragma unroll
        for (int k = 0; k < 16; ++k) o[k] = 0.f;
        v4u pb[3]; v2u t0[3], t1[3];
#define PV_LOAD(K) { pb[(K) % 3] = *(LAS v4u*)(sm + po + (K) * 1024); t0[(K) % 3] = TRR(a0 + (K) * 16384); t1[(K) % 3] = TRR(a1 + (K) * 16384); }
        PV_LOAD(0) PV_LOAD(1)
#pragma unroll
        for (int ks = 0; ks < 8; ++ks) {
          if (ks < 6) PV_LOAD(ks + 2)
          __builtin_amdgcn_sched_barrier(0);
          o = mfma32(v4u{t0[ks % 3].x, t0[ks % 3].y, t1[ks % 3].x, t1[ks % 3].y}, pb[ks % 3], o);
          __builtin_amdgcn_sched_barrier(0);
        }
#undef PV_LOAD
#pragma unroll
        for (int k = 0; k < 4; ++k)
          *(LAS v2u*)(sm + so + (sx ^ (4 * e + k)) * 16) = v2u{cvtpk(o[4 * k] * inv, o[4 * k + 1] * inv), cvtpk(o[4 * k + 2] * inv, o[4 * k + 3] * inv)};
        asm volatile("s_waitcnt lgkmcnt(0)" ::: "memory");
        const v4u v0 = *(LAS v4u*)(sm + ro + (rx ^ (4 * e)) * 16), v1 = *(LAS v4u*)(sm + ro + 16384 + (rx ^ (4 * e)) * 16);
        __builtin_amdgcn_raw_buffer_store_b128(v0, rs, go + e * 64, 0, 16);
        __builtin_amdgcn_raw_buffer_store_b128(v1, rs, go + e * 64 + 16 * 2 * 2 * 64 * 512, 0, 16);

      }
#undef TRR
    }
    BAR();
    if (!isT) {
      dma4<false, true, false>(vb + 32768, lo16, lo16, lo16, lo16, ring + 3 * 4096);
#pragma unroll
      for (int i = 0; i < 4; ++i) *(LAS v4u*)(sm + ringo + 8192 + i * 1024 + lo16) = UW[i];
    }
#pragma unroll
    for (int i = 0; i < 8; ++i) *(LAS v4u*)(sm + ringo + i * 1024 + lo16) = KV[i];
    if (isT) VMWAIT(0); else VMWAIT(4);
    lds_add(sm, F_DRAIN, lane);
  }
  if (!isB) {
    dma4<true, true>(vb, lo16, lo16, lo16, lo16, ring + 4096);
    dma4<true, true>(kb + 32768, ko0, ko1, ko2, ko3, ring + 2 * 4096);
    dma4<true, true>(vb + 32768, lo16, lo16, lo16, lo16, ring + 3 * 4096);
  }
  lds_wait(sm, F_Q, 1);
  float Mw;
  {
    v4u qf[4];
#pragma unroll
    for (int k = 0; k < 4; ++k) qf[k] = *(LAS v4u*)(sm + L_QIMG + (c & 3) * 256 + k * 64 + g * 16);
    const u32 pm0 = (c >> 2) == 0 ? 0x0000FFFFu : (c >> 2) == 1 ? 0xFFFF0000u : 0u, pm1 = (c >> 2) == 2 ? 0x0000FFFFu : (c >> 2) == 3 ? 0xFFFF0000u : 0u;
    float m = -INFINITY, l = 0.f;
    f32x4 o0 = ZERO4, o1 = ZERO4;
    const u32 kr = ringo + c * 256, vr = ringo + 4096 + g * 1024 + c * 16;
    v2u y[4];
#define QK_STEP(SO) { \
      const f32x4 sa = mfma16(*(LAS v4u*)(sm + kr + (SO) + ((8 + g) ^ c) * 16), qf[2], mfma16(*(LAS v4u*)(sm + kr + (SO) + ((0 + g) ^ c) * 16), qf[0], ZERO4)) \
                     + mfma16(*(LAS v4u*)(sm + kr + (SO) + ((12 + g) ^ c) * 16), qf[3], mfma16(*(LAS v4u*)(sm + kr + (SO) + ((4 + g) ^ c) * 16), qf[1], ZERO4)); \
      const float mn = fmaxf(m, x32max(x16max(max4(sa)))); \
      const float al = ex2((m - mn) * C2), mc = mn * C2; \
      m = mn; \
      const float p[4] = {ex2(fmaf(sa.x, C2, -mc)), ex2(fmaf(sa.y, C2, -mc)), ex2(fmaf(sa.z, C2, -mc)), ex2(fmaf(sa.w, C2, -mc))}; \
      l = fmaf(l, al, (p[0] + p[1]) + (p[2] + p[3])); \
      o0 *= al; o1 *= al; \
      _Pragma("unroll") for (int i = 0; i < 4; ++i) { const u32 pp = cvtpk(p[i], p[i]); y[i] = v2u{pp & pm0, pp & pm1}; } }
#define PV_STEP(SO) _Pragma("unroll") for (int i = 0; i < 4; ++i) { \
      const v4u V = *(LAS v4u*)(sm + vr + (SO) + i * 256); \
      o0 = mfma16h(v2u{V.x, V.y}, y[i], o0); o1 = mfma16h(v2u{V.z, V.w}, y[i], o1); }
#pragma unroll 1
    for (int t = -1; t < T - 1; ++t) {
      const u32 so = (t & 1) * 8192;
      if (t >= 0) {
        if (t + 2 < T) dma4<8, true>(kb + (u32)((t + 2) * 32768), ko0, ko1, ko2, ko3, ring + so);
        else VMWAIT(4);
        PV_STEP(so)
      }
      QK_STEP(so ^ 8192)
      if (t >= 0 && t + 2 < T) dma4<8, true>(vb + (u32)((t + 2) * 32768), lo16, lo16, lo16, lo16, ring + so + 4096);
    }
    if (lane < 4) *(LAS float*)(sm + L_MST + lane * 32 + w * 4) = m;
    lds_add(sm, F_M, lane);
    l = x32sum(x16sum(l));
    u32 mo = L_MRG + w * 2048 + (c & 3) * 512 + ((4 * g + (c >> 2)) ^ (2 * (c & 3))) * 16, lo = L_MST + 128 + lane * 32 + w * 4, so = ((T - 1) & 1) * 8192;
    asm volatile("" : "+v"(mo), "+v"(lo), "+v"(l));
    lds_wait(sm, F_M, 8);
    Mw = fmaxf(max4(*(LAS f32x4*)(sm + L_MST + (c & 3) * 32)), max4(*(LAS f32x4*)(sm + L_MST + (c & 3) * 32 + 16)));
    float f = ex2((m - Mw) * C2);
    asm volatile("" : "+v"(f), "+v"(Mw));
    if (!isB) {
      const u32 f0 = g * 1024, fx = (c ^ (4 * g)) * 16;
      dma4<false, true, false>(fb, f0 + fx, f0 + 3072 + (fx ^ 16), f0 + 2 * 3072 + (fx ^ 32), f0 + 3 * 3072 + (fx ^ 48), ring);
      VMWAIT(4);
    } else if (!isT) VMWAIT(0);
    {
      v4u V[4];
#pragma unroll
      for (int i = 0; i < 4; ++i) V[i] = *(LAS v4u*)(sm + vr + so + i * 256);
      __builtin_amdgcn_sched_barrier(0);
#pragma unroll
      for (int i = 0; i < 4; ++i) { o0 = mfma16h(v2u{V[i].x, V[i].y}, y[i], o0); o1 = mfma16h(v2u{V[i].z, V[i].w}, y[i], o1); }
    }
    *(LAS f32x4*)(sm + mo) = o0 * f;
    *(LAS f32x4*)(sm + mo + 256) = o1 * f;
    if (lane < 4) *(LAS float*)(sm + lo) = l * f;
#undef QK_STEP
#undef PV_STEP
  }
  {
    u32 ro = L_MRG + w * 512 + ((lane & 31) ^ (2 * w)) * 16, go = OA_OFF + (u32)((4 * xcd + w) * 32 + bA * 16 + sA) * 640 + lane * 16;
    const float Mc = C2 * asf(__builtin_amdgcn_readlane(asu(Mw), w & 3));
    asm volatile("" : "+v"(ro), "+v"(go));
    BAR();
    if (isT) {
#pragma unroll
      for (int i = 0; i < 8; ++i) X[i] = ld2(rs, PB_OFF + (u32)((h * 2 + half) * 128 + 16 * w + 4 * g + (i & 3)) * 512 + (i >> 2) * 256 + c * 16);
      asm volatile("s_sleep 2" ::: "memory");
#pragma unroll
      for (int i = 0; i < 4; ++i) UW[4 + i] = __builtin_nontemporal_load((const GAS v4u*)(pBwuv + (u32)((256 * half + 32 * w + 16 + 4 * g + i) * 8192 + h * 256 + c * 16)));
      asm volatile("" ::: "memory");
    }
    if (w < 4) {
      f32x4 v[8];
#pragma unroll
      for (int ww = 0; ww < 8; ++ww) v[ww] = *(LAS f32x4*)(sm + ro + ww * 2048);
      const f32x4 la = *(LAS f32x4*)(sm + L_MST + 128 + w * 32), lb = *(LAS f32x4*)(sm + L_MST + 128 + w * 32 + 16);
      __builtin_amdgcn_sched_barrier(0);
      const float Ls = sum4(la + lb), inv = rcp(Ls);
      const f32x4 a = (((v[0] + v[1]) + (v[2] + v[3])) + ((v[4] + v[5]) + (v[6] + v[7]))) * inv;
      u32 lse = asu(Mc + __builtin_amdgcn_logf(Ls));
      asm volatile("" : "+v"(lse));
      const u32 p0 = lane < 32 ? cvtpk(a.x, a.y) : lse;
      if (lane < 40) __builtin_amdgcn_raw_buffer_store_b128(v4u{p0, tag, cvtpk(a.z, a.w), tag}, rs, go, 0, 16);
    }
  }
    if (isT) {
      v2u xw = *(LAS v2u*)(sm + L_LBV + w * 32 + g * 8);
      if ((c >> 3) != (w >> 2)) xw = v2u{0u, 0u};
      *(LAS f32x4*)(sm + L_PART + w * 1024 + lo16) = gemv4(X, xw, L, ZERO4);
      *(LAS f32x4*)(sm + L_PARTB + w * 1024 + lo16) = gemv4(X + 4, xw, L, ZERO4);
      asm volatile("" ::: "memory");
      v4u FW[8];
      const u32 po = (tid < 256 ? L_PART : L_PARTB) + tr * 4;
      BAR();
      {
        const u32 pq = po + tb * 4096;
        const float p0 = *(LAS float*)(sm + pq), p1 = *(LAS float*)(sm + pq + 1024), p2 = *(LAS float*)(sm + pq + 2048), p3 = *(LAS float*)(sm + pq + 3072);
        *(LAS __bf16*)(sm + L_XBUF + (tb * 256 + (tid >> 8) * 128 + tcol) * 2) = (__bf16)((p0 + p1) + (p2 + p3));
      }
      BAR();
      const u32 xo = L_XBUF + (c >> 3) * 512 + w * 64 + g * 8;
      *(LAS f32x4*)(sm + L_PART + w * 1024 + lo16) = gemv4(UW + 4, *(LAS v2u*)(sm + xo + 32), L, gemv4(UW, *(LAS v2u*)(sm + xo), L, ZERO4));
      BAR();
      float own = 0.f;
      if (w < 4) {
        own = reduce8<1024>(sm, po);
        st_gran(ws, CX_OFF + (u32)((h * 2 + half) * 256 + tid) * 8, asu(own), tag);
      } else asm volatile("s_sleep 3" ::: "memory");
#pragma unroll
      for (int i = 0; i < 8; ++i) FW[i] = __builtin_nontemporal_load((const GAS v4u*)(pBwo + (u32)((128 * h + 16 * w + 4 * g + (i & 3)) * 1024 + half * 512 + (i >> 2) * 256 + c * 16)));
      asm volatile("" ::: "memory");
      if (w < 4) {
        u64 x;
        asm volatile("s_sleep %0" :: "i"(CDELAY) : "memory");
        _Pragma("nounroll") for (u32 n = 0;;) { x = ld_gran(ws, CX_OFF + (u32)((h * 2 + (half ^ 1)) * 256 + tid) * 8); if (__all((u32)(x >> 32) == tag) || spin(n)) break; }
        *(LAS __bf16*)(sm + L_XBUF + (tb * 128 + tcol) * 2) = (__bf16)(half ? asf((u32)x) + own : own + asf((u32)x));
      }
      BAR();
      const v2u xc = *(LAS v2u*)(sm + L_XBUF + (c >> 3) * 256 + w * 32 + g * 8);
      *(LAS f32x4*)(sm + L_PART + w * 1024 + lo16) = gemv4(FW, xc, L, ZERO4);
      *(LAS f32x4*)(sm + L_PARTB + w * 1024 + lo16) = gemv4(FW + 4, xc, L, ZERO4);
      BAR();
      st_gran(ws, YG_OFF + (u32)((((2 + tb) * 64 + 32 * half + 16 * (tid >> 8) + (tcol >> 3)) * 32 + h) * 8 + (tcol & 7)) * 8, asu(reduce8<1024>(sm, po)), tag);
      if (w < 2) final_reduce(ws, pOut, 1, (xcd * 8 + (jj >> 1)) * 2 + w, lane, tag);
      return;
    }
  if (isB) { return; }
  if (w >= 2) { VMWAIT(0); lds_add(sm, F_FIN, lane); }
  if (w < 4) {
    if (w >= 2) return;
    const int ob = w;
    const u32 ro = OA_OFF + (u32)(h * 32 + 16 * ob + g) * 640;
    v4u x[8]; u64 ls[4];
    {
      const u32 so = OA_OFF + (u32)(h * 32 + 16 * ob + c) * 640 + 512;
      _Pragma("nounroll") for (u32 n = 0;;) {
        const u64 z = ld_gran(ws, so);
        if (__builtin_popcount((u32)__ballot((u32)(z >> 32) == tag) & 0xFFFFu) >= OTHR || ++n > SPIN_CAP) break;
        asm volatile("s_sleep 1" ::: "memory");
      }
    }
    lds_add(sm, F_FIN, lane);
    _Pragma("nounroll") for (u32 n = 0;;) {
      bool ok = true;
#pragma unroll
      for (int k = 0; k < 4; ++k) { x[2 * k] = ld2(rs, ro + k * 2560 + c * 32); x[2 * k + 1] = ld2(rs, ro + k * 2560 + c * 32 + 16); ls[k] = ld_gran(ws, ro + k * 2560 + 512); }
#pragma unroll
      for (int k = 0; k < 4; ++k) ok &= (x[2 * k].y == tag) & (x[2 * k].w == tag) & (x[2 * k + 1].y == tag) & (x[2 * k + 1].w == tag) & ((u32)(ls[k] >> 32) == tag);
      if (__all(ok) || ++n > SPIN_CAP) break;
      asm volatile("s_sleep 1" ::: "memory");
    }
    const float M = x32max(x16max(fmaxf(fmaxf(asf((u32)ls[0]), asf((u32)ls[1])), fmaxf(asf((u32)ls[2]), asf((u32)ls[3])))));
    f32x4 oc = ZERO4; float lw = 0.f;
#pragma unroll
    for (int k = 0; k < 4; ++k) {
      const float e = ex2(asf((u32)ls[k]) - M);
      const u32 pp = cvtpk(e, e);
      lw += asf(pp << 16);
      oc = mfma16(v4u{x[2 * k].x, x[2 * k].z, x[2 * k + 1].x, x[2 * k + 1].z}, v4u{pp & L.m0, pp & L.m1, pp & L.m2, pp & L.m3}, oc);
    }
    const float inv = rcp(x32sum(x16sum(lw)));
    if (c < 8) {
      const u32 xo = L_XBUF + ob * 256 + (16 * ((c >> 1) & 1) + 8 * (c & 1) + 4 * (g >> 1) + (c >> 2)) * 2;
#pragma unroll
      for (int r = 0; r < 4; ++r) *(LAS __bf16*)(sm + xo + (32 * ((2 * (g & 1) + (r >> 1))) + 2 * (r & 1)) * 2) = (__bf16)(oc[r] * inv);
    }
    lds_add(sm, F_FIN + 8 + 4 * ob, lane);
    return;
  }
  {
    const int q = (lane >> 2) & 3, p = lane & 3;
    lds_wait(sm, F_FIN, 8);
    v4u wt[4][2];
#pragma unroll
    for (int k = 0; k < 4; ++k) {
      const u32 ra = (2 * k + (g >> 1)) * 16384 + (8 * (g & 1) + q) * 256 + 8 * (p & 1);
#pragma unroll
      for (int e = 0; e < 2; ++e) {
        const u32 ch = 4 * (w - 4) + 2 * e + (p >> 1);
        const v2u t0 = __builtin_bit_cast(v2u, __builtin_amdgcn_ds_read_tr16_b64_v4i16((LAS s16x4*)(sm + ra + (ch ^ ((q << 2) | (2 * (g & 1)))) * 16)));
        const v2u t1 = __builtin_bit_cast(v2u, __builtin_amdgcn_ds_read_tr16_b64_v4i16((LAS s16x4*)(sm + ra + 1024 + (ch ^ ((q << 2) | (2 * (g & 1) + 1))) * 16)));
        wt[k][e] = v4u{t0.x, t0.y, t1.x, t1.y};
        asm volatile("" : "+v"(wt[k][e]));
      }
    }
    u32 xa = L_XBUF + (c & 1) * 256 + g * 16, yo = YG_OFF + (u32)((((g & 1) * 64 + 16 * qt + 4 * (w - 4) + 2 * (g >> 1) + (c >> 3)) * 32 + h) * 8 + (c & 7)) * 8;
    asm volatile("" : "+v"(xa), "+v"(yo));
    _Pragma("nounroll") for (u32 n = 0; n < (SPIN_CAP << 4); ++n) {
      const u64 f12 = __hip_atomic_load((LAS u64*)(sm + F_FIN + 8), RLX_WG);
      if (__builtin_amdgcn_readfirstlane(((u32)f12 > 0) & ((u32)(f12 >> 32) > 0))) break;
    }
    asm volatile("" ::: "memory");
    v4u xo[4];
#pragma unroll
    for (int k = 0; k < 4; ++k) xo[k] = *(LAS v4u*)(sm + xa + k * 64);
    __builtin_amdgcn_sched_barrier(0);
    f32x4 y0 = ZERO4, y1 = ZERO4;
#pragma unroll
    for (int k = 0; k < 4; ++k) { y0 = mfma16(xo[k], wt[k][0], y0); y1 = mfma16(xo[k], wt[k][1], y1); }
    st_gran(ws, yo, asu(g == 0 ? y0.x : g == 1 ? y0.y : g == 2 ? y1.x : y1.y), tag);
  }
  if (w != 4) return;
  final_reduce(ws, pOut, 0, bid, lane, tag);
  if (bid == 0 && lane == 0) *(GAS u32*)ws = tag;
}

extern "C" void kernel_launch(void* const* d_in, const int* in_sizes, int n_in, void* d_out, int out_size, void* d_ws, size_t ws_size, hipStream_t stream) {
  fused_decode<<<256, 576, 0, stream>>>((const u16*)d_in[7], (const u16*)d_in[6], (char*)d_ws, (const u16*)d_in[5], (const u16*)d_in[0], (const u16*)d_in[3],
                                        (const u16*)d_in[8], (const u16*)d_in[1], (const u16*)d_in[2], (const u16*)d_in[4], (const u16*)d_in[9],
                                        (const u16*)d_in[10], (u16*)d_out);
}
```

```cpp
#include <hip/hip_runtime.h>
#include <stdint.h>

typedef unsigned short u16;
typedef unsigned u32;
typedef unsigned long long u64;
#define GAS __attribute__((address_space(1)))
#define LAS __attribute__((address_space(3)))
typedef u32 v4u __attribute__((ext_vector_type(4)));
typedef u32 v2u __attribute__((ext_vector_type(2)));
typedef float f32x2 __attribute__((ext_vector_type(2)));
typedef float f32x4 __attribute__((ext_vector_type(4)));
typedef float f32x16 __attribute__((ext_vector_type(16)));
typedef __bf16 bf16x8 __attribute__((ext_vector_type(8)));
typedef __bf16 bf16x2 __attribute__((ext_vector_type(2)));
typedef short s16x4 __attribute__((ext_vector_type(4)));
#define RLX_AGENT __ATOMIC_RELAXED, __HIP_MEMORY_SCOPE_AGENT
#define RLX_WG __ATOMIC_RELAXED, __HIP_MEMORY_SCOPE_WORKGROUP

constexpr int SK = 8192;
constexpr int NA = 46, NT = 8, NX = 128 - 2 * NA - NT;
static_assert(8 * NA + 4 * NX + 4 * NT == 512 && NX >= 16 && NA >= 16 && NT == 8, "tile split");
constexpr float C2 = 0.08838834764831845f * 1.4426950408889634f;
constexpr u32 SPIN_CAP = 1u << 17;

constexpr u32 QG_OFF = 4096;
constexpr u32 QA_OFF = QG_OFF + 131072;
constexpr u32 OA_OFF = QA_OFF + 262144;
constexpr u32 LB_OFF = OA_OFF + 32 * 32 * 640;
constexpr u32 CX_OFF = LB_OFF + 32768;
constexpr u32 YG_OFF = CX_OFF + 131072;
constexpr u32 PB_OFF = YG_OFF + 524288;
constexpr u32 WS_USED = PB_OFF + 4194304;
constexpr u32 EXTRA = 131072;
constexpr u32 L_PART = EXTRA, L_XBUF = EXTRA + 8192, L_FLAG = EXTRA + 15040, L_MST = EXTRA + 15104, L_QIMG = EXTRA + 15360, L_MRG = EXTRA + 16384, L_XS = EXTRA + 16384;
constexpr u32 L_QB = EXTRA, L_PF = EXTRA + 2048, L_BM = EXTRA, L_BL = EXTRA + 10240, L_LSE = EXTRA + 11264, L_PARTB = 0;
constexpr u32 F_Q = L_FLAG, F_DRAIN = L_FLAG + 8, F_M = L_FLAG + 12, F_FIN = L_FLAG + 32, L_LBV = EXTRA + 11392;
constexpr u32 QA_T = 530, QS_T = 720;
constexpr u32 CODE_KB = 21;
constexpr int LDELAY = 25, CDELAY = 12, YDELAY = 15, OTHR = 14;

__device__ __forceinline__ u32 cvtpk(float lo, float hi) { bf16x2 p = {(__bf16)lo, (__bf16)hi}; return __builtin_bit_cast(u32, p); }
__device__ __forceinline__ f32x4 mfma16(v4u a, v4u b, f32x4 c) { return __builtin_amdgcn_mfma_f32_16x16x32_bf16(__builtin_bit_cast(bf16x8, a), __builtin_bit_cast(bf16x8, b), c, 0, 0, 0); }
__device__ __forceinline__ f32x4 mfma16h(v2u a, v2u b, f32x4 c) { return __builtin_amdgcn_mfma_f32_16x16x16bf16_1k(__builtin_bit_cast(s16x4, a), __builtin_bit_cast(s16x4, b), c, 0, 0, 0); }
__device__ __forceinline__ f32x16 mfma32(v4u a, v4u b, f32x16 c) { return __builtin_amdgcn_mfma_f32_32x32x16_bf16(__builtin_bit_cast(bf16x8, a), __builtin_bit_cast(bf16x8, b), c, 0, 0, 0); }
__device__ __forceinline__ float ex2(float x) { return __builtin_amdgcn_exp2f(x); }
__device__ __forceinline__ float rcp(float x) { return __builtin_amdgcn_rcpf(x); }
__device__ __forceinline__ float asf(u32 x) { return __builtin_bit_cast(float, x); }
__device__ __forceinline__ u32 asu(float x) { return __builtin_bit_cast(u32, x); }
template <int CTRL> __device__ __forceinline__ float dpp(float x) { return asf((u32)__builtin_amdgcn_mov_dpp((int)asu(x), CTRL, 0xf, 0xf, true)); }
__device__ __forceinline__ float x16max(float x) { auto s = __builtin_amdgcn_permlane16_swap(asu(x), asu(x), false, false); return fmaxf(asf(s[0]), asf(s[1])); }
__device__ __forceinline__ float x32max(float x) { auto s = __builtin_amdgcn_permlane32_swap(asu(x), asu(x), false, false); return fmaxf(asf(s[0]), asf(s[1])); }
__device__ __forceinline__ float x16sum(float x) { auto s = __builtin_amdgcn_permlane16_swap(asu(x), asu(x), false, false); return asf(s[0]) + asf(s[1]); }
__device__ __forceinline__ float x32sum(float x) { auto s = __builtin_amdgcn_permlane32_swap(asu(x), asu(x), false, false); return asf(s[0]) + asf(s[1]); }
__device__ __forceinline__ float row16max(float x) { x = fmaxf(x, dpp<0x128>(x)); x = fmaxf(x, dpp<0x124>(x)); x = fmaxf(x, dpp<0x122>(x)); return fmaxf(x, dpp<0x121>(x)); }
__device__ __forceinline__ float row16sum(float x) { x += dpp<0x128>(x); x += dpp<0x124>(x); x += dpp<0x122>(x); return x + dpp<0x121>(x); }
__device__ __forceinline__ float max4(f32x4 a) { return fmaxf(fmaxf(a.x, a.y), fmaxf(a.z, a.w)); }
__device__ __forceinline__ float sum4(f32x4 a) { return (a.x + a.y) + (a.z + a.w); }
#define ZERO4 f32x4{0.f, 0.f, 0.f, 0.f}

#define BAR() asm volatile("s_waitcnt lgkmcnt(0)\n\ts_barrier" ::: "memory")
#define VMWAIT(n) asm volatile("s_waitcnt vmcnt(" #n ")" ::: "memory")

template <int PACED, bool NT, bool WAR = true>
__device__ __forceinline__ void dma4(u64 base, u32 o0, u32 o1, u32 o2, u32 o3, u32 lds) {
  if (WAR) asm volatile("s_waitcnt lgkmcnt(0)" ::: "memory");
#define DMA_BODY(W, P) \
  asm volatile("s_mov_b32 m0, %5\n\ts_nop 0\n\t" W "global_load_lds_dwordx4 %0, %4" P "\n\t" W "global_load_lds_dwordx4 %1, %4 offset:1024" P "\n\t" \
               W "global_load_lds_dwordx4 %2, %4 offset:2048" P "\n\t" W "global_load_lds_dwordx4 %3, %4 offset:3072" P \
               :: "v"(o0), "v"(o1), "v"(o2), "v"(o3), "s"(base), "s"(lds) : "memory")
  if (PACED == 8) DMA_BODY("s_waitcnt vmcnt(7)\n\t", " nt");
  else if (PACED && NT) DMA_BODY("s_waitcnt vmcnt(3)\n\t", " nt");
  else if (PACED) DMA_BODY("s_waitcnt vmcnt(3)\n\t", "");
  else if (NT) DMA_BODY("", " nt");
  else DMA_BODY("", "");
#undef DMA_BODY
}

struct Lane { u32 m0, m1, m2, m3; };
__device__ __forceinline__ Lane make_lane(int c) {
  const int j0 = c & 7; const u32 hm = (j0 & 1) ? 0xFFFF0000u : 0x0000FFFFu;
  return Lane{(j0 >> 1) == 0 ? hm : 0u, (j0 >> 1) == 1 ? hm : 0u, (j0 >> 1) == 2 ? hm : 0u, (j0 >> 1) == 3 ? hm : 0u};
}
__device__ __forceinline__ v4u diag(v2u xs, int i, const Lane& L) {
  const u32 src = i < 2 ? xs.x : xs.y;
  const u32 pp = __builtin_amdgcn_perm(src, src, (i & 1) ? 0x03020302u : 0x01000100u);
  return v4u{pp & L.m0, pp & L.m1, pp & L.m2, pp & L.m3};
}
__device__ __forceinline__ f32x4 gemv4(const v4u* W, v2u xs, const Lane& L, f32x4 acc) {
#pragma unroll
  for (int i = 0; i < 4; ++i) acc = mfma16(W[i], diag(xs, i, L), acc);
  return acc;
}
__device__ __forceinline__ void ldsW(v4u (&W)[4], LAS char* sm, u32 unit, int g, int c) {
#pragma unroll
  for (int i = 0; i < 4; ++i) W[i] = *(LAS v4u*)(sm + unit + (4 * g + i) * 256 + c * 16);
}
template <int STRIDE> __device__ __forceinline__ float reduce8(LAS char* sm, u32 off) {
  float v[8];
  asm volatile("" : "+v"(off));
#pragma unroll
  for (int w = 0; w < 8; ++w) v[w] = *(LAS float*)(sm + off + w * STRIDE);
  __builtin_amdgcn_sched_barrier(0);
  return ((v[0] + v[1]) + (v[2] + v[3])) + ((v[4] + v[5]) + (v[6] + v[7]));
}
__device__ __forceinline__ void st_gran(char* ws, u32 off, u32 val, u32 tag) { __hip_atomic_store((GAS u64*)(ws + off), ((u64)tag << 32) | val, RLX_AGENT); }
__device__ __forceinline__ u64 ld_gran(char* ws, u32 off) { return __hip_atomic_load((GAS u64*)(ws + off), RLX_AGENT); }
__device__ __forceinline__ v4u ld2(__amdgpu_buffer_rsrc_t rs, u32 off) { return __builtin_amdgcn_raw_buffer_load_b128(rs, off, 0, 16); }
__device__ __forceinline__ bool spin(u32& n) { asm volatile("s_sleep 1" ::: "memory"); return ++n > SPIN_CAP; }
__device__ __forceinline__ bool spinl(u32& n) { asm volatile("s_sleep 6" ::: "memory"); return ++n > SPIN_CAP; }
__device__ __forceinline__ void sleep_until(u64 t0, u32 ticks) {
  _Pragma("nounroll") for (u32 n = 0; n < 4096 && __builtin_amdgcn_s_memrealtime() - t0 < ticks; ++n) asm volatile("s_sleep 1" ::: "memory");
}
__device__ __forceinline__ void lds_add(LAS char* sm, u32 off, int lane) {
  asm volatile("s_waitcnt lgkmcnt(0)" ::: "memory");
  if (lane == 0) __hip_atomic_fetch_add((LAS u32*)(sm + off), 1u, RLX_WG);
}
__device__ __forceinline__ void lds_wait(LAS char* sm, u32 off, u32 want) {
  for (u32 n = 0; n < (SPIN_CAP << 4); ++n) {
    if ((u32)__builtin_amdgcn_readfirstlane(__hip_atomic_load((LAS u32*)(sm + off), RLX_WG)) >= want) break;
  }
  asm volatile("" ::: "memory");
}
__device__ __forceinline__ void qa_issue(v4u (&q)[16], __amdgpu_buffer_rsrc_t rs, int xcd, int b, int lane) {
#pragma unroll
  for (int k = 0; k < 16; ++k) q[k] = ld2(rs, QA_OFF + (u32)(((16 * xcd + k) * 256 + b * 128 + 2 * lane) * 8));
}
__device__ __forceinline__ v4u qa_finish(v4u (&q)[16], __amdgpu_buffer_rsrc_t rs, char* ws, int xcd, int b, int lane, u32 tag) {
  _Pragma("nounroll") for (u32 n = 0;;) {
    bool ok = true;
#pragma unroll
    for (int k = 0; k < 16; ++k) ok &= (q[k].y == tag) & (q[k].w == tag);
    if (__all(ok) || spinl(n)) break;
    _Pragma("nounroll") while (!__all((u32)(ld_gran(ws, QA_OFF + (u32)((16 * xcd + (lane & 15)) * 256 + b * 128) * 8) >> 32) == tag) && !spinl(n)) {}
    qa_issue(q, rs, xcd, b, lane);
  }
  v4u r;
#pragma unroll
  for (int hh = 0; hh < 4; ++hh)
    r[hh] = cvtpk((asf(q[4 * hh].x) + asf(q[4 * hh + 1].x)) + (asf(q[4 * hh + 2].x) + asf(q[4 * hh + 3].x)),
                  (asf(q[4 * hh].z) + asf(q[4 * hh + 1].z)) + (asf(q[4 * hh + 2].z) + asf(q[4 * hh + 3].z)));
  return r;
}
__device__ __forceinline__ void put_qa(LAS char* sm, v4u r, int lane) {
#pragma unroll
  for (int hh = 0; hh < 4; ++hh) *(LAS u32*)(sm + L_QIMG + hh * 256 + lane * 4) = r[hh];
  lds_add(sm, F_Q, lane);
}
__device__ __forceinline__ void final_reduce(char* ws, u64 pOut, int which, int idx, int lane, u32 tag) {
  const u32 yo = YG_OFF + (u32)((which * 128 + idx) * 256 + lane) * 8;
  const u32 oo = (u32)(((idx >> 6) * 1024 + which * 512 + (idx & 63) * 8 + (lane & 7)) * 2);
  asm volatile("s_sleep %0" :: "i"(YDELAY) : "memory");
  u64 y0, y1, y2, y3;
  _Pragma("nounroll") for (u32 n = 0;;) {
    y0 = ld_gran(ws, yo); y1 = ld_gran(ws, yo + 512); y2 = ld_gran(ws, yo + 1024); y3 = ld_gran(ws, yo + 1536);
    if (__all(((u32)(y0 >> 32) == tag) & ((u32)(y1 >> 32) == tag) & ((u32)(y2 >> 32) == tag) & ((u32)(y3 >> 32) == tag)) || spin(n)) break;
  }
  float x = (asf((u32)y0) + asf((u32)y1)) + (asf((u32)y2) + asf((u32)y3));
  x += dpp<0x128>(x);
  x = x32sum(x16sum(x));
  if (lane < 8) *(GAS __bf16*)(pOut + oo) = (__bf16)x;
}

__global__ void __launch_bounds__(576, 1)
fused_decode(const u16* __restrict__ Bwq, const u16* __restrict__ Bc, char* ws, const u16* __restrict__ Bx, const u16* __restrict__ Ax, const u16* __restrict__ Awq,
             const u16* __restrict__ Bwuk, const u16* __restrict__ Ak, const u16* __restrict__ Av, const u16* __restrict__ Awo, const u16* __restrict__ Bwuv,
             const u16* __restrict__ Bwo, u16* __restrict__ out) {
  __shared__ __attribute__((aligned(16))) char smem[163840];
  u64 pc0; asm volatile("s_getpc_b64 %0" : "=s"(pc0));
  LAS char* sm = (LAS char*)smem;
  const u32 sm0 = (u32)(size_t)sm;
  const int tid = threadIdx.x, lane = tid & 63, w = __builtin_amdgcn_readfirstlane(tid >> 6), g = lane >> 4, c = lane & 15;
  const int bid = blockIdx.x, xcd = bid & 7, isB = bid >> 7, jj = (bid >> 3) & 15, qt = jj & 3, h = 4 * xcd + (jj >> 2);
  u64 pAx = (u64)Ax, pAk = (u64)Ak, pAv = (u64)Av, pAwq = (u64)Awq, pAwo = (u64)Awo, pBx = (u64)Bx, pBc = (u64)Bc, pBwq = (u64)Bwq, pBwuk = (u64)Bwuk, pBwuv = (u64)Bwuv, pBwo = (u64)Bwo, pOut = (u64)out;
  const u32 ring = sm0 + w * 16384, ringo = w * 16384;
  const u32 wrow = (u32)(16 * w * 8192 + h * 256);
  const u32 wo0 = g * 8192 + c * 16, wo1 = wo0 + 31744, wo2 = wo0 + 2 * 31744, wo3 = wo0 + 3 * 31744, lo16 = lane * 16;
  const int bB = xcd >> 2, sB = (xcd & 3) * 16 + jj;
  const u64 cb = pBc + (u32)((bB * SK + sB * 128 + 4 * w) * 1024);
  const u32 co0 = lo16 ^ ((w & 3) * 16), cl = sm0 + w * 4096;
  u64 ht0 = 0;
  if (w == 8) asm volatile("s_memrealtime %0\n\ts_waitcnt lgkmcnt(0)" : "=s"(ht0) :: "memory");
  if (w < 8) {
    asm volatile("s_nop 4" ::: "memory");
    if (isB) dma4<false, true, false>(cb, co0, co0 ^ 64, co0 ^ 128, co0 ^ 192, cl);
    else dma4<false, false, false>(pBwq + (wrow + (u32)qt * (128 * 8192)), wo0, wo1, wo2, wo3, ring + 4096);
  }
  asm volatile("" : "+s"(pAx), "+s"(pAk), "+s"(pAv), "+s"(pAwo), "+s"(pBx), "+s"(pAwq), "+s"(pBwuk), "+s"(pBwuv), "+s"(pBwo), "+s"(pOut));
  u32 tag = *(const __attribute__((address_space(4))) u32*)(u64)ws;
  const __amdgpu_buffer_rsrc_t rs = __builtin_amdgcn_make_buffer_rsrc(ws, 0, (int)WS_USED, 0x00020000);
  const int isT = isB & ~jj & 1;
  const int bA = jj >> 3, sA = isB ? 8 + 4 * isT + ((jj >> 1) & 3) : (jj & 7);

  if (w == 8) {
    asm volatile("" : "+s"(tag)); tag += 1u;
    __builtin_amdgcn_s_setprio(3);
    u32 wa[6];
    {
      const u64 pc = pc0 & ~255ull;
      const u32 co = (u32)((jj & 3) * 6 * 1024 + lane * 16);
#pragma unroll
      for (int k = 0; k < 5; ++k) wa[k] = *(const GAS u32*)(pc + min(co + k * 1024 + (k == 4 ? (lane & 1) * 1024 : 0), (u32)(CODE_KB * 1024)));
      wa[5] = *(const GAS u32*)pOut;
      asm volatile("" ::: "memory");
    }
    if (!isB) {
      if (lane < 16) *(LAS u32*)(sm + L_FLAG + lane * 4) = 0u;
      BAR(); BAR();
      sleep_until(ht0, QA_T);
      v4u q[16];
      qa_issue(q, rs, xcd, bA, lane);
      put_qa(sm, qa_finish(q, rs, ws, xcd, bA, lane, tag), lane);
    } else {
      BAR();
      v4u q[16];
      sleep_until(ht0, QA_T + 45);
      qa_issue(q, rs, xcd, bA, lane);
      sleep_until(ht0, QS_T);
      BAR(); BAR(); BAR(); BAR(); BAR();
      if (lane < 16) *(LAS u32*)(sm + L_FLAG + lane * 4) = 0u;
      BAR(); BAR();
      put_qa(sm, qa_finish(q, rs, ws, xcd, bA, lane, tag), lane);
      lds_wait(sm, F_DRAIN, 8);
      if (lane < 32) st_gran(ws, LB_OFF + (u32)((bB * 64 + sB) * 32 + lane) * 8, *(LAS u32*)(sm + L_LSE + lane * 4), tag);
      if (isT) {
        u64 l0, l1;
        asm volatile("s_sleep %0" :: "i"(LDELAY) : "memory");
        _Pragma("nounroll") for (u32 n = 0;;) {
          l0 = ld_gran(ws, LB_OFF + (u32)(lane * 32 + h) * 8); l1 = ld_gran(ws, LB_OFF + (u32)((64 + lane) * 32 + h) * 8);
          if (__all(((u32)(l0 >> 32) == tag) & ((u32)(l1 >> 32) == tag)) || spin(n)) break;
        }
        const float f0 = asf((u32)l0), f1 = asf((u32)l1);
        const float e0 = ex2(f0 - x32max(x16max(row16max(f0)))), e1 = ex2(f1 - x32max(x16max(row16max(f1))));
        *(LAS __bf16*)(sm + L_LBV + lane * 2) = (__bf16)(e0 * rcp(x32sum(x16sum(row16sum(e0)))));
        *(LAS __bf16*)(sm + L_LBV + 128 + lane * 2) = (__bf16)(e1 * rcp(x32sum(x16sum(row16sum(e1)))));
        asm volatile("s_waitcnt lgkmcnt(0)" ::: "memory");
      }
    }
    asm volatile("" :: "v"(wa[0]), "v"(wa[1]), "v"(wa[2]), "v"(wa[3]), "v"(wa[4]), "v"(wa[5]));
    return;
  }

  const u32 sw = (c ^ g) * 16;
  const u32 ko0 = g * 256 + sw, ko1 = g * 256 + (sw ^ 64), ko2 = g * 256 + (sw ^ 128), ko3 = g * 256 + (sw ^ 192);
  const int nt = isT ? NT : isB ? NX : NA, T = (nt >> 3) + (w < (nt & 7));
  const u32 kv_off = (u32)((bA * 8 + xcd) * SK + 16 * ((isT ? 8 * NA + 4 * NX + NT * (sA - 12) : isB ? 8 * NA + NX * (sA - 8) : NA * sA) + w)) * 256;
  u64 kb = pAk + kv_off, vb = pAv + kv_off, fb = pAwo + (u32)((128 * h + 16 * w) * 1024 + qt * 256);
  asm volatile("" : "+s"(kb), "+s"(vb), "+s"(fb));
  const int tb = (tid >> 5) & 1, tcol = 32 * ((tid >> 6) & 3) + (tid & 31), tr = (tid & 0xE0) | ((tid & 7) << 2) | ((tid >> 3) & 3);
  const Lane L = make_lane(c);

  const int half = (jj >> 1) & 1;
  v4u UW[8], X[8];
  if (!isB) {
    {
      const u64 src = pBx + (u32)(((lane >> 1) & 1) * 512 + 128 * ((lane >> 2) & 3) + 16 * w + 8 * (lane & 1)) * 2;
      asm volatile("s_mov_b32 m0, %1\n\ts_nop 4\n\tglobal_load_lds_dwordx4 %0, off" :: "v"(src), "s"(sm0 + L_XS + w * 1024) : "memory");
    }
    const u64 wq_b = pBwq + wrow;
    const u32 xso = L_XS + w * 1024 + (c >> 3) * 32 + g * 8;
    f32x4 acc = ZERO4;
    v4u W[4];
    asm volatile("" : "+s"(tag)); tag += 1u;
    dma4<false, true, false>(pBwuk + (wrow + (u32)qt * (128 * 8192)), g * 8192 + sw, g * 8192 + (sw ^ 64) + 31744, g * 8192 + (sw ^ 128) + 2 * 31744, g * 8192 + (sw ^ 192) + 3 * 31744, ring + 3 * 4096);
    VMWAIT(4);
    dma4<false, false, false>(wq_b + (u32)(((qt + 1) & 3) * (128 * 8192)), wo0, wo1, wo2, wo3, ring + 2 * 4096);
    dma4<false, false, false>(wq_b + (u32)(((qt + 2) & 3) * (128 * 8192)), wo0, wo1, wo2, wo3, ring);
    ldsW(W, sm, ringo + 4096, g, c); acc = gemv4(W, *(LAS v2u*)(sm + xso + qt * 64), L, acc);
    dma4<false, false>(wq_b + (u32)(((qt + 3) & 3) * (128 * 8192)), wo0, wo1, wo2, wo3, ring + 4096);
    VMWAIT(8);
    ldsW(W, sm, ringo + 2 * 4096, g, c); acc = gemv4(W, *(LAS v2u*)(sm + xso + ((qt + 1) & 3) * 64), L, acc);
    VMWAIT(4);
    ldsW(W, sm, ringo, g, c); acc = gemv4(W, *(LAS v2u*)(sm + xso + ((qt + 2) & 3) * 64), L, acc);
    dma4<true, true>(kb, ko0, ko1, ko2, ko3, ring);
    ldsW(W, sm, ringo + 4096, g, c); acc = gemv4(W, *(LAS v2u*)(sm + xso + ((qt + 3) & 3) * 64), L, acc);
    *(LAS f32x4*)(sm + L_PART + w * 1024 + lo16) = acc;
    BAR();
    if (tid < 256) *(LAS __bf16*)(sm + L_XBUF + (tb * 128 + tcol) * 2) = (__bf16)reduce8<1024>(sm, L_PART + tr * 4);
    BAR();
    {
      f32x4 qa = ZERO4;
#pragma unroll
      for (int k = 0; k < 4; ++k)
        qa = mfma16(*(LAS v4u*)(sm + ringo + 3 * 4096 + c * 256 + ((4 * k + g) ^ c) * 16), *(LAS v4u*)(sm + L_XBUF + (c & 1) * 256 + k * 64 + g * 16), qa);
      if (c < 2) {
        __builtin_amdgcn_raw_buffer_store_b128(v4u{cvtpk(qa.x, qa.y), tag, cvtpk(qa.z, qa.w), tag}, rs, QG_OFF + (u32)c * 65536 + (u32)(((8 * qt + w) * 32 + h) * 4 + g) * 16, 0, 16);
      }
    }
  } else {
    const int h2 = lane >> 5, hd = lane & 31;
    v2u xs;
    asm volatile("global_load_dwordx2 %0, %1, %2" : "=v"(xs) : "v"((u32)(((c >> 3) * 512 + 128 * qt + 16 * w + 4 * g) * 2)), "s"(pAx) : "memory");
    dma4<true, true, false>(pAwq + (wrow + (u32)qt * (128 * 8192)), wo0, wo1, wo2, wo3, sm0 + EXTRA + w * 4096);
    asm volatile("" : "+v"(xs));
    asm volatile("" : "+s"(tag)); tag += 1u;
    dma4<true, true, false>(cb + 32768, co0, co0 ^ 64, co0 ^ 128, co0 ^ 192, cl + 32768);
    v4u KV[8];
#pragma unroll
    for (int i = 0; i < 4; ++i) KV[i] = __builtin_nontemporal_load((const GAS v4u*)(kb + (u32)((ko0 ^ (64 * i)) + 1024 * i)));
    const u64 ub = isT ? pBwuv + (u32)((256 * half + 32 * w) * 8192 + h * 256) : kb + 32768;
    const u32 uo = isT ? g * 32768 + c * 16 : ko0, us = isT ? 8192 : 1024, ux = isT ? 0 : 64;
    {
      v4u W[4];
      ldsW(W, sm, EXTRA + w * 4096, g, c);
      const f32x4 acc = gemv4(W, xs, L, ZERO4);
      *(LAS f32x4*)(sm + EXTRA + w * 4096 + lo16) = acc;
    }
    BAR();
    if (tid < 256) st_gran(ws, QA_OFF + (u32)((h * 4 + qt) * 256 + tb * 128 + tcol) * 8, asu(reduce8<4096>(sm, EXTRA + tr * 4)), tag);
    dma4<true, true, false>(cb + 2 * 32768, co0, co0 ^ 64, co0 ^ 128, co0 ^ 192, cl + 2 * 32768);
    dma4<true, true, false>(cb + 3 * 32768, co0, co0 ^ 64, co0 ^ 128, co0 ^ 192, cl + 3 * 32768);
    BAR();
    {
      const u32 qg = QG_OFF + (u32)bB * 65536 + (u32)tid * 16, ql = L_QB + (tid >> 7) * 1024 + ((tid >> 1) & 1) * 512 + ((tid >> 2) & 31) * 16 + (tid & 1) * 8;
      v4u q[8]; bool ok = true;
#pragma unroll
      for (int k = 0; k < 8; ++k) q[k] = ld2(rs, qg + ((k + jj) & 7) * 8192);
      __builtin_amdgcn_sched_barrier(0);
      __builtin_amdgcn_s_barrier();
      __builtin_amdgcn_sched_barrier(0);
#pragma unroll
      for (int i = 0; i < 4; ++i) KV[4 + i] = __builtin_nontemporal_load((const GAS v4u*)(vb + (u32)(lo16 + 1024 * i)));
      __builtin_amdgcn_sched_barrier(0);
#pragma unroll
      for (int k = 0; k < 8; ++k) { ok &= (q[k].y == tag) & (q[k].w == tag); *(LAS v2u*)(sm + ql + ((k + jj) & 7) * 4096) = v2u{q[k].x, q[k].z}; }
      _Pragma("nounroll") for (u32 n = 0; !__all(ok) && !spinl(n);) {
        ok = true;
#pragma unroll 1
        for (int k = 0; k < 8; k += 4) {
          v4u x[4];
#pragma unroll
          for (int j = 0; j < 4; ++j) x[j] = ld2(rs, qg + (k + j) * 8192);
#pragma unroll
          for (int j = 0; j < 4; ++j) { ok &= (x[j].y == tag) & (x[j].w == tag); *(LAS v2u*)(sm + ql + (k + j) * 4096) = v2u{x[j].x, x[j].z}; }
        }
      }
    }
    BAR();
    f32x16 sc;
#pragma unroll
    for (int k = 0; k < 16; ++k) sc[k] = 0.f;
    float mw = 0.f;
    if (w < 4) {
      const u32 ca = (32 * w + hd) * 1024, cx = (h2 ^ (((hd & 3) << 2) | ((hd >> 2) & 3))) * 16;
      u32 qo = L_QB + lo16;
      asm volatile("" : "+v"(qo));
      v4u fa[3][2], fq[3][2];
#define QK_LOAD(B) _Pragma("unroll") for (int k = 0; k < 2; ++k) { fa[(B) % 3][k] = *(LAS v4u*)(sm + ca + ((B) >> 2) * 256 + (cx ^ ((2 * ((B) & 3) + k) * 32))); fq[(B) % 3][k] = *(LAS v4u*)(sm + qo + (2 * (B) + k) * 1024); }
      QK_LOAD(0) QK_LOAD(1)
      BAR();
#pragma unroll
      for (int bb = 0; bb < 16; ++bb) {
        if (bb < 14) { QK_LOAD(bb + 2) }
        __builtin_amdgcn_sched_barrier(0);
#pragma unroll
        for (int k = 0; k < 2; ++k) sc = mfma32(fa[bb % 3][k], fq[bb % 3][k], sc);
        __builtin_amdgcn_sched_barrier(0);
      }
#undef QK_LOAD
      mw = sc[0];
#pragma unroll
      for (int k = 1; k < 16; ++k) mw = fmaxf(mw, sc[k]);
      mw = x32max(mw);
    } else BAR();
#pragma unroll
    for (int i = 0; i < 4; ++i) UW[i] = __builtin_nontemporal_load((const GAS v4u*)(ub + (u32)((uo ^ (ux * i)) + us * i)));
    asm volatile("" ::: "memory");
    if (w < 4 && lane < 32) *(LAS float*)(sm + L_BM + hd * 16 + w * 4) = mw;
    BAR();
    if (w < 4) {
      const float M = C2 * max4(*(LAS f32x4*)(sm + L_BM + hd * 16));
      float ls = 0.f;
#pragma unroll
      for (int k = 0; k < 16; ++k) { sc[k] = ex2(fmaf(sc[k], C2, -M)); ls += sc[k]; }
      *(LAS v4u*)(sm + L_PF + w * 2048 + lo16) = v4u{cvtpk(sc[0], sc[1]), cvtpk(sc[2], sc[3]), cvtpk(sc[4], sc[5]), cvtpk(sc[6], sc[7])};
      *(LAS v4u*)(sm + L_PF + w * 2048 + 1024 + lo16) = v4u{cvtpk(sc[8], sc[9]), cvtpk(sc[10], sc[11]), cvtpk(sc[12], sc[13]), cvtpk(sc[14], sc[15])};
      ls = x32sum(ls);
      if (lane < 32) *(LAS float*)(sm + L_BL + hd * 16 + w * 4) = ls;
    }
    BAR();
    {
      const int q = (lane >> 2) & 3, p = lane & 3, blk = g & 1;
      const u32 base = (4 * h2 + q) * 1024 + (w >> 1) * 256 + 8 * (p & 1), lo4 = 8 * (w & 1) + 2 * blk + (p >> 1);
      u32 po = L_PF + lo16;
      asm volatile("" : "+v"(po));
      const float Ls = sum4(*(LAS f32x4*)(sm + L_BL + hd * 16));
      const float inv = rcp(Ls);
      if (tid < 32) *(LAS float*)(sm + L_LSE + tid * 4) = fmaf(C2, max4(*(LAS f32x4*)(sm + L_BM + hd * 16)), __builtin_amdgcn_logf(Ls));
      const u32 so = hd * 1024 + (w >> 1) * 256 + 8 * h2, sx = (8 * (w & 1)) ^ (((hd & 3) << 2) | ((hd >> 2) & 3));
      const int hr = lane >> 2;
      const u32 ro = hr * 1024 + (w >> 1) * 256, rx = (8 * (w & 1) + p) ^ (((hr & 3) << 2) | (hr >> 2));
      const u32 go = PB_OFF + (u32)(((hr * 2 + (w >> 2)) * 2 + bB) * 64 + sB) * 512 + (w & 3) * 128 + p * 16;
#define TRR(A) __builtin_bit_cast(v2u, __builtin_amdgcn_ds_read_tr16_b64_v4i16((LAS s16x4*)(sm + (A))))
#pragma unroll
      for (int e = 0; e < 2; ++e) {
        const u32 a0 = base + 16 * ((lo4 + 4 * e) ^ ((q << 2) | h2)), a1 = base + 8192 + 16 * ((lo4 + 4 * e) ^ ((q << 2) | (2 + h2)));
        f32x16 o;
#pragma unroll
        for (int k = 0; k < 16; ++k) o[k] = 0.f;
        v4u pb[3]; v2u t0[3], t1[3];
#define PV_LOAD(K) { pb[(K) % 3] = *(LAS v4u*)(sm + po + (K) * 1024); t0[(K) % 3] = TRR(a0 + (K) * 16384); t1[(K) % 3] = TRR(a1 + (K) * 16384); }
        PV_LOAD(0) PV_LOAD(1)
#pragma unroll
        for (int ks = 0; ks < 8; ++ks) {
          if (ks < 6) PV_LOAD(ks + 2)
          __builtin_amdgcn_sched_barrier(0);
          o = mfma32(v4u{t0[ks % 3].x, t0[ks % 3].y, t1[ks % 3].x, t1[ks % 3].y}, pb[ks % 3], o);
          __builtin_amdgcn_sched_barrier(0);
        }
#undef PV_LOAD
#pragma unroll
        for (int k = 0; k < 4; ++k)
          *(LAS v2u*)(sm + so + (sx ^ (4 * e + k)) * 16) = v2u{cvtpk(o[4 * k] * inv, o[4 * k + 1] * inv), cvtpk(o[4 * k + 2] * inv, o[4 * k + 3] * inv)};
        asm volatile("s_waitcnt lgkmcnt(0)" ::: "memory");
        const v4u v0 = *(LAS v4u*)(sm + ro + (rx ^ (4 * e)) * 16), v1 = *(LAS v4u*)(sm + ro + 16384 + (rx ^ (4 * e)) * 16);
        __builtin_amdgcn_raw_buffer_store_b128(v0, rs, go + e * 64, 0, 16);
        __builtin_amdgcn_raw_buffer_store_b128(v1, rs, go + e * 64 + 16 * 2 * 2 * 64 * 512, 0, 16);

      }
#undef TRR
    }
    BAR();
    if (!isT) {
      dma4<false, true, false>(vb + 32768, lo16, lo16, lo16, lo16, ring + 3 * 4096);
#pragma unroll
      for (int i = 0; i < 4; ++i) *(LAS v4u*)(sm + ringo + 8192 + i * 1024 + lo16) = UW[i];
    }
#pragma unroll
    for (int i = 0; i < 8; ++i) *(LAS v4u*)(sm + ringo + i * 1024 + lo16) = KV[i];
    if (isT) VMWAIT(0); else VMWAIT(4);
    lds_add(sm, F_DRAIN, lane);
  }
  if (!isB) {
    dma4<true, true>(vb, lo16, lo16, lo16, lo16, ring + 4096);
    dma4<true, true>(kb + 32768, ko0, ko1, ko2, ko3, ring + 2 * 4096);
    dma4<true, true>(vb + 32768, lo16, lo16, lo16, lo16, ring + 3 * 4096);
  }
  lds_wait(sm, F_Q, 1);
  float Mw;
  {
    v4u qf[4];
#pragma unroll
    for (int k = 0; k < 4; ++k) qf[k] = *(LAS v4u*)(sm + L_QIMG + (c & 3) * 256 + k * 64 + g * 16);
    const u32 pm0 = (c >> 2) == 0 ? 0x0000FFFFu : (c >> 2) == 1 ? 0xFFFF0000u : 0u, pm1 = (c >> 2) == 2 ? 0x0000FFFFu : (c >> 2) == 3 ? 0xFFFF0000u : 0u;
    float m = -INFINITY, l = 0.f;
    f32x4 o0 = ZERO4, o1 = ZERO4;
    const u32 kr = ringo + c * 256, vr = ringo + 4096 + g * 1024 + c * 16;
    v2u y[4];
#define QK_STEP(SO) { \
      const f32x4 sa = mfma16(*(LAS v4u*)(sm + kr + (SO) + ((8 + g) ^ c) * 16), qf[2], mfma16(*(LAS v4u*)(sm + kr + (SO) + ((0 + g) ^ c) * 16), qf[0], ZERO4)) \
                     + mfma16(*(LAS v4u*)(sm + kr + (SO) + ((12 + g) ^ c) * 16), qf[3], mfma16(*(LAS v4u*)(sm + kr + (SO) + ((4 + g) ^ c) * 16), qf[1], ZERO4)); \
      const float mn = fmaxf(m, x32max(x16max(max4(sa)))); \
      const float al = ex2((m - mn) * C2), mc = mn * C2; \
      m = mn; \
      const float p[4] = {ex2(fmaf(sa.x, C2, -mc)), ex2(fmaf(sa.y, C2, -mc)), ex2(fmaf(sa.z, C2, -mc)), ex2(fmaf(sa.w, C2, -mc))}; \
      l = fmaf(l, al, (p[0] + p[1]) + (p[2] + p[3])); \
      o0 *= al; o1 *= al; \
      _Pragma("unroll") for (int i = 0; i < 4; ++i) { const u32 pp = cvtpk(p[i], p[i]); y[i] = v2u{pp & pm0, pp & pm1}; } }
#define PV_STEP(SO) _Pragma("unroll") for (int i = 0; i < 4; ++i) { \
      const v4u V = *(LAS v4u*)(sm + vr + (SO) + i * 256); \
      o0 = mfma16h(v2u{V.x, V.y}, y[i], o0); o1 = mfma16h(v2u{V.z, V.w}, y[i], o1); }
#pragma unroll 1
    for (int t = -1; t < T - 1; ++t) {
      const u32 so = (t & 1) * 8192;
      if (t >= 0) {
        if (t + 2 < T) dma4<8, true>(kb + (u32)((t + 2) * 32768), ko0, ko1, ko2, ko3, ring + so);
        else VMWAIT(4);
        PV_STEP(so)
      }
      QK_STEP(so ^ 8192)
      if (t >= 0 && t + 2 < T) dma4<8, true>(vb + (u32)((t + 2) * 32768), lo16, lo16, lo16, lo16, ring + so + 4096);
    }
    if (lane < 4) *(LAS float*)(sm + L_MST + lane * 32 + w * 4) = m;
    lds_add(sm, F_M, lane);
    l = x32sum(x16sum(l));
    u32 mo = L_MRG + w * 2048 + (c & 3) * 512 + ((4 * g + (c >> 2)) ^ (2 * (c & 3))) * 16, lo = L_MST + 128 + lane * 32 + w * 4, so = ((T - 1) & 1) * 8192;
    asm volatile("" : "+v"(mo), "+v"(lo), "+v"(l));
    lds_wait(sm, F_M, 8);
    Mw = fmaxf(max4(*(LAS f32x4*)(sm + L_MST + (c & 3) * 32)), max4(*(LAS f32x4*)(sm + L_MST + (c & 3) * 32 + 16)));
    float f = ex2((m - Mw) * C2);
    asm volatile("" : "+v"(f), "+v"(Mw));
    if (!isB) {
      const u32 f0 = g * 1024, fx = (c ^ (4 * g)) * 16;
      dma4<false, true, false>(fb, f0 + fx, f0 + 3072 + (fx ^ 16), f0 + 2 * 3072 + (fx ^ 32), f0 + 3 * 3072 + (fx ^ 48), ring);
      VMWAIT(4);
    } else if (!isT) VMWAIT(0);
    {
      v4u V[4];
#pragma unroll
      for (int i = 0; i < 4; ++i) V[i] = *(LAS v4u*)(sm + vr + so + i * 256);
      __builtin_amdgcn_sched_barrier(0);
#pragma unroll
      for (int i = 0; i < 4; ++i) { o0 = mfma16h(v2u{V[i].x, V[i].y}, y[i], o0); o1 = mfma16h(v2u{V[i].z, V[i].w}, y[i], o1); }
    }
    *(LAS f32x4*)(sm + mo) = o0 * f;
    *(LAS f32x4*)(sm + mo + 256) = o1 * f;
    if (lane < 4) *(LAS float*)(sm + lo) = l * f;
#undef QK_STEP
#undef PV_STEP
  }
  {
    u32 ro = L_MRG + w * 512 + ((lane & 31) ^ (2 * w)) * 16, go = OA_OFF + (u32)((4 * xcd + w) * 32 + bA * 16 + sA) * 640 + lane * 16;
    const float Mc = C2 * asf(__builtin_amdgcn_readlane(asu(Mw), w & 3));
    asm volatile("" : "+v"(ro), "+v"(go));
    BAR();
    if (isT) {
#pragma unroll
      for (int i = 0; i < 8; ++i) X[i] = ld2(rs, PB_OFF + (u32)((h * 2 + half) * 128 + 16 * w + 4 * g + (i & 3)) * 512 + (i >> 2) * 256 + c * 16);
      asm volatile("s_sleep 2" ::: "memory");
#pragma unroll
      for (int i = 0; i < 4; ++i) UW[4 + i] = __builtin_nontemporal_load((const GAS v4u*)(pBwuv + (u32)((256 * half + 32 * w + 16 + 4 * g + i) * 8192 + h * 256 + c * 16)));
      asm volatile("" ::: "memory");
    }
    if (w < 4) {
      f32x4 v[8];
#pragma unroll
      for (int ww = 0; ww < 8; ++ww) v[ww] = *(LAS f32x4*)(sm + ro + ww * 2048);
      const f32x4 la = *(LAS f32x4*)(sm + L_MST + 128 + w * 32), lb = *(LAS f32x4*)(sm + L_MST + 128 + w * 32 + 16);
      __builtin_amdgcn_sched_barrier(0);
      const float Ls = sum4(la + lb), inv = rcp(Ls);
      const f32x4 a = (((v[0] + v[1]) + (v[2] + v[3])) + ((v[4] + v[5]) + (v[6] + v[7]))) * inv;
      u32 lse = asu(Mc + __builtin_amdgcn_logf(Ls));
      asm volatile("" : "+v"(lse));
      const u32 p0 = lane < 32 ? cvtpk(a.x, a.y) : lse;
      if (lane < 40) __builtin_amdgcn_raw_buffer_store_b128(v4u{p0, tag, cvtpk(a.z, a.w), tag}, rs, go, 0, 16);
    }
  }
  if (isT) {
    v2u xw = *(LAS v2u*)(sm + L_LBV + w * 32 + g * 8);
    if ((c >> 3) != (w >> 2)) xw = v2u{0u, 0u};
    *(LAS f32x4*)(sm + L_PART + w * 1024 + lo16) = gemv4(X, xw, L, ZERO4);
    *(LAS f32x4*)(sm + L_PARTB + w * 1024 + lo16) = gemv4(X + 4, xw, L, ZERO4);
    asm volatile("" ::: "memory");
    v4u FW[8];
    const u32 po = (tid < 256 ? L_PART : L_PARTB) + tr * 4;
    BAR();
    {
      const u32 pq = po + tb * 4096;
      const float p0 = *(LAS float*)(sm + pq), p1 = *(LAS float*)(sm + pq + 1024), p2 = *(LAS float*)(sm + pq + 2048), p3 = *(LAS float*)(sm + pq + 3072);
      *(LAS __bf16*)(sm + L_XBUF + (tb * 256 + (tid >> 8) * 128 + tcol) * 2) = (__bf16)((p0 + p1) + (p2 + p3));
    }
    BAR();
    const u32 xo = L_XBUF + (c >> 3) * 512 + w * 64 + g * 8;
    *(LAS f32x4*)(sm + L_PART + w * 1024 + lo16) = gemv4(UW + 4, *(LAS v2u*)(sm + xo + 32), L, gemv4(UW, *(LAS v2u*)(sm + xo), L, ZERO4));
    BAR();
    float own = 0.f;
    if (w < 4) {
      own = reduce8<1024>(sm, po);
      st_gran(ws, CX_OFF + (u32)((h * 2 + half) * 256 + tid) * 8, asu(own), tag);
    } else asm volatile("s_sleep 3" ::: "memory");
#pragma unroll
    for (int i = 0; i < 8; ++i) FW[i] = __builtin_nontemporal_load((const GAS v4u*)(pBwo + (u32)((128 * h + 16 * w + 4 * g + (i & 3)) * 1024 + half * 512 + (i >> 2) * 256 + c * 16)));
    asm volatile("" ::: "memory");
    if (w < 4) {
      u64 x;
      asm volatile("s_sleep %0" :: "i"(CDELAY) : "memory");
      _Pragma("nounroll") for (u32 n = 0;;) { x = ld_gran(ws, CX_OFF + (u32)((h * 2 + (half ^ 1)) * 256 + tid) * 8); if (__all((u32)(x >> 32) == tag) || spin(n)) break; }
      *(LAS __bf16*)(sm + L_XBUF + (tb * 128 + tcol) * 2) = (__bf16)(half ? asf((u32)x) + own : own + asf((u32)x));
    }
    BAR();
    const v2u xc = *(LAS v2u*)(sm + L_XBUF + (c >> 3) * 256 + w * 32 + g * 8);
    *(LAS f32x4*)(sm + L_PART + w * 1024 + lo16) = gemv4(FW, xc, L, ZERO4);
    *(LAS f32x4*)(sm + L_PARTB + w * 1024 + lo16) = gemv4(FW + 4, xc, L, ZERO4);
    BAR();
    st_gran(ws, YG_OFF + (u32)((((2 + tb) * 64 + 32 * half + 16 * (tid >> 8) + (tcol >> 3)) * 32 + h) * 8 + (tcol & 7)) * 8, asu(reduce8<1024>(sm, po)), tag);
    if (w < 2) final_reduce(ws, pOut, 1, (xcd * 8 + (jj >> 1)) * 2 + w, lane, tag);
    return;
  }
  if (isB) return;
  if (w >= 2) { VMWAIT(0); lds_add(sm, F_FIN, lane); }
  if (w < 4) {
    if (w >= 2) return;
    const int ob = w;
    const u32 ro = OA_OFF + (u32)(h * 32 + 16 * ob + g) * 640;
    v4u x[8]; u64 ls[4];
    {
      const u32 so = OA_OFF + (u32)(h * 32 + 16 * ob + c) * 640 + 512;
      _Pragma("nounroll") for (u32 n = 0;;) {
        const u64 z = ld_gran(ws, so);
        if (__builtin_popcount((u32)__ballot((u32)(z >> 32) == tag) & 0xFFFFu) >= OTHR || ++n > SPIN_CAP) break;
        asm volatile("s_sleep 1" ::: "memory");
      }
    }
    lds_add(sm, F_FIN, lane);
    _Pragma("nounroll") for (u32 n = 0;;) {
      bool ok = true;
#pragma unroll
      for (int k = 0; k < 4; ++k) { x[2 * k] = ld2(rs, ro + k * 2560 + c * 32); x[2 * k + 1] = ld2(rs, ro + k * 2560 + c * 32 + 16); ls[k] = ld_gran(ws, ro + k * 2560 + 512); }
#pragma unroll
      for (int k = 0; k < 4; ++k) ok &= (x[2 * k].y == tag) & (x[2 * k].w == tag) & (x[2 * k + 1].y == tag) & (x[2 * k + 1].w == tag) & ((u32)(ls[k] >> 32) == tag);
      if (__all(ok) || ++n > SPIN_CAP) break;
      asm volatile("s_sleep 1" ::: "memory");
    }
    const float M = x32max(x16max(fmaxf(fmaxf(asf((u32)ls[0]), asf((u32)ls[1])), fmaxf(asf((u32)ls[2]), asf((u32)ls[3])))));
    f32x4 oc = ZERO4; float lw = 0.f;
#pragma unroll
    for (int k = 0; k < 4; ++k) {
      const float e = ex2(asf((u32)ls[k]) - M);
      const u32 pp = cvtpk(e, e);
      lw += asf(pp << 16);
      oc = mfma16(v4u{x[2 * k].x, x[2 * k].z, x[2 * k + 1].x, x[2 * k + 1].z}, v4u{pp & L.m0, pp & L.m1, pp & L.m2, pp & L.m3}, oc);
    }
    const float inv = rcp(x32sum(x16sum(lw)));
    if (c < 8) {
      const u32 xo = L_XBUF + ob * 256 + (16 * ((c >> 1) & 1) + 8 * (c & 1) + 4 * (g >> 1) + (c >> 2)) * 2;
#pragma unroll
      for (int r = 0; r < 4; ++r) *(LAS __bf16*)(sm + xo + (32 * ((2 * (g & 1) + (r >> 1))) + 2 * (r & 1)) * 2) = (__bf16)(oc[r] * inv);
    }
    lds_add(sm, F_FIN + 8 + 4 * ob, lane);
    return;
  }
  {
    const int q = (lane >> 2) & 3, p = lane & 3;
    lds_wait(sm, F_FIN, 8);
    v4u wt[4][2];
#pragma unroll
    for (int k = 0; k < 4; ++k) {
      const u32 ra = (2 * k + (g >> 1)) * 16384 + (8 * (g & 1) + q) * 256 + 8 * (p & 1);
#pragma unroll
      for (int e = 0; e < 2; ++e) {
        const u32 ch = 4 * (w - 4) + 2 * e + (p >> 1);
        const v2u t0 = __builtin_bit_cast(v2u, __builtin_amdgcn_ds_read_tr16_b64_v4i16((LAS s16x4*)(sm + ra + (ch ^ ((q << 2) | (2 * (g & 1)))) * 16)));
        const v2u t1 = __builtin_bit_cast(v2u, __builtin_amdgcn_ds_read_tr16_b64_v4i16((LAS s16x4*)(sm + ra + 1024 + (ch ^ ((q << 2) | (2 * (g & 1) + 1))) * 16)));
        wt[k][e] = v4u{t0.x, t0.y, t1.x, t1.y};
        asm volatile("" : "+v"(wt[k][e]));
      }
    }
    u32 xa = L_XBUF + (c & 1) * 256 + g * 16, yo = YG_OFF + (u32)((((g & 1) * 64 + 16 * qt + 4 * (w - 4) + 2 * (g >> 1) + (c >> 3)) * 32 + h) * 8 + (c & 7)) * 8;
    asm volatile("" : "+v"(xa), "+v"(yo));
    _Pragma("nounroll") for (u32 n = 0; n < (SPIN_CAP << 4); ++n) {
      const u64 f12 = __hip_atomic_load((LAS u64*)(sm + F_FIN + 8), RLX_WG);
      if (__builtin_amdgcn_readfirstlane(((u32)f12 > 0) & ((u32)(f12 >> 32) > 0))) break;
    }
    asm volatile("" ::: "memory");
    v4u xo[4];
#pragma unroll
    for (int k = 0; k < 4; ++k) xo[k] = *(LAS v4u*)(sm + xa + k * 64);
    __builtin_amdgcn_sched_barrier(0);
    f32x4 y0 = ZERO4, y1 = ZERO4;
#pragma unroll
    for (int k = 0; k < 4; ++k) { y0 = mfma16(xo[k], wt[k][0], y0); y1 = mfma16(xo[k], wt[k][1], y1); }
    st_gran(ws, yo, asu(g == 0 ? y0.x : g == 1 ? y0.y : g == 2 ? y1.x : y1.y), tag);
  }
  if (w != 4) return;
  final_reduce(ws, pOut, 0, bid, lane, tag);
  if (bid == 0 && lane == 0) *(GAS u32*)ws = tag;
}

extern "C" void kernel_launch(void* const* d_in, const int* in_sizes, int n_in, void* d_out, int out_size, void* d_ws, size_t ws_size, hipStream_t stream) {
  fused_decode<<<256, 576, 0, stream>>>((const u16*)d_in[7], (const u16*)d_in[6], (char*)d_ws, (const u16*)d_in[5], (const u16*)d_in[0], (const u16*)d_in[3],
                                        (const u16*)d_in[8], (const u16*)d_in[1], (const u16*)d_in[2], (const u16*)d_in[4], (const u16*)d_in[9],
                                        (const u16*)d_in[10], (u16*)d_out);
}
```

```cpp
#include <hip/hip_runtime.h>
#include <stdint.h>

typedef unsigned short u16;
typedef unsigned u32;
typedef unsigned long long u64;
#define GAS __attribute__((address_space(1)))
#define LAS __attribute__((address_space(3)))
typedef u32 v4u __attribute__((ext_vector_type(4)));
typedef u32 v2u __attribute__((ext_vector_type(2)));
typedef float f32x2 __attribute__((ext_vector_type(2)));
typedef float f32x4 __attribute__((ext_vector_type(4)));
typedef float f32x16 __attribute__((ext_vector_type(16)));
typedef __bf16 bf16x8 __attribute__((ext_vector_type(8)));
typedef __bf16 bf16x2 __attribute__((ext_vector_type(2)));
typedef short s16x4 __attribute__((ext_vector_type(4)));
#define RLX_AGENT __ATOMIC_RELAXED, __HIP_MEMORY_SCOPE_AGENT
#define RLX_WG __ATOMIC_RELAXED, __HIP_MEMORY_SCOPE_WORKGROUP

constexpr int SK = 8192;
constexpr int NA = 46, NT = 8, NX = 128 - 2 * NA - NT;
static_assert(8 * NA + 4 * NX + 4 * NT == 512 && NX >= 16 && NA >= 16 && NT == 8, "tile split");
constexpr float C2 = 0.08838834764831845f * 1.4426950408889634f;
constexpr u32 SPIN_CAP = 1u << 17;

constexpr u32 QG_OFF = 4096;
constexpr u32 QA_OFF = QG_OFF + 131072;
constexpr u32 OA_OFF = QA_OFF + 262144;
constexpr u32 LB_OFF = OA_OFF + 32 * 32 * 640;
constexpr u32 CX_OFF = LB_OFF + 32768;
constexpr u32 YG_OFF = CX_OFF + 131072;
constexpr u32 PB_OFF = YG_OFF + 524288;
constexpr u32 WS_USED = PB_OFF + 4194304;
constexpr u32 EXTRA = 131072;
constexpr u32 L_PART = EXTRA, L_XBUF = EXTRA + 8192, L_FLAG = EXTRA + 15040, L_MST = EXTRA + 15104, L_QIMG = EXTRA + 15360, L_MRG = EXTRA + 16384, L_XS = EXTRA + 16384;
constexpr u32 L_QB = EXTRA, L_PF = EXTRA + 2048, L_BM = EXTRA, L_BL = EXTRA + 10240, L_LSE = EXTRA + 11264, L_PARTB = 0;
constexpr u32 F_Q = L_FLAG, F_DRAIN = L_FLAG + 8, F_M = L_FLAG + 12, F_FIN = L_FLAG + 32, L_LBV = EXTRA + 11392;
constexpr u32 QA_T = 520, QS_T = 650;
constexpr u32 CODE_KB = 20;
constexpr int LDELAY = 25, CDELAY = 12, YDELAY = 15, OTHR = 14;

__device__ __forceinline__ u32 cvtpk(float lo, float hi) { bf16x2 p = {(__bf16)lo, (__bf16)hi}; return __builtin_bit_cast(u32, p); }
__device__ __forceinline__ f32x4 mfma16(v4u a, v4u b, f32x4 c) { return __builtin_amdgcn_mfma_f32_16x16x32_bf16(__builtin_bit_cast(bf16x8, a), __builtin_bit_cast(bf16x8, b), c, 0, 0, 0); }
__device__ __forceinline__ f32x4 mfma16h(v2u a, v2u b, f32x4 c) { return __builtin_amdgcn_mfma_f32_16x16x16bf16_1k(__builtin_bit_cast(s16x4, a), __builtin_bit_cast(s16x4, b), c, 0, 0, 0); }
__device__ __forceinline__ f32x16 mfma32(v4u a, v4u b, f32x16 c) { return __builtin_amdgcn_mfma_f32_32x32x16_bf16(__builtin_bit_cast(bf16x8, a), __builtin_bit_cast(bf16x8, b), c, 0, 0, 0); }
__device__ __forceinline__ float ex2(float x) { return __builtin_amdgcn_exp2f(x); }
__device__ __forceinline__ float rcp(float x) { return __builtin_amdgcn_rcpf(x); }
__device__ __forceinline__ float asf(u32 x) { return __builtin_bit_cast(float, x); }
__device__ __forceinline__ u32 asu(float x) { return __builtin_bit_cast(u32, x); }
template <int CTRL> __device__ __forceinline__ float dpp(float x) { return asf((u32)__builtin_amdgcn_mov_dpp((int)asu(x), CTRL, 0xf, 0xf, true)); }
__device__ __forceinline__ float x16max(float x) { auto s = __builtin_amdgcn_permlane16_swap(asu(x), asu(x), false, false); return fmaxf(asf(s[0]), asf(s[1])); }
__device__ __forceinline__ float x32max(float x) { auto s = __builtin_amdgcn_permlane32_swap(asu(x), asu(x), false, false); return fmaxf(asf(s[0]), asf(s[1])); }
__device__ __forceinline__ float x16sum(float x) { auto s = __builtin_amdgcn_permlane16_swap(asu(x), asu(x), false, false); return asf(s[0]) + asf(s[1]); }
__device__ __forceinline__ float x32sum(float x) { auto s = __builtin_amdgcn_permlane32_swap(asu(x), asu(x), false, false); return asf(s[0]) + asf(s[1]); }
__device__ __forceinline__ float row16max(float x) { x = fmaxf(x, dpp<0x128>(x)); x = fmaxf(x, dpp<0x124>(x)); x = fmaxf(x, dpp<0x122>(x)); return fmaxf(x, dpp<0x121>(x)); }
__device__ __forceinline__ float row16sum(float x) { x += dpp<0x128>(x); x += dpp<0x124>(x); x += dpp<0x122>(x); return x + dpp<0x121>(x); }
__device__ __forceinline__ float max4(f32x4 a) { return fmaxf(fmaxf(a.x, a.y), fmaxf(a.z, a.w)); }
__device__ __forceinline__ float sum4(f32x4 a) { return (a.x + a.y) + (a.z + a.w); }
#define ZERO4 f32x4{0.f, 0.f, 0.f, 0.f}

#define BAR() asm volatile("s_waitcnt lgkmcnt(0)\n\ts_barrier" ::: "memory")
#define VMWAIT(n) asm volatile("s_waitcnt vmcnt(" #n ")" ::: "memory")

template <int PACED, bool NT, bool WAR = true>
__device__ __forceinline__ void dma4(u64 base, u32 o0, u32 o1, u32 o2, u32 o3, u32 lds) {
  if (WAR) asm volatile("s_waitcnt lgkmcnt(0)" ::: "memory");
#define DMA_BODY(W, P) \
  asm volatile("s_mov_b32 m0, %5\n\ts_nop 0\n\t" W "global_load_lds_dwordx4 %0, %4" P "\n\t" W "global_load_lds_dwordx4 %1, %4 offset:1024" P "\n\t" \
               W "global_load_lds_dwordx4 %2, %4 offset:2048" P "\n\t" W "global_load_lds_dwordx4 %3, %4 offset:3072" P \
               :: "v"(o0), "v"(o1), "v"(o2), "v"(o3), "s"(base), "s"(lds) : "memory")
  if (PACED == 8) DMA_BODY("s_waitcnt vmcnt(7)\n\t", " nt");
  else if (PACED && NT) DMA_BODY("s_waitcnt vmcnt(3)\n\t", " nt");
  else if (PACED) DMA_BODY("s_waitcnt vmcnt(3)\n\t", "");
  else if (NT) DMA_BODY("", " nt");
  else DMA_BODY("", "");
#undef DMA_BODY
}

struct Lane { u32 m0, m1, m2, m3; };
__device__ __forceinline__ Lane make_lane(int c) {
  const int j0 = c & 7; const u32 hm = (j0 & 1) ? 0xFFFF0000u : 0x0000FFFFu;
  return Lane{(j0 >> 1) == 0 ? hm : 0u, (j0 >> 1) == 1 ? hm : 0u, (j0 >> 1) == 2 ? hm : 0u, (j0 >> 1) == 3 ? hm : 0u};
}
__device__ __forceinline__ v4u diag(v2u xs, int i, const Lane& L) {
  const u32 src = i < 2 ? xs.x : xs.y;
  const u32 pp = __builtin_amdgcn_perm(src, src, (i & 1) ? 0x03020302u : 0x01000100u);
  return v4u{pp & L.m0, pp & L.m1, pp & L.m2, pp & L.m3};
}
__device__ __forceinline__ f32x4 gemv4(const v4u* W, v2u xs, const Lane& L, f32x4 acc) {
#pragma unroll
  for (int i = 0; i < 4; ++i) acc = mfma16(W[i], diag(xs, i, L), acc);
  return acc;
}
__device__ __forceinline__ void ldsW(v4u (&W)[4], LAS char* sm, u32 unit, int g, int c) {
#pragma unroll
  for (int i = 0; i < 4; ++i) W[i] = *(LAS v4u*)(sm + unit + (4 * g + i) * 256 + c * 16);
}
template <int STRIDE> __device__ __forceinline__ float reduce8(LAS char* sm, u32 off) {
  float v[8];
  asm volatile("" : "+v"(off));
#pragma unroll
  for (int w = 0; w < 8; ++w) v[w] = *(LAS float*)(sm + off + w * STRIDE);
  __builtin_amdgcn_sched_barrier(0);
  return ((v[0] + v[1]) + (v[2] + v[3])) + ((v[4] + v[5]) + (v[6] + v[7]));
}
__device__ __forceinline__ void st_gran(char* ws, u32 off, u32 val, u32 tag) { __hip_atomic_store((GAS u64*)(ws + off), ((u64)tag << 32) | val, RLX_AGENT); }
__device__ __forceinline__ u64 ld_gran(char* ws, u32 off) { return __hip_atomic_load((GAS u64*)(ws + off), RLX_AGENT); }
__device__ __forceinline__ v4u ld2(__amdgpu_buffer_rsrc_t rs, u32 off) { return __builtin_amdgcn_raw_buffer_load_b128(rs, off, 0, 16); }
__device__ __forceinline__ bool spin(u32& n) { asm volatile("s_sleep 1" ::: "memory"); return ++n > SPIN_CAP; }
__device__ __forceinline__ bool spinl(u32& n) { asm volatile("s_sleep 6" ::: "memory"); return ++n > SPIN_CAP; }
__device__ __forceinline__ void sleep_until(u64 t0, u32 ticks) {
  _Pragma("nounroll") for (u32 n = 0; n < 4096 && __builtin_amdgcn_s_memrealtime() - t0 < ticks; ++n) asm volatile("s_sleep 1" ::: "memory");
}
__device__ __forceinline__ void lds_add(LAS char* sm, u32 off, int lane) {
  asm volatile("s_waitcnt lgkmcnt(0)" ::: "memory");
  if (lane == 0) __hip_atomic_fetch_add((LAS u32*)(sm + off), 1u, RLX_WG);
}
__device__ __forceinline__ void lds_wait(LAS char* sm, u32 off, u32 want) {
  _Pragma("nounroll") for (u32 n = 0; n < (SPIN_CAP << 4); ++n) {
    if ((u32)__builtin_amdgcn_readfirstlane(__hip_atomic_load((LAS u32*)(sm + off), RLX_WG)) >= want) break;
  }
  asm volatile("" ::: "memory");
}
__device__ __forceinline__ void qa_issue(v4u (&q)[16], __amdgpu_buffer_rsrc_t rs, int xcd, int b, int lane) {
#pragma unroll
  for (int k = 0; k < 16; ++k) q[k] = ld2(rs, QA_OFF + (u32)(((16 * xcd + k) * 256 + b * 128 + 2 * lane) * 8));
}
__device__ __forceinline__ v4u qa_finish(v4u (&q)[16], __amdgpu_buffer_rsrc_t rs, char* ws, int xcd, int b, int lane, u32 tag) {
  _Pragma("nounroll") for (u32 n = 0;;) {
    bool ok = true;
#pragma unroll
    for (int k = 0; k < 16; ++k) ok &= (q[k].y == tag) & (q[k].w == tag);
    if (__all(ok) || spinl(n)) break;
    _Pragma("nounroll") while (!__all((u32)(ld_gran(ws, QA_OFF + (u32)((16 * xcd + (lane & 15)) * 256 + b * 128) * 8) >> 32) == tag) && !spinl(n)) {}
    qa_issue(q, rs, xcd, b, lane);
  }
  v4u r;
#pragma unroll
  for (int hh = 0; hh < 4; ++hh)
    r[hh] = cvtpk((asf(q[4 * hh].x) + asf(q[4 * hh + 1].x)) + (asf(q[4 * hh + 2].x) + asf(q[4 * hh + 3].x)),
                  (asf(q[4 * hh].z) + asf(q[4 * hh + 1].z)) + (asf(q[4 * hh + 2].z) + asf(q[4 * hh + 3].z)));
  return r;
}
__device__ __forceinline__ void put_qa(LAS char* sm, v4u r, int lane) {
#pragma unroll
  for (int hh = 0; hh < 4; ++hh) *(LAS u32*)(sm + L_QIMG + hh * 256 + lane * 4) = r[hh];
  lds_add(sm, F_Q, lane);
}
__device__ __forceinline__ void final_reduce(char* ws, u64 pOut, int which, int idx, int lane, u32 tag) {
  const u32 yo = YG_OFF + (u32)((which * 128 + idx) * 256 + lane) * 8;
  const u32 oo = (u32)(((idx >> 6) * 1024 + which * 512 + (idx & 63) * 8 + (lane & 7)) * 2);
  asm volatile("s_sleep %0" :: "i"(YDELAY) : "memory");
  u64 y0, y1, y2, y3;
  _Pragma("nounroll") for (u32 n = 0;;) {
    y0 = ld_gran(ws, yo); y1 = ld_gran(ws, yo + 512); y2 = ld_gran(ws, yo + 1024); y3 = ld_gran(ws, yo + 1536);
    if (__all(((u32)(y0 >> 32) == tag) & ((u32)(y1 >> 32) == tag) & ((u32)(y2 >> 32) == tag) & ((u32)(y3 >> 32) == tag)) || spin(n)) break;
  }
  float x = (asf((u32)y0) + asf((u32)y1)) + (asf((u32)y2) + asf((u32)y3));
  x += dpp<0x128>(x);
  x = x32sum(x16sum(x));
  if (lane < 8) *(GAS __bf16*)(pOut + oo) = (__bf16)x;
}

__global__ void __launch_bounds__(576, 1)
fused_decode(const u16* __restrict__ Bwq, const u16* __restrict__ Bc, char* ws, const u16* __restrict__ Bx, const u16* __restrict__ Ax, const u16* __restrict__ Awq,
             const u16* __restrict__ Bwuk, const u16* __restrict__ Ak, const u16* __restrict__ Av, const u16* __restrict__ Awo, const u16* __restrict__ Bwuv,
             const u16* __restrict__ Bwo, u16* __restrict__ out) {
  __shared__ __attribute__((aligned(16))) char smem[163840];
  u64 pc0; asm volatile("s_getpc_b64 %0" : "=s"(pc0));
  LAS char* sm = (LAS char*)smem;
  const u32 sm0 = (u32)(size_t)sm;
  const int tid = threadIdx.x, lane = tid & 63, w = __builtin_amdgcn_readfirstlane(tid >> 6), g = lane >> 4, c = lane & 15;
  const int bid = blockIdx.x, xcd = bid & 7, isB = bid >> 7, jj = (bid >> 3) & 15, qt = jj & 3, h = 4 * xcd + (jj >> 2);
  u64 pAx = (u64)Ax, pAk = (u64)Ak, pAv = (u64)Av, pAwq = (u64)Awq, pAwo = (u64)Awo, pBx = (u64)Bx, pBc = (u64)Bc, pBwq = (u64)Bwq, pBwuk = (u64)Bwuk, pBwuv = (u64)Bwuv, pBwo = (u64)Bwo, pOut = (u64)out;
  const u32 ring = sm0 + w * 16384, ringo = w * 16384;
  const u32 wrow = (u32)(16 * w * 8192 + h * 256);
  const u32 wo0 = g * 8192 + c * 16, wo1 = wo0 + 31744, wo2 = wo0 + 2 * 31744, wo3 = wo0 + 3 * 31744, lo16 = lane * 16;
  const int bB = xcd >> 2, sB = (xcd & 3) * 16 + jj;
  const u64 cb = pBc + (u32)((bB * SK + sB * 128 + 4 * w) * 1024);
  const u32 co0 = lo16 ^ ((w & 3) * 16), cl = sm0 + w * 4096;
  u64 ht0 = 0;
  if (w == 8) asm volatile("s_memrealtime %0\n\ts_waitcnt lgkmcnt(0)" : "=s"(ht0) :: "memory");
  if (w < 8) {
    asm volatile("s_nop 4" ::: "memory");
    if (isB) dma4<false, true, false>(cb, co0, co0 ^ 64, co0 ^ 128, co0 ^ 192, cl);
    else dma4<false, false, false>(pBwq + (wrow + (u32)qt * (128 * 8192)), wo0, wo1, wo2, wo3, ring + 4096);
  }
  asm volatile("" : "+s"(pAx), "+s"(pAk), "+s"(pAv), "+s"(pAwo), "+s"(pBx), "+s"(pAwq), "+s"(pBwuk), "+s"(pBwuv), "+s"(pBwo), "+s"(pOut));
  u32 tag = *(const __attribute__((address_space(4))) u32*)(u64)ws;
  const __amdgpu_buffer_rsrc_t rs = __builtin_amdgcn_make_buffer_rsrc(ws, 0, (int)WS_USED, 0x00020000);
  const int isT = isB & ~jj & 1;
  const int bA = jj >> 3, sA = isB ? 8 + 4 * isT + ((jj >> 1) & 3) : (jj & 7);

  if (w == 8) {
    asm volatile("" : "+s"(tag)); tag += 1u;
    __builtin_amdgcn_s_setprio(3);
    u32 wa[6];
    {
      const u64 pc = pc0 & ~255ull;
      const u32 co = (u32)((jj & 3) * 6 * 1024 + lane * 16);
#pragma unroll
      for (int k = 0; k < 5; ++k) wa[k] = *(const GAS u32*)(pc + min(co + k * 1024 + (k == 4 ? (lane & 1) * 1024 : 0), (u32)(CODE_KB * 1024)));
      wa[5] = *(const GAS u32*)pOut;
      asm volatile("" ::: "memory");
    }
    if (!isB) {
      if (lane < 16) *(LAS u32*)(sm + L_FLAG + lane * 4) = 0u;
      BAR(); BAR();
      sleep_until(ht0, QA_T);
      v4u q[16];
      qa_issue(q, rs, xcd, bA, lane);
      put_qa(sm, qa_finish(q, rs, ws, xcd, bA, lane, tag), lane);
    } else {
      BAR();
      v4u q[16];
      sleep_until(ht0, QA_T + 45);
      qa_issue(q, rs, xcd, bA, lane);
      sleep_until(ht0, QS_T);
      BAR(); BAR(); BAR(); BAR();
      if (lane < 16) *(LAS u32*)(sm + L_FLAG + lane * 4) = 0u;
      BAR(); BAR();
      put_qa(sm, qa_finish(q, rs, ws, xcd, bA, lane, tag), lane);
      lds_wait(sm, F_DRAIN, 8);
      if (lane < 32) st_gran(ws, LB_OFF + (u32)((bB * 64 + sB) * 32 + lane) * 8, *(LAS u32*)(sm + L_LSE + lane * 4), tag);
      if (isT) {
        u64 l0, l1;
        asm volatile("s_sleep %0" :: "i"(LDELAY) : "memory");
        _Pragma("nounroll") for (u32 n = 0;;) {
          l0 = ld_gran(ws, LB_OFF + (u32)(lane * 32 + h) * 8); l1 = ld_gran(ws, LB_OFF + (u32)((64 + lane) * 32 + h) * 8);
          if (__all(((u32)(l0 >> 32) == tag) & ((u32)(l1 >> 32) == tag)) || spin(n)) break;
        }
        const float f0 = asf((u32)l0), f1 = asf((u32)l1);
        const float e0 = ex2(f0 - x32max(x16max(row16max(f0)))), e1 = ex2(f1 - x32max(x16max(row16max(f1))));
        *(LAS __bf16*)(sm + L_LBV + lane * 2) = (__bf16)(e0 * rcp(x32sum(x16sum(row16sum(e0)))));
        *(LAS __bf16*)(sm + L_LBV + 128 + lane * 2) = (__bf16)(e1 * rcp(x32sum(x16sum(row16sum(e1)))));
        asm volatile("s_waitcnt lgkmcnt(0)" ::: "memory");
      }
    }
    asm volatile("" :: "v"(wa[0]), "v"(wa[1]), "v"(wa[2]), "v"(wa[3]), "v"(wa[4]), "v"(wa[5]));
    return;
  }

  const u32 sw = (c ^ g) * 16;
  const u32 ko0 = g * 256 + sw, ko1 = g * 256 + (sw ^ 64), ko2 = g * 256 + (sw ^ 128), ko3 = g * 256 + (sw ^ 192);
  const int nt = isT ? NT : isB ? NX : NA, T = (nt >> 3) + (w < (nt & 7));
  const u32 kv_off = (u32)((bA * 8 + xcd) * SK + 16 * ((isT ? 8 * NA + 4 * NX + NT * (sA - 12) : isB ? 8 * NA + NX * (sA - 8) : NA * sA) + w)) * 256;
  u64 kb = pAk + kv_off, vb = pAv + kv_off, fb = pAwo + (u32)((128 * h + 16 * w) * 1024 + qt * 256);
  asm volatile("" : "+s"(kb), "+s"(vb), "+s"(fb));
  const int tb = (tid >> 5) & 1, tcol = 32 * ((tid >> 6) & 3) + (tid & 31), tr = (tid & 0xE0) | ((tid & 7) << 2) | ((tid >> 3) & 3);
  const Lane L = make_lane(c);

  const int half = (jj >> 1) & 1;
  v4u UW[8], X[8];
  if (!isB) {
    {
      const u64 src = pBx + (u32)(((lane >> 1) & 1) * 512 + 128 * ((lane >> 2) & 3) + 16 * w + 8 * (lane & 1)) * 2;
      asm volatile("s_mov_b32 m0, %1\n\ts_nop 4\n\tglobal_load_lds_dwordx4 %0, off" :: "v"(src), "s"(sm0 + L_XS + w * 1024) : "memory");
    }
    const u64 wq_b = pBwq + wrow;
    const u32 xso = L_XS + w * 1024 + (c >> 3) * 32 + g * 8;
    f32x4 acc = ZERO4;
    v4u W[4];
    asm volatile("" : "+s"(tag)); tag += 1u;
    dma4<false, true, false>(pBwuk + (wrow + (u32)qt * (128 * 8192)), g * 8192 + sw, g * 8192 + (sw ^ 64) + 31744, g * 8192 + (sw ^ 128) + 2 * 31744, g * 8192 + (sw ^ 192) + 3 * 31744, ring + 3 * 4096);
    VMWAIT(4);
    dma4<false, false, false>(wq_b + (u32)(((qt + 1) & 3) * (128 * 8192)), wo0, wo1, wo2, wo3, ring + 2 * 4096);
    dma4<false, false, false>(wq_b + (u32)(((qt + 2) & 3) * (128 * 8192)), wo0, wo1, wo2, wo3, ring);
    ldsW(W, sm, ringo + 4096, g, c); acc = gemv4(W, *(LAS v2u*)(sm + xso + qt * 64), L, acc);
    dma4<false, false>(wq_b + (u32)(((qt + 3) & 3) * (128 * 8192)), wo0, wo1, wo2, wo3, ring + 4096);
    VMWAIT(8);
    ldsW(W, sm, ringo + 2 * 4096, g, c); acc = gemv4(W, *(LAS v2u*)(sm + xso + ((qt + 1) & 3) * 64), L, acc);
    VMWAIT(4);
    ldsW(W, sm, ringo, g, c); acc = gemv4(W, *(LAS v2u*)(sm + xso + ((qt + 2) & 3) * 64), L, acc);
    dma4<true, true>(kb, ko0, ko1, ko2, ko3, ring);
    ldsW(W, sm, ringo + 4096, g, c); acc = gemv4(W, *(LAS v2u*)(sm + xso + ((qt + 3) & 3) * 64), L, acc);
    *(LAS f32x4*)(sm + L_PART + w * 1024 + lo16) = acc;
    BAR();
    if (tid < 256) *(LAS __bf16*)(sm + L_XBUF + (tb * 128 + tcol) * 2) = (__bf16)reduce8<1024>(sm, L_PART + tr * 4);
    BAR();
    {
      f32x4 qa = ZERO4;
#pragma unroll
      for (int k = 0; k < 4; ++k)
        qa = mfma16(*(LAS v4u*)(sm + ringo + 3 * 4096 + c * 256 + ((4 * k + g) ^ c) * 16), *(LAS v4u*)(sm + L_XBUF + (c & 1) * 256 + k * 64 + g * 16), qa);
      if (c < 2) {
        __builtin_amdgcn_raw_buffer_store_b128(v4u{cvtpk(qa.x, qa.y), tag, cvtpk(qa.z, qa.w), tag}, rs, QG_OFF + (u32)c * 65536 + (u32)(((8 * qt + w) * 32 + h) * 4 + g) * 16, 0, 16);
      }
    }
  } else {
    const int h2 = lane >> 5, hd = lane & 31;
    v2u xs;
    asm volatile("global_load_dwordx2 %0, %1, %2" : "=v"(xs) : "v"((u32)(((c >> 3) * 512 + 128 * qt + 16 * w + 4 * g) * 2)), "s"(pAx) : "memory");
    dma4<true, true, false>(pAwq + (wrow + (u32)qt * (128 * 8192)), wo0, wo1, wo2, wo3, sm0 + EXTRA + w * 4096);
    asm volatile("" : "+v"(xs));
    asm volatile("" : "+s"(tag)); tag += 1u;
    dma4<true, true, false>(cb + 32768, co0, co0 ^ 64, co0 ^ 128, co0 ^ 192, cl + 32768);
    v4u KV[8];
#pragma unroll
    for (int i = 0; i < 4; ++i) KV[i] = __builtin_nontemporal_load((const GAS v4u*)(kb + (u32)((ko0 ^ (64 * i)) + 1024 * i)));
    const u64 ub = isT ? pBwuv + (u32)((256 * half + 32 * w) * 8192 + h * 256) : kb + 32768;
    const u32 uo = isT ? g * 32768 + c * 16 : ko0, us = isT ? 8192 : 1024, ux = isT ? 0 : 64;
    {
      v4u W[4];
      ldsW(W, sm, EXTRA + w * 4096, g, c);
      const f32x4 acc = gemv4(W, xs, L, ZERO4);
      *(LAS f32x4*)(sm + EXTRA + w * 4096 + lo16) = acc;
    }
    BAR();
    if (tid < 256) st_gran(ws, QA_OFF + (u32)((h * 4 + qt) * 256 + tb * 128 + tcol) * 8, asu(reduce8<4096>(sm, EXTRA + tr * 4)), tag);
    dma4<true, true, false>(cb + 2 * 32768, co0, co0 ^ 64, co0 ^ 128, co0 ^ 192, cl + 2 * 32768);
    dma4<true, true, false>(cb + 3 * 32768, co0, co0 ^ 64, co0 ^ 128, co0 ^ 192, cl + 3 * 32768);
    BAR();
    {
      const u32 qg = QG_OFF + (u32)bB * 65536 + (u32)tid * 16, ql = L_QB + (tid >> 7) * 1024 + ((tid >> 1) & 1) * 512 + ((tid >> 2) & 31) * 16 + (tid & 1) * 8;
      _Pragma("nounroll") for (u32 n = 0;;) {
        v4u q[8]; bool ok = true;
#pragma unroll
        for (int k = 0; k < 8; ++k) q[k] = ld2(rs, qg + ((k + jj) & 7) * 8192);
#pragma unroll
        for (int k = 0; k < 8; ++k) { ok &= (q[k].y == tag) & (q[k].w == tag); *(LAS v2u*)(sm + ql + ((k + jj) & 7) * 4096) = v2u{q[k].x, q[k].z}; }
        if (__all(ok) || ++n > SPIN_CAP) break;
        asm volatile("s_sleep 4" ::: "memory");
      }
    }
    VMWAIT(0);
#pragma unroll
    for (int i = 0; i < 4; ++i) KV[4 + i] = __builtin_nontemporal_load((const GAS v4u*)(vb + (u32)(lo16 + 1024 * i)));
    BAR();
    f32x16 sc;
#pragma unroll
    for (int k = 0; k < 16; ++k) sc[k] = 0.f;
    float mw = 0.f;
    if (w < 4) {
      const u32 ca = (32 * w + hd) * 1024, cx = (h2 ^ (((hd & 3) << 2) | ((hd >> 2) & 3))) * 16;
      u32 qo = L_QB + lo16;
      asm volatile("" : "+v"(qo));
      v4u fa[3][2], fq[3][2];
#define QK_LOAD(B) _Pragma("unroll") for (int k = 0; k < 2; ++k) { fa[(B) % 3][k] = *(LAS v4u*)(sm + ca + ((B) >> 2) * 256 + (cx ^ ((2 * ((B) & 3) + k) * 32))); fq[(B) % 3][k] = *(LAS v4u*)(sm + qo + (2 * (B) + k) * 1024); }
      QK_LOAD(0) QK_LOAD(1)
      BAR();
#pragma unroll
      for (int bb = 0; bb < 16; ++bb) {
        if (bb < 14) { QK_LOAD(bb + 2) }
        __builtin_amdgcn_sched_barrier(0);
#pragma unroll
        for (int k = 0; k < 2; ++k) sc = mfma32(fa[bb % 3][k], fq[bb % 3][k], sc);
        __builtin_amdgcn_sched_barrier(0);
      }
#undef QK_LOAD
      mw = sc[0];
#pragma unroll
      for (int k = 1; k < 16; ++k) mw = fmaxf(mw, sc[k]);
      mw = x32max(mw);
    } else BAR();
#pragma unroll
    for (int i = 0; i < 4; ++i) UW[i] = __builtin_nontemporal_load((const GAS v4u*)(ub + (u32)((uo ^ (ux * i)) + us * i)));
    asm volatile("" ::: "memory");
    if (w < 4 && lane < 32) *(LAS float*)(sm + L_BM + hd * 16 + w * 4) = mw;
    BAR();
    if (w < 4) {
      const float M = C2 * max4(*(LAS f32x4*)(sm + L_BM + hd * 16));
      float ls = 0.f;
#pragma unroll
      for (int k = 0; k < 16; ++k) { sc[k] = ex2(fmaf(sc[k], C2, -M)); ls += sc[k]; }
      *(LAS v4u*)(sm + L_PF + w * 2048 + lo16) = v4u{cvtpk(sc[0], sc[1]), cvtpk(sc[2], sc[3]), cvtpk(sc[4], sc[5]), cvtpk(sc[6], sc[7])};
      *(LAS v4u*)(sm + L_PF + w * 2048 + 1024 + lo16) = v4u{cvtpk(sc[8], sc[9]), cvtpk(sc[10], sc[11]), cvtpk(sc[12], sc[13]), cvtpk(sc[14], sc[15])};
      ls = x32sum(ls);
      if (lane < 32) *(LAS float*)(sm + L_BL + hd * 16 + w * 4) = ls;
    }
    BAR();
    {
      const int q = (lane >> 2) & 3, p = lane & 3, blk = g & 1;
      const u32 base = (4 * h2 + q) * 1024 + (w >> 1) * 256 + 8 * (p & 1), lo4 = 8 * (w & 1) + 2 * blk + (p >> 1);
      u32 po = L_PF + lo16;
      asm volatile("" : "+v"(po));
      const float Ls = sum4(*(LAS f32x4*)(sm + L_BL + hd * 16));
      const float inv = rcp(Ls);
      if (tid < 32) *(LAS float*)(sm + L_LSE + tid * 4) = fmaf(C2, max4(*(LAS f32x4*)(sm + L_BM + hd * 16)), __builtin_amdgcn_logf(Ls));
      const u32 so = hd * 1024 + (w >> 1) * 256 + 8 * h2, sx = (8 * (w & 1)) ^ (((hd & 3) << 2) | ((hd >> 2) & 3));
      const int hr = lane >> 2;
      const u32 ro = hr * 1024 + (w >> 1) * 256, rx = (8 * (w & 1) + p) ^ (((hr & 3) << 2) | (hr >> 2));
      const u32 go = PB_OFF + (u32)(((hr * 2 + (w >> 2)) * 2 + bB) * 64 + sB) * 512 + (w & 3) * 128 + p * 16;
#define TRR(A) __builtin_bit_cast(v2u, __builtin_amdgcn_ds_read_tr16_b64_v4i16((LAS s16x4*)(sm + (A))))
#pragma unroll
      for (int e = 0; e < 2; ++e) {
        const u32 a0 = base + 16 * ((lo4 + 4 * e) ^ ((q << 2) | h2)), a1 = base + 8192 + 16 * ((lo4 + 4 * e) ^ ((q << 2) | (2 + h2)));
        f32x16 o;
#pragma unroll
        for (int k = 0; k < 16; ++k) o[k] = 0.f;
        v4u pb[3]; v2u t0[3], t1[3];
#define PV_LOAD(K) { pb[(K) % 3] = *(LAS v4u*)(sm + po + (K) * 1024); t0[(K) % 3] = TRR(a0 + (K) * 16384); t1[(K) % 3] = TRR(a1 + (K) * 16384); }
        PV_LOAD(0) PV_LOAD(1)
#pragma unroll
        for (int ks = 0; ks < 8; ++ks) {
          if (ks < 6) PV_LOAD(ks + 2)
          __builtin_amdgcn_sched_barrier(0);
          o = mfma32(v4u{t0[ks % 3].x, t0[ks % 3].y, t1[ks % 3].x, t1[ks % 3].y}, pb[ks % 3], o);
          __builtin_amdgcn_sched_barrier(0);
        }
#undef PV_LOAD
#pragma unroll
        for (int k = 0; k < 4; ++k)
          *(LAS v2u*)(sm + so + (sx ^ (4 * e + k)) * 16) = v2u{cvtpk(o[4 * k] * inv, o[4 * k + 1] * inv), cvtpk(o[4 * k + 2] * inv, o[4 * k + 3] * inv)};
        asm volatile("s_waitcnt lgkmcnt(0)" ::: "memory");
        const v4u v0 = *(LAS v4u*)(sm + ro + (rx ^ (4 * e)) * 16), v1 = *(LAS v4u*)(sm + ro + 16384 + (rx ^ (4 * e)) * 16);
        __builtin_amdgcn_raw_buffer_store_b128(v0, rs, go + e * 64, 0, 16);
        __builtin_amdgcn_raw_buffer_store_b128(v1, rs, go + e * 64 + 16 * 2 * 2 * 64 * 512, 0, 16);

      }
#undef TRR
    }
    BAR();
    if (!isT) {
      dma4<false, true, false>(vb + 32768, lo16, lo16, lo16, lo16, ring + 3 * 4096);
#pragma unroll
      for (int i = 0; i < 4; ++i) *(LAS v4u*)(sm + ringo + 8192 + i * 1024 + lo16) = UW[i];
    }
#pragma unroll
    for (int i = 0; i < 8; ++i) *(LAS v4u*)(sm + ringo + i * 1024 + lo16) = KV[i];
    if (isT) VMWAIT(0); else VMWAIT(4);
    lds_add(sm, F_DRAIN, lane);
  }
  if (!isB) {
    dma4<true, true>(vb, lo16, lo16, lo16, lo16, ring + 4096);
    dma4<true, true>(kb + 32768, ko0, ko1, ko2, ko3, ring + 2 * 4096);
    dma4<true, true>(vb + 32768, lo16, lo16, lo16, lo16, ring + 3 * 4096);
  }
  lds_wait(sm, F_Q, 1);
  float Mw;
  {
    v4u qf[4];
#pragma unroll
    for (int k = 0; k < 4; ++k) qf[k] = *(LAS v4u*)(sm + L_QIMG + (c & 3) * 256 + k * 64 + g * 16);
    const u32 pm0 = (c >> 2) == 0 ? 0x0000FFFFu : (c >> 2) == 1 ? 0xFFFF0000u : 0u, pm1 = (c >> 2) == 2 ? 0x0000FFFFu : (c >> 2) == 3 ? 0xFFFF0000u : 0u;
    float m = -INFINITY, l = 0.f;
    f32x4 o0 = ZERO4, o1 = ZERO4;
    const u32 kr = ringo + c * 256, vr = ringo + 4096 + g * 1024 + c * 16;
    v2u y[4];
#define QK_STEP(SO) { \
      const f32x4 sa = mfma16(*(LAS v4u*)(sm + kr + (SO) + ((8 + g) ^ c) * 16), qf[2], mfma16(*(LAS v4u*)(sm + kr + (SO) + ((0 + g) ^ c) * 16), qf[0], ZERO4)) \
                     + mfma16(*(LAS v4u*)(sm + kr + (SO) + ((12 + g) ^ c) * 16), qf[3], mfma16(*(LAS v4u*)(sm + kr + (SO) + ((4 + g) ^ c) * 16), qf[1], ZERO4)); \
      const float mn = fmaxf(m, x32max(x16max(max4(sa)))); \
      const float al = ex2((m - mn) * C2), mc = mn * C2; \
      m = mn; \
      const float p[4] = {ex2(fmaf(sa.x, C2, -mc)), ex2(fmaf(sa.y, C2, -mc)), ex2(fmaf(sa.z, C2, -mc)), ex2(fmaf(sa.w, C2, -mc))}; \
      l = fmaf(l, al, (p[0] + p[1]) + (p[2] + p[3])); \
      o0 *= al; o1 *= al; \
      _Pragma("unroll") for (int i = 0; i < 4; ++i) { const u32 pp = cvtpk(p[i], p[i]); y[i] = v2u{pp & pm0, pp & pm1}; } }
#define PV_STEP(SO) _Pragma("unroll") for (int i = 0; i < 4; ++i) { \
      const v4u V = *(LAS v4u*)(sm + vr + (SO) + i * 256); \
      o0 = mfma16h(v2u{V.x, V.y}, y[i], o0); o1 = mfma16h(v2u{V.z, V.w}, y[i], o1); }
#pragma unroll 1
    for (int t = -1; t < T - 1; ++t) {
      const u32 so = (t & 1) * 8192;
      if (t >= 0) {
        if (t + 2 < T) dma4<8, true>(kb + (u32)((t + 2) * 32768), ko0, ko1, ko2, ko3, ring + so);
        else VMWAIT(4);
        PV_STEP(so)
      }
      QK_STEP(so ^ 8192)
      if (t >= 0 && t + 2 < T) dma4<8, true>(vb + (u32)((t + 2) * 32768), lo16, lo16, lo16, lo16, ring + so + 4096);
    }
    if (lane < 4) *(LAS float*)(sm + L_MST + lane * 32 + w * 4) = m;
    lds_add(sm, F_M, lane);
    l = x32sum(x16sum(l));
    u32 mo = L_MRG + w * 2048 + (c & 3) * 512 + ((4 * g + (c >> 2)) ^ (2 * (c & 3))) * 16, lo = L_MST + 128 + lane * 32 + w * 4, so = ((T - 1) & 1) * 8192;
    asm volatile("" : "+v"(mo), "+v"(lo), "+v"(l));
    lds_wait(sm, F_M, 8);
    Mw = fmaxf(max4(*(LAS f32x4*)(sm + L_MST + (c & 3) * 32)), max4(*(LAS f32x4*)(sm + L_MST + (c & 3) * 32 + 16)));
    float f = ex2((m - Mw) * C2);
    asm volatile("" : "+v"(f), "+v"(Mw));
    if (!isB) {
      const u32 f0 = g * 1024, fx = (c ^ (4 * g)) * 16;
      dma4<false, true, false>(fb, f0 + fx, f0 + 3072 + (fx ^ 16), f0 + 2 * 3072 + (fx ^ 32), f0 + 3 * 3072 + (fx ^ 48), ring);
      VMWAIT(4);
    } else if (!isT) VMWAIT(0);
    {
      v4u V[4];
#pragma unroll
      for (int i = 0; i < 4; ++i) V[i] = *(LAS v4u*)(sm + vr + so + i * 256);
      __builtin_amdgcn_sched_barrier(0);
#pragma unroll
      for (int i = 0; i < 4; ++i) { o0 = mfma16h(v2u{V[i].x, V[i].y}, y[i], o0); o1 = mfma16h(v2u{V[i].z, V[i].w}, y[i], o1); }
    }
    *(LAS f32x4*)(sm + mo) = o0 * f;
    *(LAS f32x4*)(sm + mo + 256) = o1 * f;
    if (lane < 4) *(LAS float*)(sm + lo) = l * f;
#undef QK_STEP
#undef PV_STEP
  }
  {
    u32 ro = L_MRG + w * 512 + ((lane & 31) ^ (2 * w)) * 16, go = OA_OFF + (u32)((4 * xcd + w) * 32 + bA * 16 + sA) * 640 + lane * 16;
    const float Mc = C2 * asf(__builtin_amdgcn_readlane(asu(Mw), w & 3));
    asm volatile("" : "+v"(ro), "+v"(go));
    BAR();
    if (isT) {
#pragma unroll
      for (int i = 0; i < 8; ++i) X[i] = ld2(rs, PB_OFF + (u32)((h * 2 + half) * 128 + 16 * w + 4 * g + (i & 3)) * 512 + (i >> 2) * 256 + c * 16);
      asm volatile("s_sleep 2" ::: "memory");
#pragma unroll
      for (int i = 0; i < 4; ++i) UW[4 + i] = __builtin_nontemporal_load((const GAS v4u*)(pBwuv + (u32)((256 * half + 32 * w + 16 + 4 * g + i) * 8192 + h * 256 + c * 16)));
      asm volatile("" ::: "memory");
    }
    if (w < 4) {
      f32x4 v[8];
#pragma unroll
      for (int ww = 0; ww < 8; ++ww) v[ww] = *(LAS f32x4*)(sm + ro + ww * 2048);
      const f32x4 la = *(LAS f32x4*)(sm + L_MST + 128 + w * 32), lb = *(LAS f32x4*)(sm + L_MST + 128 + w * 32 + 16);
      __builtin_amdgcn_sched_barrier(0);
      const float Ls = sum4(la + lb), inv = rcp(Ls);
      const f32x4 a = (((v[0] + v[1]) + (v[2] + v[3])) + ((v[4] + v[5]) + (v[6] + v[7]))) * inv;
      u32 lse = asu(Mc + __builtin_amdgcn_logf(Ls));
      asm volatile("" : "+v"(lse));
      const u32 p0 = lane < 32 ? cvtpk(a.x, a.y) : lse;
      if (lane < 40) __builtin_amdgcn_raw_buffer_store_b128(v4u{p0, tag, cvtpk(a.z, a.w), tag}, rs, go, 0, 16);
    }
  }
  if (isT) {
    v2u xw = *(LAS v2u*)(sm + L_LBV + w * 32 + g * 8);
    if ((c >> 3) != (w >> 2)) xw = v2u{0u, 0u};
    *(LAS f32x4*)(sm + L_PART + w * 1024 + lo16) = gemv4(X, xw, L, ZERO4);
    *(LAS f32x4*)(sm + L_PARTB + w * 1024 + lo16) = gemv4(X + 4, xw, L, ZERO4);
    asm volatile("" ::: "memory");
    v4u FW[8];
    const u32 po = (tid < 256 ? L_PART : L_PARTB) + tr * 4;
    BAR();
    {
      const u32 pq = po + tb * 4096;
      const float p0 = *(LAS float*)(sm + pq), p1 = *(LAS float*)(sm + pq + 1024), p2 = *(LAS float*)(sm + pq + 2048), p3 = *(LAS float*)(sm + pq + 3072);
      *(LAS __bf16*)(sm + L_XBUF + (tb * 256 + (tid >> 8) * 128 + tcol) * 2) = (__bf16)((p0 + p1) + (p2 + p3));
    }
    BAR();
    const u32 xo = L_XBUF + (c >> 3) * 512 + w * 64 + g * 8;
    *(LAS f32x4*)(sm + L_PART + w * 1024 + lo16) = gemv4(UW + 4, *(LAS v2u*)(sm + xo + 32), L, gemv4(UW, *(LAS v2u*)(sm + xo), L, ZERO4));
    BAR();
    float own = 0.f;
    if (w < 4) {
      own = reduce8<1024>(sm, po);
      st_gran(ws, CX_OFF + (u32)((h * 2 + half) * 256 + tid) * 8, asu(own), tag);
    } else asm volatile("s_sleep 3" ::: "memory");
#pragma unroll
    for (int i = 0; i < 8; ++i) FW[i] = __builtin_nontemporal_load((const GAS v4u*)(pBwo + (u32)((128 * h + 16 * w + 4 * g + (i & 3)) * 1024 + half * 512 + (i >> 2) * 256 + c * 16)));
    asm volatile("" ::: "memory");
    if (w < 4) {
      u64 x;
      asm volatile("s_sleep %0" :: "i"(CDELAY) : "memory");
      _Pragma("nounroll") for (u32 n = 0;;) { x = ld_gran(ws, CX_OFF + (u32)((h * 2 + (half ^ 1)) * 256 + tid) * 8); if (__all((u32)(x >> 32) == tag) || spin(n)) break; }
      *(LAS __bf16*)(sm + L_XBUF + (tb * 128 + tcol) * 2) = (__bf16)(half ? asf((u32)x) + own : own + asf((u32)x));
    }
    BAR();
    const v2u xc = *(LAS v2u*)(sm + L_XBUF + (c >> 3) * 256 + w * 32 + g * 8);
    *(LAS f32x4*)(sm + L_PART + w * 1024 + lo16) = gemv4(FW, xc, L, ZERO4);
    *(LAS f32x4*)(sm + L_PARTB + w * 1024 + lo16) = gemv4(FW + 4, xc, L, ZERO4);
    BAR();
    st_gran(ws, YG_OFF + (u32)((((2 + tb) * 64 + 32 * half + 16 * (tid >> 8) + (tcol >> 3)) * 32 + h) * 8 + (tcol & 7)) * 8, asu(reduce8<1024>(sm, po)), tag);
    if (w < 2) final_reduce(ws, pOut, 1, (xcd * 8 + (jj >> 1)) * 2 + w, lane, tag);
    return;
  }
  if (isB) return;
  if (w >= 2) { VMWAIT(0); lds_add(sm, F_FIN, lane); }
  if (w < 4) {
    if (w >= 2) return;
    const int ob = w;
    const u32 ro = OA_OFF + (u32)(h * 32 + 16 * ob + g) * 640;
    v4u x[8]; u64 ls[4];
    {
      const u32 so = OA_OFF + (u32)(h * 32 + 16 * ob + c) * 640 + 512;
      _Pragma("nounroll") for (u32 n = 0;;) {
        const u64 z = ld_gran(ws, so);
        if (__builtin_popcount((u32)__ballot((u32)(z >> 32) == tag) & 0xFFFFu) >= OTHR || ++n > SPIN_CAP) break;
        asm volatile("s_sleep 1" ::: "memory");
      }
    }
    VMWAIT(0);
    lds_add(sm, F_FIN, lane);
    _Pragma("nounroll") for (u32 n = 0;;) {
      bool ok = true;
#pragma unroll
      for (int k = 0; k < 4; ++k) { x[2 * k] = ld2(rs, ro + k * 2560 + c * 32); x[2 * k + 1] = ld2(rs, ro + k * 2560 + c * 32 + 16); ls[k] = ld_gran(ws, ro + k * 2560 + 512); }
#pragma unroll
      for (int k = 0; k < 4; ++k) ok &= (x[2 * k].y == tag) & (x[2 * k].w == tag) & (x[2 * k + 1].y == tag) & (x[2 * k + 1].w == tag) & ((u32)(ls[k] >> 32) == tag);
      if (__all(ok) || ++n > SPIN_CAP) break;
      asm volatile("s_sleep 1" ::: "memory");
    }
    const float M = x32max(x16max(fmaxf(fmaxf(asf((u32)ls[0]), asf((u32)ls[1])), fmaxf(asf((u32)ls[2]), asf((u32)ls[3])))));
    f32x4 oc = ZERO4; float lw = 0.f;
#pragma unroll
    for (int k = 0; k < 4; ++k) {
      const float e = ex2(asf((u32)ls[k]) - M);
      const u32 pp = cvtpk(e, e);
      lw += asf(pp << 16);
      oc = mfma16(v4u{x[2 * k].x, x[2 * k].z, x[2 * k + 1].x, x[2 * k + 1].z}, v4u{pp & L.m0, pp & L.m1, pp & L.m2, pp & L.m3}, oc);
    }
    const float inv = rcp(x32sum(x16sum(lw)));
    if (c < 8) {
      const u32 xo = L_XBUF + ob * 256 + (16 * ((c >> 1) & 1) + 8 * (c & 1) + 4 * (g >> 1) + (c >> 2)) * 2;
#pragma unroll
      for (int r = 0; r < 4; ++r) *(LAS __bf16*)(sm + xo + (32 * ((2 * (g & 1) + (r >> 1))) + 2 * (r & 1)) * 2) = (__bf16)(oc[r] * inv);
    }
    lds_add(sm, F_FIN + 8 + 4 * ob, lane);
    return;
  }
  {
    const int q = (lane >> 2) & 3, p = lane & 3;
    lds_wait(sm, F_FIN, 8);
    v4u wt[4][2];
#pragma unroll
    for (int k = 0; k < 4; ++k) {
      const u32 ra = (2 * k + (g >> 1)) * 16384 + (8 * (g & 1) + q) * 256 + 8 * (p & 1);
#pragma unroll
      for (int e = 0; e < 2; ++e) {
        const u32 ch = 4 * (w - 4) + 2 * e + (p >> 1);
        const v2u t0 = __builtin_bit_cast(v2u, __builtin_amdgcn_ds_read_tr16_b64_v4i16((LAS s16x4*)(sm + ra + (ch ^ ((q << 2) | (2 * (g & 1)))) * 16)));
        const v2u t1 = __builtin_bit_cast(v2u, __builtin_amdgcn_ds_read_tr16_b64_v4i16((LAS s16x4*)(sm + ra + 1024 + (ch ^ ((q << 2) | (2 * (g & 1) + 1))) * 16)));
        wt[k][e] = v4u{t0.x, t0.y, t1.x, t1.y};
        asm volatile("" : "+v"(wt[k][e]));
      }
    }
    u32 xa = L_XBUF + (c & 1) * 256 + g * 16, yo = YG_OFF + (u32)((((g & 1) * 64 + 16 * qt + 4 * (w - 4) + 2 * (g >> 1) + (c >> 3)) * 32 + h) * 8 + (c & 7)) * 8;
    asm volatile("" : "+v"(xa), "+v"(yo));
    _Pragma("nounroll") for (u32 n = 0; n < (SPIN_CAP << 4); ++n) {
      const u64 f12 = __hip_atomic_load((LAS u64*)(sm + F_FIN + 8), RLX_WG);
      if (__builtin_amdgcn_readfirstlane(((u32)f12 > 0) & ((u32)(f12 >> 32) > 0))) break;
    }
    asm volatile("" ::: "memory");
    v4u xo[4];
#pragma unroll
    for (int k = 0; k < 4; ++k) xo[k] = *(LAS v4u*)(sm + xa + k * 64);
    __builtin_amdgcn_sched_barrier(0);
    f32x4 y0 = ZERO4, y1 = ZERO4;
#pragma unroll
    for (int k = 0; k < 4; ++k) { y0 = mfma16(xo[k], wt[k][0], y0); y1 = mfma16(xo[k], wt[k][1], y1); }
    st_gran(ws, yo, asu(g == 0 ? y0.x : g == 1 ? y0.y : g == 2 ? y1.x : y1.y), tag);
  }
  if (w != 4) return;
  final_reduce(ws, pOut, 0, bid, lane, tag);
  if (bid == 0 && lane == 0) *(GAS u32*)ws = tag;
}

extern "C" void kernel_launch(void* const* d_in, const int* in_sizes, int n_in, void* d_out, int out_size, void* d_ws, size_t ws_size, hipStream_t stream) {
  fused_decode<<<256, 576, 0, stream>>>((const u16*)d_in[7], (const u16*)d_in[6], (char*)d_ws, (const u16*)d_in[5], (const u16*)d_in[0], (const u16*)d_in[3],
                                        (const u16*)d_in[8], (const u16*)d_in[1], (const u16*)d_in[2], (const u16*)d_in[4], (const u16*)d_in[9],
                                        (const u16*)d_in[10], (u16*)d_out);
}
```
